```python
import torch

def _softmax(x, axis=-1):
    return torch.softmax(x, dim=axis)
def _gelu(x):
    return torch.nn.functional.gelu(x, approximate="tanh")
def _silu(x):
    return torch.nn.functional.silu(x)
def _sigmoid(x):
    return torch.sigmoid(x)
def _layernorm(x, axis=-1, eps=1e-5):
    m = x.mean(dim=axis, keepdim=True)
    v = x.var(dim=axis, keepdim=True, unbiased=False)
    return (x - m) / torch.sqrt(v + eps)
def _scatter_add(tgt, idx, src, axis=0):
    return tgt.clone().index_add_(axis, idx, src)
def _segment_sum(src, seg, n):
    out = torch.zeros((n,) + src.shape[1:], dtype=src.dtype)
    return out.index_add_(0, seg, src)
def _pad(x, pw):
    flat = tuple(v for pair in reversed(pw) for v in pair)
    return torch.nn.functional.pad(x, flat)

kernel_name = 'standalone__modern_conv_block_replknet__vision'

def setup_inputs():

    _w = (lambda a: (lambda t: t.to(torch.bfloat16) if t.is_floating_point() else t)(torch.as_tensor(a)))
    H = W = 512
    C = 96
    K = 1
    return {
        "x": _w((lambda *s: torch.randn(*s, dtype=torch.bfloat16) * 0.05)(1, H, W, C)),
        "dw_large": _w((lambda *s: torch.randn(*s, dtype=torch.bfloat16) * 0.4)(K, K, C) * (1.0 / K)),
        "dw_small": _w((lambda *s: torch.randn(*s, dtype=torch.bfloat16) * 0.4)(5, 5, C) * 0.2),
        "w1": _w((lambda *s: torch.randn(*s, dtype=torch.bfloat16) * 0.4)(C, 4 * C) * (C ** -0.5)),
        "w2": _w((lambda *s: torch.randn(*s, dtype=torch.bfloat16) * 0.1)(4 * C, C) * ((4 * C) ** -0.5)),
    }

def reference(x, dw_large, dw_small, w1, w2):
    B, H, W, C = x.shape
    Kl = dw_large.shape[0]
    pl = (Kl - 1) // 2
    xpl = _pad(x, ((0,0),(pl,pl),(pl,pl),(0,0)))
    out = torch.zeros_like(x)
    for i in range(Kl):
        for j in range(Kl):
            out = out + xpl[:, i:i+H, j:j+W, :] * dw_large[i, j][None, None, None, :]
    Ks = dw_small.shape[0]
    ps = (Ks - 1) // 2
    xps = _pad(x, ((0,0),(ps,ps),(ps,ps),(0,0)))
    for i in range(Ks):
        for j in range(Ks):
            out = out + xps[:, i:i+H, j:j+W, :] * dw_small[i, j][None, None, None, :]
    h = _layernorm(out, axis=-1)
    h = _gelu(torch.einsum('bhwc,ce->bhwe', h, w1))
    h = torch.einsum('bhwe,ec->bhwc', h, w2)
    return x + h
```

```cpp
#include <hip/hip_runtime.h>
#include <hip/hip_bf16.h>
#include <stdint.h>

constexpr int H = 512, W = 512, C = 96, E = 384;
__device__ __forceinline__ float bf2f(unsigned short v) { return __uint_as_float(((unsigned)v) << 16); }
__device__ __forceinline__ unsigned short f2bf(float f) { unsigned u = __float_as_uint(f); u += 0x7fffu + ((u >> 16) & 1u); return (unsigned short)(u >> 16); }

__global__ __launch_bounds__(256) void naive_kernel(const unsigned short* __restrict__ x, const unsigned short* __restrict__ dwl,
    const unsigned short* __restrict__ dws, const unsigned short* __restrict__ w1, const unsigned short* __restrict__ w2, unsigned short* __restrict__ out) {
  __shared__ float y[32][C + 1];
  __shared__ float g[32][E + 1];
  const int tid = threadIdx.x;
  const int p0 = blockIdx.x * 32;
  const int h = p0 / W, w0 = p0 % W;
  for (int i = tid; i < 32 * C; i += 256) {
    int p = i / C, c = i % C; int w = w0 + p;
    float acc = bf2f(x[(h * W + w) * C + c]) * bf2f(dwl[c]);
    for (int a = 0; a < 5; ++a) for (int b = 0; b < 5; ++b) {
      int hh = h + a - 2, ww = w + b - 2;
      if (hh >= 0 && hh < H && ww >= 0 && ww < W) acc += bf2f(x[(hh * W + ww) * C + c]) * bf2f(dws[(a * 5 + b) * C + c]);
    }
    y[p][c] = acc;
  }
  __syncthreads();
  if (tid < 32) {
    float m = 0; for (int c = 0; c < C; ++c) m += y[tid][c]; m /= C;
    float v = 0; for (int c = 0; c < C; ++c) { float d = y[tid][c] - m; v += d * d; } v /= C;
    float r = rsqrtf(v + 1e-5f);
    for (int c = 0; c < C; ++c) y[tid][c] = bf2f(f2bf((y[tid][c] - m) * r));
  }
  __syncthreads();
  for (int i = tid; i < 32 * E; i += 256) {
    int p = i / E, e = i % E; float acc = 0;
    for (int c = 0; c < C; ++c) acc += y[p][c] * bf2f(w1[c * E + e]);
    float u = 0.7978845608028654f * (acc + 0.044715f * acc * acc * acc);
    float t = tanhf(u);
    g[p][e] = bf2f(f2bf(0.5f * acc * (1.f + t)));
  }
  __syncthreads();
  for (int i = tid; i < 32 * C; i += 256) {
    int p = i / C, c = i % C; float acc = 0;
    for (int e = 0; e < E; ++e) acc += g[p][e] * bf2f(w2[e * C + c]);
    int idx = (p0 + p) * C + c;
    out[idx] = f2bf(bf2f(x[idx]) + acc);
  }
}

extern "C" void kernel_launch(void* const* d_in, const int* in_sizes, int n_in, void* d_out, int out_size, void* d_ws, size_t ws_size, hipStream_t stream) {
  naive_kernel<<<H * W / 32, 256, 0, stream>>>((const unsigned short*)d_in[0], (const unsigned short*)d_in[1], (const unsigned short*)d_in[2],
      (const unsigned short*)d_in[3], (const unsigned short*)d_in[4], (unsigned short*)d_out);
}
```

```cpp
#include <hip/hip_runtime.h>
#include <hip/hip_bf16.h>
#include <stdint.h>

__device__ const unsigned short gelu_tab[8192] = {
0,13636,13734,13803,13847,13882,13916,13950,13968,13985,14002,14019,14036,14053,14070,14084,
14092,14101,14109,14118,14126,14135,14143,14152,14161,14169,14178,14186,14195,14203,14210,14214,
14218,14223,14227,14231,14235,14240,14244,14248,14253,14257,14261,14265,14270,14274,14278,14282,
14287,14291,14295,14299,14304,14308,14312,14317,14321,14325,14329,14334,14337,14339,14341,14343,
14345,14348,14350,14352,14354,14356,14358,14360,14363,14365,14367,14369,14371,14373,14375,14377,
14380,14382,14384,14386,14388,14390,14392,14395,14397,14399,14401,14403,14405,14407,14409,14412,
14414,14416,14418,14420,14422,14424,14427,14429,14431,14433,14435,14437,14439,14441,14444,14446,
14448,14450,14452,14454,14456,14459,14461,14463,14464,14466,14467,14468,14469,14470,14471,14472,
14473,14474,14475,14476,14477,14478,14479,14480,14482,14483,14484,14485,14486,14487,14488,14489,
14490,14491,14492,14493,14494,14495,14496,14498,14499,14500,14501,14502,14503,14504,14505,14506,
14507,14508,14509,14510,14511,14512,14514,14515,14516,14517,14518,14519,14520,14521,14522,14523,
14524,14525,14526,14527,14528,14530,14531,14532,14533,14534,14535,14536,14537,14538,14539,14540,
14541,14542,14543,14544,14546,14547,14548,14549,14550,14551,14552,14553,14554,14555,14556,14557,
14558,14559,14561,14562,14563,14564,14565,14566,14567,14568,14569,14570,14571,14572,14573,14574,
14575,14577,14578,14579,14580,14581,14582,14583,14584,14585,14586,14587,14588,14589,14590,14591,
14592,14593,14593,14594,14594,14595,14595,14596,14597,14597,14598,14598,14599,14599,14600,14600,
14601,14602,14603,14604,14605,14606,14607,14608,14610,14611,14612,14613,14614,14615,14616,14617,
14618,14619,14620,14621,14622,14623,14625,14626,14627,14628,14629,14630,14631,14632,14633,14634,
14635,14636,14637,14638,14639,14641,14642,14643,14644,14645,14646,14647,14648,14649,14650,14651,
14652,14653,14654,14655,14657,14658,14659,14660,14661,14662,14663,14664,14665,14666,14667,14668,
14669,14670,14671,14673,14674,14675,14676,14677,14678,14679,14680,14681,14682,14683,14684,14685,
14686,14687,14689,14690,14691,14692,14693,14694,14695,14696,14697,14698,14699,14700,14701,14702,
14703,14705,14706,14707,14708,14709,14710,14711,14712,14713,14714,14715,14716,14717,14718,14719,
14720,14721,14721,14722,14722,14723,14723,14724,14725,14725,14726,14726,14727,14727,14728,14728,
14729,14730,14731,14732,14733,14734,14735,14737,14738,14739,14740,14741,14742,14743,14744,14745,
14746,14747,14748,14749,14750,14751,14753,14754,14755,14756,14757,14758,14759,14760,14761,14762,
14763,14764,14765,14766,14767,14769,14770,14771,14772,14773,14774,14775,14776,14777,14778,14779,
14780,14781,14782,14784,14785,14786,14787,14788,14789,14790,14791,14792,14793,14794,14795,14796,
14797,14798,14800,14801,14802,14803,14804,14805,14806,14807,14808,14809,14810,14811,14812,14813,
14814,14816,14817,14818,14819,14820,14821,14822,14823,14824,14825,14826,14827,14828,14829,14831,
14832,14833,14834,14835,14836,14837,14838,14839,14840,14841,14842,14843,14844,14845,14847,14848,
14848,14849,14849,14850,14850,14851,14852,14852,14853,14853,14854,14854,14855,14855,14856,14856,
14857,14858,14859,14860,14861,14862,14864,14865,14866,14867,14868,14869,14870,14871,14872,14873,
14874,14875,14876,14877,14878,14880,14881,14882,14883,14884,14885,14886,14887,14888,14889,14890,
14891,14892,14893,14895,14896,14897,14898,14899,14900,14901,14902,14903,14904,14905,14906,14907,
14908,14909,14911,14912,14913,14914,14915,14916,14917,14918,14919,14920,14921,14922,14923,14924,
14926,14927,14928,14929,14930,14931,14932,14933,14934,14935,14936,14937,14938,14939,14940,14942,
14943,14944,14945,14946,14947,14948,14949,14950,14951,14952,14953,14954,14955,14957,14958,14959,
14960,14961,14962,14963,14964,14965,14966,14967,14968,14969,14970,14972,14973,14974,14975,14976,
14976,14977,14978,14978,14979,14979,14980,14980,14981,14981,14982,14982,14983,14983,14984,14984,
14985,14986,14987,14988,14990,14991,14992,14993,14994,14995,14996,14997,14998,14999,15000,15001,
15002,15003,15004,15006,15007,15008,15009,15010,15011,15012,15013,15014,15015,15016,15017,15018,
15019,15021,15022,15023,15024,15025,15026,15027,15028,15029,15030,15031,15032,15033,15034,15036,
15037,15038,15039,15040,15041,15042,15043,15044,15045,15046,15047,15048,15049,15051,15052,15053,
15054,15055,15056,15057,15058,15059,15060,15061,15062,15063,15065,15066,15067,15068,15069,15070,
15071,15072,15073,15074,15075,15076,15077,15078,15080,15081,15082,15083,15084,15085,15086,15087,
15088,15089,15090,15091,15092,15093,15095,15096,15097,15098,15099,15100,15101,15102,15103,15104,
15105,15105,15106,15106,15107,15107,15108,15108,15109,15109,15110,15111,15111,15112,15112,15113,
15113,15115,15116,15117,15118,15119,15120,15121,15122,15123,15124,15125,15126,15127,15128,15130,
15131,15132,15133,15134,15135,15136,15137,15138,15139,15140,15141,15142,15144,15145,15146,15147,
15148,15149,15150,15151,15152,15153,15154,15155,15156,15158,15159,15160,15161,15162,15163,15164,
15165,15166,15167,15168,15169,15170,15172,15173,15174,15175,15176,15177,15178,15179,15180,15181,
15182,15183,15184,15186,15187,15188,15189,15190,15191,15192,15193,15194,15195,15196,15197,15198,
15200,15201,15202,15203,15204,15205,15206,15207,15208,15209,15210,15211,15212,15214,15215,15216,
15217,15218,15219,15220,15221,15222,15223,15224,15225,15227,15228,15229,15230,15231,15232,15232,
15233,15234,15234,15235,15235,15236,15236,15237,15237,15238,15238,15239,15240,15240,15241,15241,
15242,15243,15244,15245,15246,15247,15248,15249,15251,15252,15253,15254,15255,15256,15257,15258,
15259,15260,15261,15262,15264,15265,15266,15267,15268,15269,15270,15271,15272,15273,15274,15275,
15277,15278,15279,15280,15281,15282,15283,15284,15285,15286,15287,15288,15290,15291,15292,15293,
15294,15295,15296,15297,15298,15299,15300,15302,15303,15304,15305,15306,15307,15308,15309,15310,
15311,15312,15313,15315,15316,15317,15318,15319,15320,15321,15322,15323,15324,15325,15327,15328,
15329,15330,15331,15332,15333,15334,15335,15336,15337,15339,15340,15341,15342,15343,15344,15345,
15346,15347,15348,15349,15351,15352,15353,15354,15355,15356,15357,15358,15359,15360,15361,15361,
15362,15362,15363,15363,15364,15365,15365,15366,15366,15367,15367,15368,15368,15369,15369,15370,
15371,15372,15373,15374,15375,15376,15377,15378,15380,15381,15382,15383,15384,15385,15386,15387,
15388,15389,15391,15392,15393,15394,15395,15396,15397,15398,15399,15400,15402,15403,15404,15405,
15406,15407,15408,15409,15410,15411,15413,15414,15415,15416,15417,15418,15419,15420,15421,15423,
15424,15425,15426,15427,15428,15429,15430,15431,15433,15434,15435,15436,15437,15438,15439,15440,
15441,15442,15444,15445,15446,15447,15448,15449,15450,15451,15452,15454,15455,15456,15457,15458,
15459,15460,15461,15462,15464,15465,15466,15467,15468,15469,15470,15471,15473,15474,15475,15476,
15477,15478,15479,15480,15481,15483,15484,15485,15486,15487,15488,15489,15489,15490,15490,15491,
15491,15492,15493,15493,15494,15494,15495,15495,15496,15496,15497,15498,15498,15499,15499,15500,
15501,15502,15503,15504,15505,15506,15507,15509,15510,15511,15512,15513,15514,15515,15516,15518,
15519,15520,15521,15522,15523,15524,15525,15527,15528,15529,15530,15531,15532,15533,15535,15536,
15537,15538,15539,15540,15541,15543,15544,15545,15546,15547,15548,15549,15551,15552,15553,15554,
15555,15556,15557,15559,15560,15561,15562,15563,15564,15565,15567,15568,15569,15570,15571,15572,
15573,15575,15576,15577,15578,15579,15580,15582,15583,15584,15585,15586,15587,15588,15590,15591,
15592,15593,15594,15595,15597,15598,15599,15600,15601,15602,15604,15605,15606,15607,15608,15609,
15611,15612,15613,15614,15615,15616,15617,15617,15618,15619,15619,15620,15620,15621,15621,15622,
15623,15623,15624,15624,15625,15626,15626,15627,15627,15628,15629,15629,15630,15630,15631,15631,
15632,15633,15635,15636,15637,15638,15639,15641,15642,15643,15644,15645,15647,15648,15649,15650,
15651,15653,15654,15655,15656,15657,15658,15660,15661,15662,15663,15664,15666,15667,15668,15669,
15671,15672,15673,15674,15675,15677,15678,15679,15680,15681,15683,15684,15685,15686,15688,15689,
15690,15691,15692,15694,15695,15696,15697,15699,15700,15701,15702,15703,15705,15706,15707,15708,
15710,15711,15712,15713,15715,15716,15717,15718,15720,15721,15722,15723,15725,15726,15727,15728,
15730,15731,15732,15733,15735,15736,15737,15738,15740,15741,15742,15743,15744,15745,15746,15746,
15747,15747,15748,15749,15749,15750,15751,15751,15752,15753,15753,15754,15754,15755,15756,15756,
15757,15758,15758,15759,15760,15760,15761,15761,15762,15763,15763,15764,15765,15765,15766,15767,
15768,15769,15770,15771,15773,15774,15775,15777,15778,15779,15781,15782,15783,15785,15786,15787,
15788,15790,15791,15792,15794,15795,15796,15798,15799,15800,15802,15803,15804,15806,15807,15808,
15810,15811,15813,15814,15815,15817,15818,15819,15821,15822,15823,15825,15826,15828,15829,15830,
15832,15833,15834,15836,15837,15839,15840,15841,15843,15844,15845,15847,15848,15850,15851,15852,
15854,15855,15857,15858,15860,15861,15862,15864,15865,15867,15868,15869,15871,15872,15873,15874,
15874,15875,15876,15876,15877,15878,15879,15879,15880,15881,15881,15882,15883,15884,15884,15885,
15886,15887,15887,15888,15889,15889,15890,15891,15892,15892,15893,15894,15895,15895,15896,15897,
15898,15898,15899,15900,15901,15901,15902,15903,15904,15904,15905,15906,15907,15907,15908,15909,
15910,15911,15913,15914,15916,15918,15919,15921,15922,15924,15925,15927,15928,15930,15931,15933,
15935,15936,15938,15939,15941,15942,15944,15946,15947,15949,15950,15952,15953,15955,15957,15958,
15960,15962,15963,15965,15966,15968,15970,15971,15973,15975,15976,15978,15980,15981,15983,15985,
15986,15988,15990,15991,15993,15995,15996,15998,16000,16001,16001,16002,16003,16004,16005,16006,
16007,16007,16008,16009,16010,16011,16012,16013,16014,16014,16015,16016,16017,16018,16019,16020,
16021,16021,16022,16023,16024,16025,16026,16027,16028,16028,16029,16030,16031,16032,16033,16034,
16035,16036,16037,16037,16038,16039,16040,16041,16042,16043,16044,16045,16046,16047,16048,16048,
16049,16050,16051,16052,16053,16054,16055,16056,16057,16058,16059,16060,16061,16062,16063,16063,
16065,16067,16069,16071,16072,16074,16076,16078,16080,16082,16084,16086,16088,16090,16092,16094,
16096,16098,16100,16102,16104,16106,16108,16110,16112,16114,16116,16118,16120,16122,16124,16126,
16128,16129,16130,16131,16132,16133,16134,16135,16136,16137,16138,16139,16140,16142,16143,16144,
16145,16146,16147,16148,16149,16150,16151,16152,16153,16154,16155,16156,16158,16159,16160,16161,
16162,16163,16164,16165,16166,16167,16168,16170,16171,16172,16173,16174,16175,16176,16177,16178,
16179,16181,16182,16183,16184,16185,16186,16187,16188,16190,16191,16192,16193,16194,16195,16196,
16198,16199,16200,16201,16202,16203,16204,16205,16207,16208,16209,16210,16211,16212,16214,16215,
16216,16217,16218,16219,16220,16222,16223,16224,16225,16226,16227,16229,16230,16231,16232,16233,
16235,16237,16240,16242,16244,16247,16249,16251,16254,16256,16257,16258,16260,16261,16262,16263,
16264,16266,16267,16268,16269,16270,16272,16273,16274,16275,16276,16278,16279,16280,16281,16282,
16284,16285,16286,16287,16288,16290,16291,16292,16293,16294,16296,16297,16298,16299,16300,16302,
16303,16304,16305,16306,16308,16309,16310,16311,16312,16314,16315,16316,16317,16318,16320,16321,
16322,16323,16324,16326,16327,16328,16329,16330,16332,16333,16334,16335,16336,16338,16339,16340,
16341,16342,16344,16345,16346,16347,16348,16349,16351,16352,16353,16354,16355,16357,16358,16359,
16360,16361,16362,16364,16365,16366,16367,16368,16369,16371,16372,16373,16374,16375,16376,16378,
16379,16380,16381,16382,16383,16384,16385,16385,16386,16387,16387,16388,16388,16389,16389,16390,
16391,16392,16393,16394,16395,16396,16398,16399,16400,16401,16402,16403,16404,16406,16407,16408,
16409,16410,16411,16412,16413,16414,16416,16417,16418,16419,16420,16421,16422,16423,16424,16425,
16427,16428,16429,16430,16431,16432,16433,16434,16435,16436,16437,16439,16440,16441,16442,16443,
16444,16445,16446,16447,16448,16449,16450,16451,16453,16454,16455,16456,16457,16458,16459,16460,
16461,16462,16463,16464,16465,16467,16468,16469,16470,16471,16472,16473,16474,16475,16476,16477,
16478,16479,16480,16481,16483,16484,16485,16486,16487,16488,16489,16490,16491,16492,16493,16494,
16495,16496,16498,16499,16500,16501,16502,16503,16504,16505,16506,16507,16508,16509,16510,16511,
16512,16513,16513,16514,16514,16515,16515,16516,16516,16517,16518,16518,16519,16519,16520,16520,
16521,16522,16523,16524,16525,16526,16527,16528,16530,16531,16532,16533,16534,16535,16536,16537,
16538,16539,16540,16541,16542,16543,16544,16546,16547,16548,16549,16550,16551,16552,16553,16554,
16555,16556,16557,16558,16559,16560,16562,16563,16564,16565,16566,16567,16568,16569,16570,16571,
16572,16573,16574,16575,16576,16578,16579,16580,16581,16582,16583,16584,16585,16586,16587,16588,
16589,16590,16591,16592,16594,16595,16596,16597,16598,16599,16600,16601,16602,16603,16604,16605,
16606,16607,16608,16610,16611,16612,16613,16614,16615,16616,16617,16618,16619,16620,16621,16622,
16623,16624,16626,16627,16628,16629,16630,16631,16632,16633,16634,16635,16636,16637,16638,16639,
16640,16641,16641,16642,16642,16643,16643,16644,16644,16645,16646,16646,16647,16647,16648,16648,
16649,16650,16651,16652,16653,16654,16655,16656,16658,16659,16660,16661,16662,16663,16664,16665,
16666,16667,16668,16669,16670,16671,16672,16674,16675,16676,16677,16678,16679,16680,16681,16682,
16683,16684,16685,16686,16687,16688,16690,16691,16692,16693,16694,16695,16696,16697,16698,16699,
16700,16701,16702,16703,16704,16706,16707,16708,16709,16710,16711,16712,16713,16714,16715,16716,
16717,16718,16719,16720,16722,16723,16724,16725,16726,16727,16728,16729,16730,16731,16732,16733,
16734,16735,16736,16738,16739,16740,16741,16742,16743,16744,16745,16746,16747,16748,16749,16750,
16751,16752,16754,16755,16756,16757,16758,16759,16760,16761,16762,16763,16764,16765,16766,16767,
16768,16769,16769,16770,16770,16771,16771,16772,16772,16773,16774,16774,16775,16775,16776,16776,
16777,16778,16779,16780,16781,16782,16783,16784,16786,16787,16788,16789,16790,16791,16792,16793,
16794,16795,16796,16797,16798,16799,16800,16802,16803,16804,16805,16806,16807,16808,16809,16810,
16811,16812,16813,16814,16815,16816,16818,16819,16820,16821,16822,16823,16824,16825,16826,16827,
16828,16829,16830,16831,16832,16834,16835,16836,16837,16838,16839,16840,16841,16842,16843,16844,
16845,16846,16847,16848,16850,16851,16852,16853,16854,16855,16856,16857,16858,16859,16860,16861,
16862,16863,16864,16866,16867,16868,16869,16870,16871,16872,16873,16874,16875,16876,16877,16878,
16879,16880,16882,16883,16884,16885,16886,16887,16888,16889,16890,16891,16892,16893,16894,16895,
16896,16897,16897,16898,16898,16899,16899,16900,16900,16901,16902,16902,16903,16903,16904,16904,
16905,16906,16907,16908,16909,16910,16911,16912,16914,16915,16916,16917,16918,16919,16920,16921,
16922,16923,16924,16925,16926,16927,16928,16930,16931,16932,16933,16934,16935,16936,16937,16938,
16939,16940,16941,16942,16943,16944,16946,16947,16948,16949,16950,16951,16952,16953,16954,16955,
16956,16957,16958,16959,16960,16962,16963,16964,16965,16966,16967,16968,16969,16970,16971,16972,
16973,16974,16975,16976,16978,16979,16980,16981,16982,16983,16984,16985,16986,16987,16988,16989,
16990,16991,16992,16994,16995,16996,16997,16998,16999,17000,17001,17002,17003,17004,17005,17006,
17007,17008,17010,17011,17012,17013,17014,17015,17016,17017,17018,17019,17020,17021,17022,17023,
17024,17025,17025,17026,17026,17027,17027,17028,17028,17029,17030,17030,17031,17031,17032,17032,
17033,17034,17035,17036,17037,17038,17039,17040,17042,17043,17044,17045,17046,17047,17048,17049,
17050,17051,17052,17053,17054,17055,17056,17058,17059,17060,17061,17062,17063,17064,17065,17066,
17067,17068,17069,17070,17071,17072,17074,17075,17076,17077,17078,17079,17080,17081,17082,17083,
17084,17085,17086,17087,17088,17090,17091,17092,17093,17094,17095,17096,17097,17098,17099,17100,
17101,17102,17103,17104,17106,17107,17108,17109,17110,17111,17112,17113,17114,17115,17116,17117,
17118,17119,17120,17122,17123,17124,17125,17126,17127,17128,17129,17130,17131,17132,17133,17134,
17135,17136,17138,17139,17140,17141,17142,17143,17144,17145,17146,17147,17148,17149,17150,17151,
17152,17153,17153,17154,17154,17155,17155,17156,17156,17157,17158,17158,17159,17159,17160,17160,
17161,17162,17163,17164,17165,17166,17167,17168,17170,17171,17172,17173,17174,17175,17176,17177,
17178,17179,17180,17181,17182,17183,17184,17186,17187,17188,17189,17190,17191,17192,17193,17194,
17195,17196,17197,17198,17199,17200,17202,17203,17204,17205,17206,17207,17208,17209,17210,17211,
17212,17213,17214,17215,17216,17218,17219,17220,17221,17222,17223,17224,17225,17226,17227,17228,
17229,17230,17231,17232,17234,17235,17236,17237,17238,17239,17240,17241,17242,17243,17244,17245,
17246,17247,17248,17250,17251,17252,17253,17254,17255,17256,17257,17258,17259,17260,17261,17262,
17263,17264,17266,17267,17268,17269,17270,17271,17272,17273,17274,17275,17276,17277,17278,17279,
17280,17281,17281,17282,17282,17283,17283,17284,17284,17285,17286,17286,17287,17287,17288,17288,
17289,17290,17291,17292,17293,17294,17295,17296,17298,17299,17300,17301,17302,17303,17304,17305,
17306,17307,17308,17309,17310,17311,17312,17314,17315,17316,17317,17318,17319,17320,17321,17322,
17323,17324,17325,17326,17327,17328,17330,17331,17332,17333,17334,17335,17336,17337,17338,17339,
17340,17341,17342,17343,17344,17346,17347,17348,17349,17350,17351,17352,17353,17354,17355,17356,
17357,17358,17359,17360,17362,17363,17364,17365,17366,17367,17368,17369,17370,17371,17372,17373,
17374,17375,17376,17378,17379,17380,17381,17382,17383,17384,17385,17386,17387,17388,17389,17390,
17391,17392,17394,17395,17396,17397,17398,17399,17400,17401,17402,17403,17404,17405,17406,17407,
17408,17409,17409,17410,17410,17411,17411,17412,17412,17413,17414,17414,17415,17415,17416,17416,
17417,17418,17419,17420,17421,17422,17423,17424,17426,17427,17428,17429,17430,17431,17432,17433,
17434,17435,17436,17437,17438,17439,17440,17442,17443,17444,17445,17446,17447,17448,17449,17450,
17451,17452,17453,17454,17455,17456,17458,17459,17460,17461,17462,17463,17464,17465,17466,17467,
17468,17469,17470,17471,17472,17474,17475,17476,17477,17478,17479,17480,17481,17482,17483,17484,
17485,17486,17487,17488,17490,17491,17492,17493,17494,17495,17496,17497,17498,17499,17500,17501,
17502,17503,17504,17506,17507,17508,17509,17510,17511,17512,17513,17514,17515,17516,17517,17518,
17519,17520,17522,17523,17524,17525,17526,17527,17528,17529,17530,17531,17532,17533,17534,17535,
17536,17537,17537,17538,17538,17539,17539,17540,17540,17541,17542,17542,17543,17543,17544,17544,
17545,17546,17547,17548,17549,17550,17551,17552,17554,17555,17556,17557,17558,17559,17560,17561,
17562,17563,17564,17565,17566,17567,17568,17570,17571,17572,17573,17574,17575,17576,17577,17578,
17579,17580,17581,17582,17583,17584,17586,17587,17588,17589,17590,17591,17592,17593,17594,17595,
17596,17597,17598,17599,17600,17602,17603,17604,17605,17606,17607,17608,17609,17610,17611,17612,
17613,17614,17615,17616,17618,17619,17620,17621,17622,17623,17624,17625,17626,17627,17628,17629,
17630,17631,17632,17634,17635,17636,17637,17638,17639,17640,17641,17642,17643,17644,17645,17646,
17647,17648,17650,17651,17652,17653,17654,17655,17656,17657,17658,17659,17660,17661,17662,17663,
17664,17665,17665,17666,17666,17667,17667,17668,17668,17669,17670,17670,17671,17671,17672,17672,
17673,17674,17675,17676,17677,17678,17679,17680,17682,17683,17684,17685,17686,17687,17688,17689,
17690,17691,17692,17693,17694,17695,17696,17698,17699,17700,17701,17702,17703,17704,17705,17706,
17707,17708,17709,17710,17711,17712,17714,17715,17716,17717,17718,17719,17720,17721,17722,17723,
17724,17725,17726,17727,17728,17730,17731,17732,17733,17734,17735,17736,17737,17738,17739,17740,
17741,17742,17743,17744,17746,17747,17748,17749,17750,17751,17752,17753,17754,17755,17756,17757,
17758,17759,17760,17762,17763,17764,17765,17766,17767,17768,17769,17770,17771,17772,17773,17774,
17775,17776,17778,17779,17780,17781,17782,17783,17784,17785,17786,17787,17788,17789,17790,17791,
17792,17793,17793,17794,17794,17795,17795,17796,17796,17797,17798,17798,17799,17799,17800,17800,
17801,17802,17803,17804,17805,17806,17807,17808,17810,17811,17812,17813,17814,17815,17816,17817,
17818,17819,17820,17821,17822,17823,17824,17826,17827,17828,17829,17830,17831,17832,17833,17834,
17835,17836,17837,17838,17839,17840,17842,17843,17844,17845,17846,17847,17848,17849,17850,17851,
17852,17853,17854,17855,17856,17858,17859,17860,17861,17862,17863,17864,17865,17866,17867,17868,
17869,17870,17871,17872,17874,17875,17876,17877,17878,17879,17880,17881,17882,17883,17884,17885,
17886,17887,17888,17890,17891,17892,17893,17894,17895,17896,17897,17898,17899,17900,17901,17902,
17903,17904,17906,17907,17908,17909,17910,17911,17912,17913,17914,17915,17916,17917,17918,17919,
17920,17921,17921,17922,17922,17923,17923,17924,17924,17925,17926,17926,17927,17927,17928,17928,
17929,17930,17931,17932,17933,17934,17935,17936,17938,17939,17940,17941,17942,17943,17944,17945,
17946,17947,17948,17949,17950,17951,17952,17954,17955,17956,17957,17958,17959,17960,17961,17962,
17963,17964,17965,17966,17967,17968,17970,17971,17972,17973,17974,17975,17976,17977,17978,17979,
17980,17981,17982,17983,17984,17986,17987,17988,17989,17990,17991,17992,17993,17994,17995,17996,
17997,17998,17999,18000,18002,18003,18004,18005,18006,18007,18008,18009,18010,18011,18012,18013,
18014,18015,18016,18018,18019,18020,18021,18022,18023,18024,18025,18026,18027,18028,18029,18030,
18031,18032,18034,18035,18036,18037,18038,18039,18040,18041,18042,18043,18044,18045,18046,18047,
18048,18049,18049,18050,18050,18051,18051,18052,18052,18053,18054,18054,18055,18055,18056,18056,
18057,18058,18059,18060,18061,18062,18063,18064,18066,18067,18068,18069,18070,18071,18072,18073,
18074,18075,18076,18077,18078,18079,18080,18082,18083,18084,18085,18086,18087,18088,18089,18090,
18091,18092,18093,18094,18095,18096,18098,18099,18100,18101,18102,18103,18104,18105,18106,18107,
18108,18109,18110,18111,18112,18114,18115,18116,18117,18118,18119,18120,18121,18122,18123,18124,
18125,18126,18127,18128,18130,18131,18132,18133,18134,18135,18136,18137,18138,18139,18140,18141,
18142,18143,18144,18146,18147,18148,18149,18150,18151,18152,18153,18154,18155,18156,18157,18158,
18159,18160,18162,18163,18164,18165,18166,18167,18168,18169,18170,18171,18172,18173,18174,18175,
18176,18177,18177,18178,18178,18179,18179,18180,18180,18181,18182,18182,18183,18183,18184,18184,
18185,18186,18187,18188,18189,18190,18191,18192,18194,18195,18196,18197,18198,18199,18200,18201,
18202,18203,18204,18205,18206,18207,18208,18210,18211,18212,18213,18214,18215,18216,18217,18218,
18219,18220,18221,18222,18223,18224,18226,18227,18228,18229,18230,18231,18232,18233,18234,18235,
18236,18237,18238,18239,18240,18242,18243,18244,18245,18246,18247,18248,18249,18250,18251,18252,
18253,18254,18255,18256,18258,18259,18260,18261,18262,18263,18264,18265,18266,18267,18268,18269,
18270,18271,18272,18274,18275,18276,18277,18278,18279,18280,18281,18282,18283,18284,18285,18286,
18287,18288,18290,18291,18292,18293,18294,18295,18296,18297,18298,18299,18300,18301,18302,18303,
18304,18305,18305,18306,18306,18307,18307,18308,18308,18309,18310,18310,18311,18311,18312,18312,
18313,18314,18315,18316,18317,18318,18319,18320,18322,18323,18324,18325,18326,18327,18328,18329,
18330,18331,18332,18333,18334,18335,18336,18338,18339,18340,18341,18342,18343,18344,18345,18346,
18347,18348,18349,18350,18351,18352,18354,18355,18356,18357,18358,18359,18360,18361,18362,18363,
18364,18365,18366,18367,18368,18370,18371,18372,18373,18374,18375,18376,18377,18378,18379,18380,
18381,18382,18383,18384,18386,18387,18388,18389,18390,18391,18392,18393,18394,18395,18396,18397,
18398,18399,18400,18402,18403,18404,18405,18406,18407,18408,18409,18410,18411,18412,18413,18414,
18415,18416,18418,18419,18420,18421,18422,18423,18424,18425,18426,18427,18428,18429,18430,18431,
18432,18433,18433,18434,18434,18435,18435,18436,18436,18437,18438,18438,18439,18439,18440,18440,
32704,32704,32704,32704,32704,32704,32704,32704,32704,32704,32704,32704,32704,32704,32704,32704,
32704,32704,32704,32704,32704,32704,32704,32704,32704,32704,32704,32704,32704,32704,32704,32704,
32704,32704,32704,32704,32704,32704,32704,32704,32704,32704,32704,32704,32704,32704,32704,32704,
32704,32704,32704,32704,32704,32704,32704,32704,32704,32704,32704,32704,32704,32704,32704,32704,
32704,32704,32704,32704,32704,32704,32704,32704,32704,32704,32704,32704,32704,32704,32704,32704,
32704,32704,32704,32704,32704,32704,32704,32704,32704,32704,32704,32704,32704,32704,32704,32704,
32704,32704,32704,32704,32704,32704,32704,32704,32704,32704,32704,32704,32704,32704,32704,32704,
32704,32704,32704,32704,32704,32704,32704,32704,32704,32704,32704,32704,32704,32704,32704,32704,
32768,46404,46502,46571,46615,46650,46684,46718,46736,46753,46770,46787,46804,46821,46838,46852,
46860,46869,46877,46886,46894,46903,46911,46920,46929,46937,46946,46954,46963,46971,46978,46982,
46986,46991,46995,46999,47003,47008,47012,47016,47021,47025,47029,47033,47038,47042,47046,47050,
47055,47059,47063,47067,47072,47076,47080,47085,47089,47093,47097,47102,47105,47107,47109,47111,
47113,47116,47118,47120,47122,47124,47126,47128,47131,47133,47135,47137,47139,47141,47143,47145,
47148,47150,47152,47154,47156,47158,47160,47163,47165,47167,47169,47171,47173,47175,47177,47180,
47182,47184,47186,47188,47190,47192,47195,47197,47199,47201,47203,47205,47207,47209,47212,47214,
47216,47218,47220,47222,47224,47227,47229,47231,47232,47234,47235,47236,47237,47238,47239,47240,
47241,47242,47243,47244,47245,47246,47247,47248,47250,47251,47252,47253,47254,47255,47256,47257,
47258,47259,47260,47261,47262,47263,47264,47266,47267,47268,47269,47270,47271,47272,47273,47274,
47275,47276,47277,47278,47279,47280,47282,47283,47284,47285,47286,47287,47288,47289,47290,47291,
47292,47293,47294,47295,47296,47298,47299,47300,47301,47302,47303,47304,47305,47306,47307,47308,
47309,47310,47311,47312,47313,47315,47316,47317,47318,47319,47320,47321,47322,47323,47324,47325,
47326,47327,47328,47329,47331,47332,47333,47334,47335,47336,47337,47338,47339,47340,47341,47342,
47343,47344,47345,47347,47348,47349,47350,47351,47352,47353,47354,47355,47356,47357,47358,47359,
47360,47361,47361,47362,47362,47363,47363,47364,47364,47365,47366,47366,47367,47367,47368,47368,
47369,47370,47371,47372,47373,47374,47375,47376,47378,47379,47380,47381,47382,47383,47384,47385,
47386,47387,47388,47389,47390,47391,47392,47393,47395,47396,47397,47398,47399,47400,47401,47402,
47403,47404,47405,47406,47407,47408,47409,47411,47412,47413,47414,47415,47416,47417,47418,47419,
47420,47421,47422,47423,47424,47425,47427,47428,47429,47430,47431,47432,47433,47434,47435,47436,
47437,47438,47439,47440,47441,47443,47444,47445,47446,47447,47448,47449,47450,47451,47452,47453,
47454,47455,47456,47457,47459,47460,47461,47462,47463,47464,47465,47466,47467,47468,47469,47470,
47471,47472,47473,47475,47476,47477,47478,47479,47480,47481,47482,47483,47484,47485,47486,47487,
47488,47489,47489,47490,47490,47491,47491,47492,47492,47493,47494,47494,47495,47495,47496,47496,
47497,47498,47499,47500,47501,47502,47503,47504,47505,47507,47508,47509,47510,47511,47512,47513,
47514,47515,47516,47517,47518,47519,47520,47521,47523,47524,47525,47526,47527,47528,47529,47530,
47531,47532,47533,47534,47535,47536,47537,47539,47540,47541,47542,47543,47544,47545,47546,47547,
47548,47549,47550,47551,47552,47553,47554,47556,47557,47558,47559,47560,47561,47562,47563,47564,
47565,47566,47567,47568,47569,47570,47572,47573,47574,47575,47576,47577,47578,47579,47580,47581,
47582,47583,47584,47585,47586,47588,47589,47590,47591,47592,47593,47594,47595,47596,47597,47598,
47599,47600,47601,47602,47603,47605,47606,47607,47608,47609,47610,47611,47612,47613,47614,47615,
47616,47617,47617,47618,47618,47619,47619,47620,47620,47621,47621,47622,47623,47623,47624,47624,
47625,47626,47627,47628,47629,47630,47631,47632,47633,47634,47636,47637,47638,47639,47640,47641,
47642,47643,47644,47645,47646,47647,47648,47649,47650,47652,47653,47654,47655,47656,47657,47658,
47659,47660,47661,47662,47663,47664,47665,47666,47667,47669,47670,47671,47672,47673,47674,47675,
47676,47677,47678,47679,47680,47681,47682,47683,47684,47686,47687,47688,47689,47690,47691,47692,
47693,47694,47695,47696,47697,47698,47699,47700,47702,47703,47704,47705,47706,47707,47708,47709,
47710,47711,47712,47713,47714,47715,47716,47717,47719,47720,47721,47722,47723,47724,47725,47726,
47727,47728,47729,47730,47731,47732,47733,47734,47736,47737,47738,47739,47740,47741,47742,47743,
47744,47745,47745,47746,47746,47747,47747,47748,47748,47749,47749,47750,47750,47751,47751,47752,
47753,47754,47755,47756,47757,47758,47759,47760,47761,47762,47763,47764,47766,47767,47768,47769,
47770,47771,47772,47773,47774,47775,47776,47777,47778,47779,47780,47781,47783,47784,47785,47786,
47787,47788,47789,47790,47791,47792,47793,47794,47795,47796,47797,47798,47800,47801,47802,47803,
47804,47805,47806,47807,47808,47809,47810,47811,47812,47813,47814,47815,47817,47818,47819,47820,
47821,47822,47823,47824,47825,47826,47827,47828,47829,47830,47831,47832,47833,47835,47836,47837,
47838,47839,47840,47841,47842,47843,47844,47845,47846,47847,47848,47849,47850,47852,47853,47854,
47855,47856,47857,47858,47859,47860,47861,47862,47863,47864,47865,47866,47867,47868,47870,47871,
47872,47872,47873,47873,47874,47874,47875,47876,47876,47877,47877,47878,47878,47879,47879,47880,
47881,47882,47883,47884,47885,47886,47887,47888,47889,47890,47891,47892,47893,47894,47895,47896,
47897,47899,47900,47901,47902,47903,47904,47905,47906,47907,47908,47909,47910,47911,47912,47913,
47914,47915,47917,47918,47919,47920,47921,47922,47923,47924,47925,47926,47927,47928,47929,47930,
47931,47932,47933,47935,47936,47937,47938,47939,47940,47941,47942,47943,47944,47945,47946,47947,
47948,47949,47950,47951,47952,47954,47955,47956,47957,47958,47959,47960,47961,47962,47963,47964,
47965,47966,47967,47968,47969,47970,47971,47973,47974,47975,47976,47977,47978,47979,47980,47981,
47982,47983,47984,47985,47986,47987,47988,47989,47990,47991,47993,47994,47995,47996,47997,47998,
47999,48000,48000,48001,48002,48002,48003,48003,48004,48004,48005,48005,48006,48006,48007,48007,
48008,48009,48010,48011,48012,48013,48014,48015,48017,48018,48019,48020,48021,48022,48023,48024,
48025,48026,48027,48028,48029,48030,48031,48032,48033,48034,48035,48036,48038,48039,48040,48041,
48042,48043,48044,48045,48046,48047,48048,48049,48050,48051,48052,48053,48054,48055,48056,48057,
48058,48060,48061,48062,48063,48064,48065,48066,48067,48068,48069,48070,48071,48072,48073,48074,
48075,48076,48077,48078,48079,48080,48081,48083,48084,48085,48086,48087,48088,48089,48090,48091,
48092,48093,48094,48095,48096,48097,48098,48099,48100,48101,48102,48103,48104,48105,48107,48108,
48109,48110,48111,48112,48113,48114,48115,48116,48117,48118,48119,48120,48121,48122,48123,48124,
48125,48126,48127,48128,48129,48129,48130,48130,48131,48131,48132,48132,48133,48133,48134,48134,
48135,48136,48137,48138,48139,48140,48141,48142,48143,48145,48146,48147,48148,48149,48150,48151,
48152,48153,48154,48155,48156,48157,48158,48159,48160,48161,48162,48163,48164,48165,48166,48167,
48168,48169,48170,48171,48172,48173,48174,48175,48177,48178,48179,48180,48181,48182,48183,48184,
48185,48186,48187,48188,48189,48190,48191,48192,48193,48194,48195,48196,48197,48198,48199,48200,
48201,48202,48203,48204,48205,48206,48207,48208,48209,48210,48211,48212,48213,48214,48215,48216,
48218,48219,48220,48221,48222,48223,48224,48225,48226,48227,48228,48229,48230,48231,48232,48233,
48234,48235,48236,48237,48238,48239,48240,48241,48242,48243,48244,48245,48246,48247,48248,48249,
48250,48251,48252,48253,48254,48255,48256,48257,48257,48258,48258,48259,48259,48260,48260,48261,
48261,48262,48263,48264,48265,48266,48267,48268,48269,48270,48271,48272,48273,48274,48275,48276,
48277,48278,48279,48280,48281,48282,48283,48284,48285,48286,48287,48288,48289,48290,48291,48292,
48293,48294,48295,48296,48297,48298,48299,48300,48301,48302,48303,48304,48305,48306,48307,48308,
48309,48310,48311,48312,48313,48314,48315,48316,48317,48318,48319,48320,48321,48322,48323,48324,
48325,48326,48327,48328,48329,48330,48331,48332,48333,48334,48335,48336,48337,48338,48339,48340,
48341,48342,48343,48344,48345,48346,48347,48348,48348,48349,48350,48351,48352,48353,48354,48355,
48356,48357,48358,48359,48360,48361,48362,48363,48364,48365,48366,48367,48368,48369,48370,48371,
48372,48373,48374,48375,48375,48376,48377,48378,48379,48380,48381,48382,48383,48384,48385,48385,
48386,48387,48388,48389,48389,48390,48391,48392,48393,48394,48395,48396,48397,48398,48399,48400,
48401,48402,48403,48404,48405,48406,48406,48407,48408,48409,48410,48411,48412,48413,48414,48415,
48416,48417,48418,48419,48419,48420,48421,48422,48423,48424,48425,48426,48427,48428,48429,48430,
48430,48431,48432,48433,48434,48435,48436,48437,48438,48439,48439,48440,48441,48442,48443,48444,
48445,48446,48447,48448,48448,48449,48450,48451,48452,48453,48454,48455,48456,48456,48457,48458,
48459,48460,48461,48462,48463,48464,48464,48465,48466,48467,48468,48469,48470,48471,48471,48472,
48473,48474,48475,48476,48477,48477,48478,48479,48480,48481,48482,48483,48483,48484,48485,48486,
48487,48488,48489,48489,48490,48491,48492,48493,48494,48495,48495,48496,48497,48498,48499,48500,
48501,48503,48504,48506,48508,48509,48511,48512,48513,48514,48515,48516,48516,48517,48518,48519,
48520,48520,48521,48522,48523,48524,48524,48525,48526,48527,48528,48528,48529,48530,48531,48532,
48532,48533,48534,48535,48536,48536,48537,48538,48539,48539,48540,48541,48542,48542,48543,48544,
48545,48546,48546,48547,48548,48549,48549,48550,48551,48552,48552,48553,48554,48554,48555,48556,
48557,48557,48558,48559,48560,48560,48561,48562,48562,48563,48564,48565,48565,48566,48567,48567,
48568,48569,48570,48570,48571,48572,48572,48573,48574,48574,48575,48576,48576,48577,48578,48578,
48579,48580,48580,48581,48582,48583,48583,48584,48585,48585,48586,48586,48587,48588,48588,48589,
48590,48590,48591,48592,48592,48593,48594,48594,48595,48596,48596,48597,48597,48598,48599,48599,
48600,48601,48603,48604,48605,48606,48608,48609,48610,48611,48612,48613,48615,48616,48617,48618,
48619,48620,48622,48623,48624,48625,48626,48627,48628,48629,48630,48631,48632,48634,48635,48636,
48637,48638,48639,48640,48640,48641,48641,48642,48642,48643,48643,48644,48644,48645,48645,48646,
48646,48647,48647,48648,48648,48649,48649,48649,48650,48650,48651,48651,48652,48652,48652,48653,
48653,48654,48654,48655,48655,48655,48656,48656,48657,48657,48657,48658,48658,48659,48659,48659,
48660,48660,48660,48661,48661,48661,48662,48662,48663,48663,48663,48664,48664,48664,48665,48665,
48665,48666,48666,48666,48667,48667,48667,48667,48668,48668,48668,48669,48669,48669,48670,48670,
48670,48670,48671,48671,48671,48671,48672,48672,48672,48673,48673,48673,48673,48674,48674,48674,
48674,48675,48675,48676,48676,48677,48677,48677,48678,48678,48679,48679,48679,48680,48680,48680,
48681,48681,48681,48682,48682,48682,48682,48683,48683,48683,48683,48684,48684,48684,48684,48684,
48684,48685,48685,48685,48685,48685,48685,48685,48686,48686,48686,48686,48686,48686,48686,48686,
48686,48686,48686,48686,48686,48686,48686,48686,48686,48686,48686,48686,48686,48686,48686,48686,
48686,48686,48685,48685,48685,48685,48685,48685,48685,48685,48684,48684,48684,48684,48684,48684,
48683,48683,48683,48683,48683,48682,48682,48682,48682,48681,48681,48681,48681,48680,48680,48680,
48680,48679,48679,48679,48678,48678,48678,48677,48677,48677,48677,48676,48676,48676,48675,48675,
48674,48674,48674,48673,48673,48673,48672,48672,48671,48671,48671,48670,48670,48669,48669,48669,
48668,48667,48666,48666,48665,48664,48663,48662,48661,48660,48659,48658,48657,48656,48655,48654,
48653,48652,48651,48650,48649,48648,48647,48646,48645,48644,48643,48642,48641,48639,48637,48635,
48633,48630,48628,48626,48624,48622,48619,48617,48615,48613,48611,48608,48606,48604,48602,48600,
48597,48595,48593,48591,48589,48587,48584,48582,48580,48578,48576,48574,48571,48569,48567,48565,
48563,48561,48559,48557,48555,48553,48551,48549,48547,48545,48543,48541,48539,48537,48535,48533,
48531,48529,48527,48525,48523,48521,48519,48518,48516,48514,48512,48509,48505,48502,48498,48495,
48491,48488,48485,48481,48478,48475,48471,48468,48465,48462,48459,48456,48453,48450,48447,48444,
48441,48438,48435,48432,48429,48426,48424,48421,48418,48416,48413,48410,48408,48405,48403,48400,
48397,48392,48387,48381,48373,48364,48355,48347,48339,48332,48324,48317,48310,48303,48296,48289,
48283,48277,48271,48265,48260,48253,48243,48233,48223,48214,48204,48196,48187,48179,48171,48164,
48156,48149,48143,48136,48130,48120,48108,48097,48087,48076,48067,48057,48048,48039,48031,48023,
48016,48008,48001,47990,47977,47965,47953,47942,47932,47922,47912,47903,47894,47886,47878,47869,
47855,47841,47829,47816,47805,47794,47783,47774,47764,47755,47747,47734,47719,47705,47691,47679,
47667,47656,47645,47635,47626,47617,47601,47585,47570,47556,47543,47531,47519,47508,47498,47489,
47471,47455,47439,47424,47411,47398,47386,47375,47365,47350,47332,47315,47299,47284,47271,47258,
47246,47235,47218,47200,47182,47166,47151,47137,47124,47112,47098,47078,47059,47041,47025,47011,
46991,46960,46921,46887,46859,46823,46784,46751,46723,46679,46641,46609,46573,46529,46493,46464,
46415,46375,46343,46297,46254,46220,46175,46130,46093,46048,46001,45964,45916,45869,45831,45779,
45732,45696,45638,45593,45548,45493,45451,45397,45346,45303,45243,45198,45142,45089,45042,44981,
44935,44873,44821,44764,44706,44655,44591,44544,44475,44424,44357,44302,44237,44179,44115,44054,
43990,43928,43863,43800,43733,43669,43601,43537,43466,43404,43329,43268,43189,43128,43049,42981,
42907,42833,42764,42684,42618,42535,42461,42386,42304,42236,42149,42070,41995,41908,41832,41749,
41663,41587,41499,41412,41335,41243,41154,41075,40983,40891,40807,40718,40622,40532,40449,40348,
40252,40162,40071,39969,39872,39779,39686,39581,39480,39382,39288,39183,39077,38973,38872,38774,
38617,38409,38186,37968,37754,37523,37291,37059,36827,36594,36355,36107,35858,35606,35351,35094,
34834,34571,34307,34033,33754,33473,33192,32911,32798,32774,32769,32768,32768,32768,32768,32768,
32768,32768,32768,32768,32768,32768,32768,32768,32768,32768,32768,32768,32768,32768,32768,32768,
32768,32768,32768,32768,32768,32768,32768,32768,32768,32768,32768,32768,32768,32768,32768,32768,
32768,32768,32768,32768,32768,32768,32768,32768,32768,32768,32768,32768,32768,32768,32768,32768,
32768,32768,32768,32768,32768,32768,32768,32768,32768,32768,32768,32768,32768,32768,32768,32768,
32768,32768,32768,32768,32768,32768,32768,32768,32768,32768,32768,32768,32768,32768,32768,32768,
32768,32768,32768,32768,32768,32768,32768,32768,32768,32768,32768,32768,32768,32768,32768,32768,
32768,32768,32768,32768,32768,32768,32768,32768,32768,32768,32768,32768,32768,32768,32768,32768,
32768,32768,32768,32768,32768,32768,32768,32768,32768,32768,32768,32768,32768,32768,32768,32768,
32768,32768,32768,32768,32768,32768,32768,32768,32768,32768,32768,32768,32768,32768,32768,32768,
32768,32768,32768,32768,32768,32768,32768,32768,32768,32768,32768,32768,32768,32768,32768,32768,
32768,32768,32768,32768,32768,32768,32768,32768,32768,32768,32768,32768,32768,32768,32768,32768,
32768,32768,32768,32768,32768,32768,32768,32768,32768,32768,32768,32768,32768,32768,32768,32768,
32768,32768,32768,32768,32768,32768,32768,32768,32768,32768,32768,32768,32768,32768,32768,32768,
32768,32768,32768,32768,32768,32768,32768,32768,32768,32768,32768,32768,32768,32768,32768,32768,
32768,32768,32768,32768,32768,32768,32768,32768,32768,32768,32768,32768,32768,32768,32768,32768,
32768,32768,32768,32768,32768,32768,32768,32768,32768,32768,32768,32768,32768,32768,32768,32768,
32768,32768,32768,32768,32768,32768,32768,32768,32768,32768,32768,32768,32768,32768,32768,32768,
32768,32768,32768,32768,32768,32768,32768,32768,32768,32768,32768,32768,32768,32768,32768,32768,
32768,32768,32768,32768,32768,32768,32768,32768,32768,32768,32768,32768,32768,32768,32768,32768,
32768,32768,32768,32768,32768,32768,32768,32768,32768,32768,32768,32768,32768,32768,32768,32768,
32768,32768,32768,32768,32768,32768,32768,32768,32768,32768,32768,32768,32768,32768,32768,32768,
32768,32768,32768,32768,32768,32768,32768,32768,32768,32768,32768,32768,32768,32768,32768,32768,
32768,32768,32768,32768,32768,32768,32768,32768,32768,32768,32768,32768,32768,32768,32768,32768,
32768,32768,32768,32768,32768,32768,32768,32768,32768,32768,32768,32768,32768,32768,32768,32768,
32768,32768,32768,32768,32768,32768,32768,32768,32768,32768,32768,32768,32768,32768,32768,32768,
32768,32768,32768,32768,32768,32768,32768,32768,32768,32768,32768,32768,32768,32768,32768,32768,
32768,32768,32768,32768,32768,32768,32768,32768,32768,32768,32768,32768,32768,32768,32768,32768,
32768,32768,32768,32768,32768,32768,32768,32768,32768,32768,32768,32768,32768,32768,32768,32768,
32768,32768,32768,32768,32768,32768,32768,32768,32768,32768,32768,32768,32768,32768,32768,32768,
32768,32768,32768,32768,32768,32768,32768,32768,32768,32768,32768,32768,32768,32768,32768,32768,
32768,32768,32768,32768,32768,32768,32768,32768,32768,32768,32768,32768,32768,32768,32768,32768,
32768,32768,32768,32768,32768,32768,32768,32768,32768,32768,32768,32768,32768,32768,32768,32768,
32768,32768,32768,32768,32768,32768,32768,32768,32768,32768,32768,32768,32768,32768,32768,32768,
32768,32768,32768,32768,32768,32768,32768,32768,32768,32768,32768,32768,32768,32768,32768,32768,
32768,32768,32768,32768,32768,32768,32768,32768,32768,32768,32768,32768,32768,32768,32768,32768,
32768,32768,32768,32768,32768,32768,32768,32768,32768,32768,32768,32768,32768,32768,32768,32768,
32768,32768,32768,32768,32768,32768,32768,32768,32768,32768,32768,32768,32768,32768,32768,32768,
32768,32768,32768,32768,32768,32768,32768,32768,32768,32768,32768,32768,32768,32768,32768,32768,
32768,32768,32768,32768,32768,32768,32768,32768,32768,32768,32768,32768,32768,32768,32768,32768,
32768,32768,32768,32768,32768,32768,32768,32768,32768,32768,32768,32768,32768,32768,32768,32768,
32768,32768,32768,32768,32768,32768,32768,32768,32768,32768,32768,32768,32768,32768,32768,32768,
32768,32768,32768,32768,32768,32768,32768,32768,32768,32768,32768,32768,32768,32768,32768,32768,
32768,32768,32768,32768,32768,32768,32768,32768,32768,32768,32768,32768,32768,32768,32768,32768,
32768,32768,32768,32768,32768,32768,32768,32768,32768,32768,32768,32768,32768,32768,32768,32768,
32768,32768,32768,32768,32768,32768,32768,32768,32768,32768,32768,32768,32768,32768,32768,32768,
32768,32768,32768,32768,32768,32768,32768,32768,32768,32768,32768,32768,32768,32768,32768,32768,
32768,32768,32768,32768,32768,32768,32768,32768,32768,32768,32768,32768,32768,32768,32768,32768,
32768,32768,32768,32768,32768,32768,32768,32768,32768,32768,32768,32768,32768,32768,32768,32768,
32768,32768,32768,32768,32768,32768,32768,32768,32768,32768,32768,32768,32768,32768,32768,32768,
32768,32768,32768,32768,32768,32768,32768,32768,32768,32768,32768,32768,32768,32768,32768,32768,
32768,32768,32768,32768,32768,32768,32768,32768,32768,32768,32768,32768,32768,32768,32768,32768,
32768,32768,32768,32768,32768,32768,32768,32768,32768,32768,32768,32768,32768,32768,32768,32768,
32768,32768,32768,32768,32768,32768,32768,32768,32768,32768,32768,32768,32768,32768,32768,32768,
32768,32768,32768,32768,32768,32768,32768,32768,32768,32768,32768,32768,32768,32768,32768,32768,
32768,32768,32768,32768,32768,32768,32768,32768,32768,32768,32768,32768,32768,32768,32768,32768,
32768,32768,32768,32768,32768,32768,32768,32768,32768,32768,32768,32768,32768,32768,32768,32768,
32768,32768,32768,32768,32768,32768,32768,32768,32768,32768,32768,32768,32768,32768,32768,32768,
32768,32768,32768,32768,32768,32768,32768,32768,32768,32768,32768,32768,32768,32768,32768,32768,
32768,32768,32768,32768,32768,32768,32768,32768,32768,32768,32768,32768,32768,32768,32768,32768,
32768,32768,32768,32768,32768,32768,32768,32768,32768,32768,32768,32768,32768,32768,32768,32768,
32768,32768,32768,32768,32768,32768,32768,32768,32768,32768,32768,32768,32768,32768,32768,32768,
32768,32768,32768,32768,32768,32768,32768,32768,32768,32768,32768,32768,32768,32768,32768,32768,
32768,32768,32768,32768,32768,32768,32768,32768,32768,32768,32768,32768,32768,32768,32768,32768,
32768,32768,32768,32768,32768,32768,32768,32768,32768,32768,32768,32768,32768,32768,32768,32768,
32768,32768,32768,32768,32768,32768,32768,32768,32768,32768,32768,32768,32768,32768,32768,32768,
32768,32768,32768,32768,32768,32768,32768,32768,32768,32768,32768,32768,32768,32768,32768,32768,
32768,32768,32768,32768,32768,32768,32768,32768,32768,32768,32768,32768,32768,32768,32768,32768,
32768,32768,32768,32768,32768,32768,32768,32768,32768,32768,32768,32768,32768,32768,32768,32768,
32768,32768,32768,32768,32768,32768,32768,32768,32768,32768,32768,32768,32768,32768,32768,32768,
32768,32768,32768,32768,32768,32768,32768,32768,32768,32768,32768,32768,32768,32768,32768,32768,
32768,32768,32768,32768,32768,32768,32768,32768,32768,32768,32768,32768,32768,32768,32768,32768,
32768,32768,32768,32768,32768,32768,32768,32768,32768,32768,32768,32768,32768,32768,32768,32768,
32768,32768,32768,32768,32768,32768,32768,32768,32768,32768,32768,32768,32768,32768,32768,32768,
32768,32768,32768,32768,32768,32768,32768,32768,32768,32768,32768,32768,32768,32768,32768,32768,
32768,32768,32768,32768,32768,32768,32768,32768,32768,32768,32768,32768,32768,32768,32768,32768,
32768,32768,32768,32768,32768,32768,32768,32768,32768,32768,32768,32768,32768,32768,32768,32768,
32768,32768,32768,32768,32768,32768,32768,32768,32768,32768,32768,32768,32768,32768,32768,32768,
32768,32768,32768,32768,32768,32768,32768,32768,32768,32768,32768,32768,32768,32768,32768,32768,
32768,32768,32768,32768,32768,32768,32768,32768,32768,32768,32768,32768,32768,32768,32768,32768,
32768,32768,32768,32768,32768,32768,32768,32768,32768,32768,32768,32768,32768,32768,32768,32768,
32768,32768,32768,32768,32768,32768,32768,32768,32768,32768,32768,32768,32768,32768,32768,32768,
32768,32768,32768,32768,32768,32768,32768,32768,32768,32768,32768,32768,32768,32768,32768,32768,
32768,32768,32768,32768,32768,32768,32768,32768,32768,32768,32768,32768,32768,32768,32768,32768,
32768,32768,32768,32768,32768,32768,32768,32768,32768,32768,32768,32768,32768,32768,32768,32768,
32768,32768,32768,32768,32768,32768,32768,32768,32768,32768,32768,32768,32768,32768,32768,32768,
32768,32768,32768,32768,32768,32768,32768,32768,32768,32768,32768,32768,32768,32768,32768,32768,
32768,32768,32768,32768,32768,32768,32768,32768,32768,32768,32768,32768,32768,32768,32768,32768,
32768,32768,32768,32768,32768,32768,32768,32768,32768,32768,32768,32768,32768,32768,32768,32768,
32768,32768,32768,32768,32768,32768,32768,32768,32768,32768,32768,32768,32768,32768,32768,32768,
32768,32768,32768,32768,32768,32768,32768,32768,32768,32768,32768,32768,32768,32768,32768,32768,
32768,32768,32768,32768,32768,32768,32768,32768,32768,32768,32768,32768,32768,32768,32768,32768,
32768,32768,32768,32768,32768,32768,32768,32768,32768,32768,32768,32768,32768,32768,32768,32768,
32768,32768,32768,32768,32768,32768,32768,32768,32768,32768,32768,32768,32768,32768,32768,32768,
32768,32768,32768,32768,32768,32768,32768,32768,32768,32768,32768,32768,32768,32768,32768,32768,
32768,32768,32768,32768,32768,32768,32768,32768,32768,32768,32768,32768,32768,32768,32768,32768,
32768,32768,32768,32768,32768,32768,32768,32768,32768,32768,32768,32768,32768,32768,32768,32768,
32768,32768,32768,32768,32768,32768,32768,32768,32768,32768,32768,32768,32768,32768,32768,32768,
32768,32768,32768,32768,32768,32768,32768,32768,32768,32768,32768,32768,32768,32768,32768,32768,
32768,32768,32768,32768,32768,32768,32768,32768,32768,32768,32768,32768,32768,32768,32768,32768,
32768,32768,32768,32768,32768,32768,32768,32768,32768,32768,32768,32768,32768,32768,32768,32768,
32768,32768,32768,32768,32768,32768,32768,32768,32768,32768,32768,32768,32768,32768,32768,32768,
32768,32768,32768,32768,32768,32768,32768,32768,32768,32768,32768,32768,32768,32768,32768,32768,
32768,32768,32768,32768,32768,32768,32768,32768,32768,32768,32768,32768,32768,32768,32768,32768,
32768,32768,32768,32768,32768,32768,32768,32768,32768,32768,32768,32768,32768,32768,32768,32768,
32768,32768,32768,32768,32768,32768,32768,32768,32768,32768,32768,32768,32768,32768,32768,32768,
32768,32768,32768,32768,32768,32768,32768,32768,32768,32768,32768,32768,32768,32768,32768,32768,
32768,32768,32768,32768,32768,32768,32768,32768,32768,32768,32768,32768,32768,32768,32768,32768,
32768,32768,32768,32768,32768,32768,32768,32768,32768,32768,32768,32768,32768,32768,32768,32768,
32768,32768,32768,32768,32768,32768,32768,32768,32768,32768,32768,32768,32768,32768,32768,32768,
32768,32768,32768,32768,32768,32768,32768,32768,32768,32768,32768,32768,32768,32768,32768,32768,
32704,32704,32704,32704,32704,32704,32704,32704,32704,32704,32704,32704,32704,32704,32704,32704,
32704,32704,32704,32704,32704,32704,32704,32704,32704,32704,32704,32704,32704,32704,32704,32704,
32704,32704,32704,32704,32704,32704,32704,32704,32704,32704,32704,32704,32704,32704,32704,32704,
32704,32704,32704,32704,32704,32704,32704,32704,32704,32704,32704,32704,32704,32704,32704,32704,
32704,32704,32704,32704,32704,32704,32704,32704,32704,32704,32704,32704,32704,32704,32704,32704,
32704,32704,32704,32704,32704,32704,32704,32704,32704,32704,32704,32704,32704,32704,32704,32704,
32704,32704,32704,32704,32704,32704,32704,32704,32704,32704,32704,32704,32704,32704,32704,32704,
32704,32704,32704,32704,32704,32704,32704,32704,32704,32704,32704,32704,32704,32704,32704,32704
};

__device__ __forceinline__ float bf2f(unsigned short v) { return __uint_as_float(((unsigned)v) << 16); }
__device__ __forceinline__ unsigned short f2bf(float f) { typedef __bf16 b1; b1 b = (b1)f; return __builtin_bit_cast(unsigned short, b); }
__device__ __forceinline__ void slow_path(char* smem, const unsigned short* x, const unsigned short* dwl, const unsigned short* dws,
                                       const unsigned short* w1, const unsigned short* w2, unsigned short* out) {
  constexpr int H = 512, W = 512, C = 96, E = 384;
  float* y = (float*)smem;
  float* g = y + 32 * 97;
  const int tid = threadIdx.x;
  const int xcd = blockIdx.x & 7, idx = blockIdx.x >> 3;
  const int R0 = 64 * (xcd >> 2) + 8 * (idx >> 2), w0 = 128 * (xcd & 3) + 32 * (idx & 3);
  for (int it = 0; it < 32; ++it) {
    const int h = 128 * (it >> 3) + R0 + (it & 7), p0 = h * W + w0;
    __syncthreads();
    for (int i = tid; i < 32 * C; i += 512) {
      int p = i / C, c = i % C, w = w0 + p;
      float acc = bf2f(x[(h * W + w) * C + c]) * bf2f(dwl[c]);
      for (int a = 0; a < 5; ++a) for (int b = 0; b < 5; ++b) {
        int hh = h + a - 2, ww = w + b - 2;
        if (hh >= 0 && hh < H && ww >= 0 && ww < W) acc += bf2f(x[(hh * W + ww) * C + c]) * bf2f(dws[(a * 5 + b) * C + c]);
      }
      y[p * 97 + c] = acc;
    }
    __syncthreads();
    if (tid < 32) {
      float m = 0; for (int c = 0; c < C; ++c) m += y[tid * 97 + c]; m *= (1.f / C);
      float v = 0; for (int c = 0; c < C; ++c) { float d = y[tid * 97 + c] - m; v += d * d; } v *= (1.f / C);
      float r = 1.f / sqrtf(v + 1e-5f);
      for (int c = 0; c < C; ++c) y[tid * 97 + c] = bf2f(f2bf((y[tid * 97 + c] - m) * r));
    }
    __syncthreads();
    for (int i = tid; i < 32 * E; i += 512) {
      int p = i / E, e = i % E; float acc = 0;
      for (int c = 0; c < C; ++c) acc += y[p * 97 + c] * bf2f(w1[c * E + e]);
      float u = 0.7978845608028654f * (acc + 0.044715f * acc * acc * acc);
      g[p * 385 + e] = bf2f(f2bf(0.5f * acc * (1.f + tanhf(u))));
    }
    __syncthreads();
    for (int i = tid; i < 32 * C; i += 512) {
      int p = i / C, c = i % C; float acc = 0;
      for (int e = 0; e < E; ++e) acc += g[p * 385 + e] * bf2f(w2[e * C + c]);
      int idx = (p0 + p) * C + c;
      out[idx] = f2bf(bf2f(x[idx]) + acc);
    }
  }
}

__global__ __launch_bounds__(512) void replk_block_kernel(const unsigned short* x, const unsigned short* dwl, const unsigned short* dws,
                                                          const unsigned short* w1, const unsigned short* w2, unsigned short* out) {
  __shared__ __attribute__((aligned(16))) char smem[163840];
  unsigned flag;
  asm volatile(
    "s_mov_b64 s[28:29], %1\n"
    "s_mov_b64 s[20:21], %4\n"
    "s_mov_b64 s[24:25], %5\n"
    "s_mov_b64 s[32:33], %6\n"
    "s_and_b32 s29, s29, 0xffff\n"
    "s_mov_b32 s30, 50331648\n"
    "s_mov_b32 s31, 0x20000\n"
    "s_and_b32 s21, s21, 0xffff\n"
    "s_mov_b32 s22, 73728\n"
    "s_mov_b32 s23, 0x20000\n"
    "s_and_b32 s25, s25, 0xffff\n"
    "s_mov_b32 s26, 73728\n"
    "s_mov_b32 s27, 0x20000\n"
    "s_and_b32 s33, s33, 0xffff\n"
    "s_mov_b32 s34, 50331648\n"
    "s_mov_b32 s35, 0x20000\n"
    "v_and_b32 v13, 63, %7\n"
    "v_lshrrev_b32 v14, 6, %7\n"
    "s_nop 0\n"
    "v_readfirstlane_b32 s43, v14\n"
    "s_and_b32 s45, %8, 7\n"
    "s_lshr_b32 s46, %8, 3\n"
    "s_lshr_b32 s55, s45, 2\n"
    "s_lshl_b32 s55, s55, 6\n"
    "s_lshr_b32 s44, s46, 2\n"
    "s_lshl_b32 s44, s44, 3\n"
    "s_add_u32 s55, s55, s44\n"
    "s_and_b32 s56, s45, 3\n"
    "s_lshl_b32 s56, s56, 7\n"
    "s_and_b32 s44, s46, 3\n"
    "s_lshl_b32 s44, s44, 5\n"
    "s_add_u32 s56, s56, s44\n"
    "s_mov_b32 s57, 0\n"
    "s_add_u32 s45, s56, 2\n"
    "s_mul_i32 s63, s45, 192\n"
    "s_sub_u32 s45, s56, 2\n"
    "s_cmp_eq_u32 s56, 0\n"
    "s_cselect_b32 s45, 0, s45\n"
    "s_mul_i32 s64, s45, 192\n"
    "v_lshlrev_b32 v3, 4, v13\n"
    "v_mov_b32 v14, 0x80000000\n"
    "v_subrev_u32 v15, 384, v3\n"
    "v_cmp_gt_u32 vcc, 24, v13\n"
    "s_nop 1\n"
    "v_cndmask_b32 v15, v15, v14, vcc\n"
    "s_cmp_eq_u32 s56, 0\n"
    "s_cselect_b64 s[70:71], -1, 0\n"
    "v_cndmask_b32 v4, v3, v15, s[70:71]\n"
    "v_cndmask_b32 v15, v14, v3, vcc\n"
    "s_cmp_eq_u32 s56, 480\n"
    "s_cselect_b64 s[70:71], -1, 0\n"
    "v_cndmask_b32 v5, v3, v15, s[70:71]\n"
    "s_cmp_lg_u32 s43, 7\n"
    "s_cbranch_scc1 L_nopf_%=\n"
    "s_getpc_b64 s[74:75]\n"
    "L_pc_%=:\n"
    "s_lshr_b32 s73, %8, 3\n"
    "s_lshl_b32 s73, s73, 10\n"
    "v_add_u32 v252, s73, v3\n"
    "s_mov_b32 s45, L_done_%=-L_pc_%=\n"
    "global_load_dwordx4 v[248:251], v252, s[74:75]\n"
    "s_add_u32 s73, s73, 32768\n"
    "s_cmp_ge_u32 s73, s45\n"
    "s_cbranch_scc1 L_nopf_%=\n"
    "v_add_u32 v252, s73, v3\n"
    "global_load_dwordx4 v[248:251], v252, s[74:75]\n"
    "L_nopf_%=:\n"
    "s_cmp_lg_u32 s43, 0\n"
    "s_cbranch_scc1 L_nodw_%=\n"
    "s_mov_b64 s[76:77], %3\n"
    "s_and_b32 s77, s77, 0xffff\n"
    "s_mov_b32 s78, 4800\n"
    "s_mov_b32 s79, 0x20000\n"
    "s_mov_b32 m0, 150208\n"
    "s_nop 0\n"
    "buffer_load_dwordx4 v3, s[76:79], 0 offen offset:0 lds\n"
    "buffer_load_dwordx4 v3, s[76:79], 0 offen offset:1024 lds\n"
    "buffer_load_dwordx4 v3, s[76:79], 0 offen offset:2048 lds\n"
    "buffer_load_dwordx4 v3, s[76:79], 0 offen offset:3072 lds\n"
    "s_mov_b32 m0, 154304\n"
    "s_movk_i32 s44, 4096\n"
    "buffer_load_dwordx4 v3, s[76:79], s44 offen lds\n"
    "s_mov_b64 s[76:77], %2\n"
    "s_and_b32 s77, s77, 0xffff\n"
    "s_mov_b32 s78, 192\n"
    "s_mov_b32 m0, 155328\n"
    "s_nop 0\n"
    "buffer_load_dwordx4 v3, s[76:79], 0 offen lds\n"
    "L_nodw_%=:\n"
    "s_sub_u32 s58, s55, 2\n"
    "s_add_u32 s58, s58, s43\n"
    "s_mul_i32 s73, s43, 7056\n"
    "s_add_u32 s73, s73, 65536\n"
    "s_cmp_lt_u32 s58, 512\n"
    "s_cselect_b32 s30, 50331648, 0\n"
    "s_cselect_b32 s58, s58, 0\n"
    "s_mul_i32 s75, s58, 98304\n"
    "s_mov_b32 exec_hi, 0xffff\n"
    "s_add_u32 m0, s73, 0\n"
    "s_add_u32 s74, s75, s64\n"
    "buffer_load_dwordx4 v4, s[28:31], s74 offen nt lds\n"
    "s_mov_b32 exec_hi, -1\n"
    "v_mov_b32 v15, v15\n"
    "s_and_b32 s45, s43, 3\n"
    "s_mul_i32 s59, s45, 21168\n"
    "s_add_u32 s59, s59, 65536\n"
    "v_lshrrev_b32 v14, 2, v13\n"
    "v_mul_u32_u24 v14, 768, v14\n"
    "v_and_b32 v15, 3, v13\n"
    "v_lshlrev_b32 v15, 4, v15\n"
    "v_add_u32 v14, v14, v15\n"
    "s_cmp_lt_u32 s43, 2\n"
    "s_cselect_b64 s[70:71], -1, 0\n"
    "v_cndmask_b32 v6, v3, v14, s[70:71]\n"
    "s_cmp_lt_u32 s43, 2\n"
    "s_cselect_b32 s36, s20, s24\n"
    "s_cselect_b32 s37, s21, s25\n"
    "s_mov_b32 s38, 73728\n"
    "s_mov_b32 s39, 0x20000\n"
    "s_movk_i32 s47, 1024\n"
    "s_cmp_lt_u32 s43, 2\n"
    "s_cselect_b32 s47, 12288, s47\n"
    "s_and_b32 s45, s43, 1\n"
    "s_mul_i32 s45, s45, 3\n"
    "s_lshl_b32 s46, s45, 10\n"
    "s_add_u32 s54, s46, 16384\n"
    "s_add_u32 s44, s54, 6144\n"
    "s_cmp_lt_u32 s43, 2\n"
    "s_cselect_b32 s54, s54, s44\n"
    "s_mul_i32 s51, s45, s47\n"
    "s_movk_i32 s49, 6144\n"
    "s_cmp_lt_u32 s43, 2\n"
    "s_cselect_b32 s49, 64, s49\n"
    "s_mul_i32 s50, s49, 12\n"
    "s_add_u32 s50, s50, s51\n"
    "s_mul_i32 s44, s49, 10\n"
    "s_cmp_lt_u32 s43, 2\n"
    "s_cselect_b32 s44, 0, s44\n"
    "s_add_u32 s48, s51, s44\n"
    "s_mov_b32 exec_hi, 0xffff\n"
    "s_add_u32 m0, s73, 784\n"
    "s_add_u32 s74, s75, s63\n"
    "buffer_load_dwordx4 v3, s[28:31], s74 offen nt lds\n"
    "s_mov_b32 exec_hi, -1\n"
    "s_mov_b32 exec_hi, 0xffff\n"
    "s_add_u32 m0, s73, 1568\n"
    "s_add_u32 s74, s74, 768\n"
    "buffer_load_dwordx4 v3, s[28:31], s74 offen nt lds\n"
    "s_mov_b32 exec_hi, -1\n"
    "v_lshrrev_b32 v156, 5, v13\n"
    "v_bfe_u32 v157, v13, 4, 1\n"
    "v_bfe_u32 v158, v13, 2, 2\n"
    "v_and_b32 v159, 3, v13\n"
    "v_lshlrev_b32 v14, 3, v156\n"
    "v_add_u32 v14, v14, v158\n"
    "v_lshlrev_b32 v14, 6, v14\n"
    "v_lshl_add_u32 v14, v157, 5, v14\n"
    "v_lshl_add_u32 v14, v159, 3, v14\n"
    "s_mov_b32 exec_hi, 0xffff\n"
    "s_add_u32 m0, s73, 2352\n"
    "s_add_u32 s74, s74, 768\n"
    "buffer_load_dwordx4 v3, s[28:31], s74 offen nt lds\n"
    "s_mov_b32 exec_hi, -1\n"
    "v_add_u32 v1, 16384, v14\n"
    "v_lshl_add_u32 v14, v156, 2, v158\n"
    "v_mul_u32_u24 v14, 192, v14\n"
    "v_lshl_add_u32 v14, v157, 5, v14\n"
    "v_and_b32 v15, 1, v159\n"
    "v_lshl_add_u32 v14, v15, 4, v14\n"
    "v_lshrrev_b32 v15, 1, v159\n"
    "v_lshl_add_u32 v14, v15, 3, v14\n"
    "v_add_u32 v2, 22528, v14\n"
    "v_bfe_u32 v160, v13, 3, 1\n"
    "v_and_b32 v161, 7, v13\n"
    "v_lshrrev_b32 v162, 4, v13\n"
    "v_mul_u32_u24 v14, 28224, v160\n"
    "v_lshrrev_b32 v15, 1, v162\n"
    "v_add_u32 v15, v15, v161\n"
    "s_mov_b32 exec_hi, 0xffff\n"
    "s_add_u32 m0, s73, 3136\n"
    "s_add_u32 s74, s74, 768\n"
    "buffer_load_dwordx4 v3, s[28:31], s74 offen nt lds\n"
    "s_mov_b32 exec_hi, -1\n"
    "v_mul_u32_u24 v15, 784, v15\n"
    "v_add_u32 v14, v14, v15\n"
    "v_and_b32 v15, 1, v162\n"
    "v_mul_u32_u24 v15, 384, v15\n"
    "v_add_u32 v14, v14, v15\n"
    "s_mul_i32 s45, s43, 24\n"
    "s_add_u32 s45, s45, 65536\n"
    "v_add_u32 v7, s45, v14\n"
    "v_lshlrev_b32 v14, 2, v160\n"
    "v_add_u32 v14, 2, v14\n"
    "v_mul_u32_u24 v14, 7056, v14\n"
    "v_add_u32 v15, 2, v162\n"
    "v_lshrrev_b32 v9, 2, v15\n"
    "v_add_u32 v9, v9, v161\n"
    "v_mul_u32_u24 v9, 784, v9\n"
    "s_mov_b32 exec_hi, 0xffff\n"
    "s_add_u32 m0, s73, 3920\n"
    "s_add_u32 s74, s74, 768\n"
    "buffer_load_dwordx4 v3, s[28:31], s74 offen nt lds\n"
    "s_mov_b32 exec_hi, -1\n"
    "v_add_u32 v14, v14, v9\n"
    "v_and_b32 v15, 3, v15\n"
    "v_mul_u32_u24 v15, 192, v15\n"
    "v_add_u32 v14, v14, v15\n"
    "v_add_u32 v9, s45, v14\n"
    "v_and_b32 v163, 31, v13\n"
    "v_mov_b32 v164, v163\n"
    "v_and_b32 v14, 15, v13\n"
    "v_mul_u32_u24 v14, 192, v14\n"
    "v_bfe_u32 v15, v13, 4, 1\n"
    "v_lshl_add_u32 v14, v15, 5, v14\n"
    "v_lshrrev_b32 v15, 5, v13\n"
    "v_lshl_add_u32 v12, v15, 4, v14\n"
    "v_add_u32 v10, 3072, v12\n"
    "v_add_u32 v164, 2, v164\n"
    "v_lshrrev_b32 v14, 2, v164\n"
    "s_mov_b32 exec_hi, 0xffff\n"
    "s_add_u32 m0, s73, 4704\n"
    "s_add_u32 s74, s74, 768\n"
    "buffer_load_dwordx4 v3, s[28:31], s74 offen nt lds\n"
    "s_mov_b32 exec_hi, -1\n"
    "v_mul_u32_u24 v14, 784, v14\n"
    "v_and_b32 v15, 3, v164\n"
    "v_mul_u32_u24 v15, 192, v15\n"
    "v_add_u32 v14, v14, v15\n"
    "v_lshl_add_u32 v14, v156, 4, v14\n"
    "s_add_u32 s45, s43, 2\n"
    "s_mul_i32 s45, s45, 7056\n"
    "s_add_u32 s45, s45, 65536\n"
    "v_add_u32 v11, s45, v14\n"
    "v_bfe_u32 v14, v163, 2, 1\n"
    "v_cmp_eq_u32 vcc, v14, v156\n"
    "v_bfe_u32 v14, v163, 3, 1\n"
    "v_lshlrev_b32 v14, 2, v14\n"
    "v_and_b32 v15, 3, v163\n"
    "v_add_u32 v14, v14, v15\n"
    "s_mov_b32 exec_hi, 0xffff\n"
    "s_add_u32 m0, s73, 5488\n"
    "s_add_u32 s74, s74, 768\n"
    "buffer_load_dwordx4 v3, s[28:31], s74 offen nt lds\n"
    "s_mov_b32 exec_hi, -1\n"
    "v_and_b32 v15, 1, v14\n"
    "v_lshlrev_b32 v15, 4, v15\n"
    "v_mov_b32 v164, 0x3f80\n"
    "v_lshlrev_b32 v15, v15, v164\n"
    "v_cndmask_b32 v15, 0, v15, vcc\n"
    "v_lshrrev_b32 v14, 1, v14\n"
    "v_lshrrev_b32 v164, 4, v163\n"
    "v_lshl_add_u32 v14, v164, 2, v14\n"
    "v_cmp_eq_u32 vcc, 0, v14\n"
    "s_nop 1\n"
    "v_cndmask_b32 v240, 0, v15, vcc\n"
    "v_cmp_eq_u32 vcc, 1, v14\n"
    "s_nop 1\n"
    "v_cndmask_b32 v241, 0, v15, vcc\n"
    "v_cmp_eq_u32 vcc, 2, v14\n"
    "s_nop 1\n"
    "v_cndmask_b32 v242, 0, v15, vcc\n"
    "v_cmp_eq_u32 vcc, 3, v14\n"
    "s_nop 1\n"
    "v_cndmask_b32 v243, 0, v15, vcc\n"
    "s_mov_b32 exec_hi, 0xffff\n"
    "s_add_u32 m0, s73, 6272\n"
    "s_add_u32 s74, s74, 768\n"
    "buffer_load_dwordx4 v5, s[28:31], s74 offen nt lds\n"
    "s_mov_b32 exec_hi, -1\n"
    "v_cmp_eq_u32 vcc, 4, v14\n"
    "s_nop 1\n"
    "v_cndmask_b32 v244, 0, v15, vcc\n"
    "v_cmp_eq_u32 vcc, 5, v14\n"
    "s_nop 1\n"
    "v_cndmask_b32 v245, 0, v15, vcc\n"
    "v_cmp_eq_u32 vcc, 6, v14\n"
    "s_nop 1\n"
    "v_cndmask_b32 v246, 0, v15, vcc\n"
    "v_cmp_eq_u32 vcc, 7, v14\n"
    "s_nop 1\n"
    "v_cndmask_b32 v247, 0, v15, vcc\n"
    "s_mov_b32 s40, 0x44e944e9\n"
    "s_mov_b32 s41, 0x37803780\n"
    "s_mov_b32 s42, 0xfff8fff8\n"
    "s_mov_b32 s65, 0x3c2aaaab\n"
    "s_mov_b32 s66, 0x3727c5ac\n"
    "s_mov_b32 s67, 0x3ef00000\n"
    "s_mov_b32 s68, 0x2b8cbccb\n"
    "s_mov_b64 s[70:71], 0\n"
    "s_cmp_lg_u32 s43, 0\n"
    "s_cbranch_scc1 L_nw0_%=\n"
    "s_waitcnt vmcnt(9)\n"
    "L_nw0_%=:\n"
    "s_barrier\n"
    "s_lshr_b32 s45, s43, 1\n"
    "s_add_u32 s45, s45, 8\n"
    "s_mul_i32 s73, s45, 7056\n"
    "s_add_u32 s73, s73, 65536\n"
    "s_add_u32 s58, s55, s45\n"
    "s_sub_u32 s58, s58, 2\n"
    "s_cmp_lt_u32 s58, 512\n"
    "s_cselect_b32 s30, 50331648, 0\n"
    "s_cselect_b32 s58, s58, 0\n"
    "s_mul_i32 s75, s58, 98304\n"
    "s_and_b32 s44, s43, 1\n"
    "s_cmp_lg_u32 s44, 0\n"
    "s_cbranch_scc1 L_xodd_%=\n"
    "s_mov_b32 exec_hi, 0xffff\n"
    "s_add_u32 m0, s73, 0\n"
    "s_add_u32 s74, s75, s64\n"
    "buffer_load_dwordx4 v4, s[28:31], s74 offen nt lds\n"
    "s_mov_b32 exec_hi, -1\n"
    "s_mov_b32 exec_hi, 0xffff\n"
    "s_add_u32 m0, s73, 784\n"
    "s_add_u32 s74, s75, s63\n"
    "buffer_load_dwordx4 v3, s[28:31], s74 offen nt lds\n"
    "s_mov_b32 exec_hi, -1\n"
    "s_mov_b32 exec_hi, 0xffff\n"
    "s_add_u32 m0, s73, 1568\n"
    "s_add_u32 s74, s74, 768\n"
    "buffer_load_dwordx4 v3, s[28:31], s74 offen nt lds\n"
    "s_mov_b32 exec_hi, -1\n"
    "s_mov_b32 exec_hi, 0xffff\n"
    "s_add_u32 m0, s73, 2352\n"
    "s_add_u32 s74, s74, 768\n"
    "buffer_load_dwordx4 v3, s[28:31], s74 offen nt lds\n"
    "s_mov_b32 exec_hi, -1\n"
    "s_mov_b32 exec_hi, 0xffff\n"
    "s_add_u32 m0, s73, 3136\n"
    "s_add_u32 s74, s74, 768\n"
    "buffer_load_dwordx4 v3, s[28:31], s74 offen nt lds\n"
    "s_mov_b32 exec_hi, -1\n"
    "s_branch L_xdone_%=\n"
    "L_xodd_%=:\n"
    "s_add_u32 s74, s75, s63\n"
    "s_add_u32 s74, s74, 2304\n"
    "s_mov_b32 exec_hi, 0xffff\n"
    "s_add_u32 m0, s73, 3920\n"
    "s_add_u32 s74, s74, 768\n"
    "buffer_load_dwordx4 v3, s[28:31], s74 offen nt lds\n"
    "s_mov_b32 exec_hi, -1\n"
    "s_mov_b32 exec_hi, 0xffff\n"
    "s_add_u32 m0, s73, 4704\n"
    "s_add_u32 s74, s74, 768\n"
    "buffer_load_dwordx4 v3, s[28:31], s74 offen nt lds\n"
    "s_mov_b32 exec_hi, -1\n"
    "s_mov_b32 exec_hi, 0xffff\n"
    "s_add_u32 m0, s73, 5488\n"
    "s_add_u32 s74, s74, 768\n"
    "buffer_load_dwordx4 v3, s[28:31], s74 offen nt lds\n"
    "s_mov_b32 exec_hi, -1\n"
    "s_mov_b32 exec_hi, 0xffff\n"
    "s_add_u32 m0, s73, 6272\n"
    "s_add_u32 s74, s74, 768\n"
    "buffer_load_dwordx4 v5, s[28:31], s74 offen nt lds\n"
    "s_mov_b32 exec_hi, -1\n"
    "L_xdone_%=:\n"
    "v_bfe_u32 v156, v13, 2, 2\n"
    "v_and_b32 v157, 3, v13\n"
    "v_lshrrev_b32 v158, 4, v13\n"
    "v_lshlrev_b32 v159, 1, v158\n"
    "v_sub_u32 v159, v159, v156\n"
    "v_and_b32 v164, 1, v157\n"
    "v_lshlrev_b32 v164, 4, v164\n"
    "v_mov_b32 v14, -1\n"
    "v_cmp_gt_u32 vcc, 5, v159\n"
    "s_nop 1\n"
    "v_cndmask_b32 v160, 0, v14, vcc\n"
    "v_add_u32 v15, 1, v159\n"
    "v_cmp_gt_u32 vcc, 5, v15\n"
    "s_nop 1\n"
    "v_cndmask_b32 v162, 0, v14, vcc\n"
    "v_cmp_eq_u32 vcc, 2, v159\n"
    "s_nop 1\n"
    "v_cndmask_b32 v167, 0, v14, vcc\n"
    "v_cmp_eq_u32 vcc, 2, v15\n"
    "s_nop 1\n"
    "v_cndmask_b32 v168, 0, v14, vcc\n"
    "v_med3_i32 v165, v159, 0, 4\n"
    "v_med3_i32 v166, v15, 0, 4\n"
    "s_mul_i32 s45, s43, 24\n"
    "s_add_u32 s46, s45, 150208\n"
    "v_mul_u32_u24 v165, 192, v165\n"
    "v_lshl_add_u32 v165, v157, 1, v165\n"
    "v_add_u32 v165, s46, v165\n"
    "v_mul_u32_u24 v166, 192, v166\n"
    "v_lshl_add_u32 v166, v157, 1, v166\n"
    "v_add_u32 v166, s46, v166\n"
    "v_cmp_gt_u32 vcc, 2, v157\n"
    "s_nop 1\n"
    "v_cndmask_b32 v161, v160, 0, vcc\n"
    "v_cndmask_b32 v160, 0, v160, vcc\n"
    "v_cndmask_b32 v163, v162, 0, vcc\n"
    "v_cndmask_b32 v162, 0, v162, vcc\n"
    "s_add_u32 s46, s45, 155328\n"
    "v_lshlrev_b32 v169, 1, v157\n"
    "v_add_u32 v169, s46, v169\n"
    "ds_read_u16 v170, v169 offset:0\n"
    "ds_read_u16 v180, v165 offset:0\n"
    "ds_read_u16 v182, v166 offset:0\n"
    "ds_read_u16 v184, v165 offset:960\n"
    "ds_read_u16 v186, v166 offset:960\n"
    "ds_read_u16 v188, v165 offset:1920\n"
    "ds_read_u16 v190, v166 offset:1920\n"
    "ds_read_u16 v192, v165 offset:2880\n"
    "ds_read_u16 v194, v166 offset:2880\n"
    "ds_read_u16 v196, v165 offset:3840\n"
    "ds_read_u16 v198, v166 offset:3840\n"
    "ds_read_u16 v171, v169 offset:8\n"
    "ds_read_u16 v200, v165 offset:8\n"
    "ds_read_u16 v202, v166 offset:8\n"
    "ds_read_u16 v204, v165 offset:968\n"
    "s_waitcnt lgkmcnt(14)\n"
    "ds_read_u16 v206, v166 offset:968\n"
    "s_waitcnt lgkmcnt(14)\n"
    "ds_read_u16 v208, v165 offset:1928\n"
    "s_waitcnt lgkmcnt(14)\n"
    "ds_read_u16 v210, v166 offset:1928\n"
    "s_waitcnt lgkmcnt(14)\n"
    "ds_read_u16 v212, v165 offset:2888\n"
    "s_waitcnt lgkmcnt(14)\n"
    "ds_read_u16 v214, v166 offset:2888\n"
    "s_waitcnt lgkmcnt(14)\n"
    "ds_read_u16 v216, v165 offset:3848\n"
    "s_waitcnt lgkmcnt(14)\n"
    "ds_read_u16 v218, v166 offset:3848\n"
    "s_waitcnt lgkmcnt(14)\n"
    "ds_read_u16 v172, v169 offset:16\n"
    "s_waitcnt lgkmcnt(14)\n"
    "ds_read_u16 v220, v165 offset:16\n"
    "s_waitcnt lgkmcnt(14)\n"
    "ds_read_u16 v222, v166 offset:16\n"
    "s_waitcnt lgkmcnt(14)\n"
    "ds_read_u16 v224, v165 offset:976\n"
    "s_waitcnt lgkmcnt(14)\n"
    "ds_read_u16 v226, v166 offset:976\n"
    "s_waitcnt lgkmcnt(14)\n"
    "ds_read_u16 v228, v165 offset:1936\n"
    "s_waitcnt lgkmcnt(14)\n"
    "ds_read_u16 v230, v166 offset:1936\n"
    "s_waitcnt lgkmcnt(14)\n"
    "ds_read_u16 v232, v165 offset:2896\n"
    "s_waitcnt lgkmcnt(14)\n"
    "ds_read_u16 v234, v166 offset:2896\n"
    "s_waitcnt lgkmcnt(14)\n"
    "ds_read_u16 v236, v165 offset:3856\n"
    "s_waitcnt lgkmcnt(14)\n"
    "ds_read_u16 v238, v166 offset:3856\n"
    "v_lshlrev_b32 v180, v164, v180\n"
    "v_lshlrev_b32 v182, v164, v182\n"
    "v_and_b32 v181, v161, v180\n"
    "v_and_b32 v180, v160, v180\n"
    "v_and_b32 v183, v163, v182\n"
    "v_and_b32 v182, v162, v182\n"
    "v_lshlrev_b32 v184, v164, v184\n"
    "v_lshlrev_b32 v186, v164, v186\n"
    "v_and_b32 v185, v161, v184\n"
    "v_and_b32 v184, v160, v184\n"
    "v_and_b32 v187, v163, v186\n"
    "v_and_b32 v186, v162, v186\n"
    "v_lshlrev_b32 v170, 16, v170\n"
    "v_lshlrev_b32 v188, 16, v188\n"
    "v_and_b32 v14, v167, v170\n"
    "v_add_f32 v188, v188, v14\n"
    "v_cvt_pk_bf16_f32 v188, v188, v188\n"
    "v_and_b32 v188, 0xffff, v188\n"
    "v_lshlrev_b32 v190, 16, v190\n"
    "v_and_b32 v14, v168, v170\n"
    "v_add_f32 v190, v190, v14\n"
    "v_cvt_pk_bf16_f32 v190, v190, v190\n"
    "v_and_b32 v190, 0xffff, v190\n"
    "v_lshlrev_b32 v188, v164, v188\n"
    "v_lshlrev_b32 v190, v164, v190\n"
    "v_and_b32 v189, v161, v188\n"
    "v_and_b32 v188, v160, v188\n"
    "v_and_b32 v191, v163, v190\n"
    "v_and_b32 v190, v162, v190\n"
    "v_lshlrev_b32 v192, v164, v192\n"
    "v_lshlrev_b32 v194, v164, v194\n"
    "v_and_b32 v193, v161, v192\n"
    "v_and_b32 v192, v160, v192\n"
    "v_and_b32 v195, v163, v194\n"
    "v_and_b32 v194, v162, v194\n"
    "v_lshlrev_b32 v196, v164, v196\n"
    "v_lshlrev_b32 v198, v164, v198\n"
    "v_and_b32 v197, v161, v196\n"
    "v_and_b32 v196, v160, v196\n"
    "v_and_b32 v199, v163, v198\n"
    "v_and_b32 v198, v162, v198\n"
    "v_lshlrev_b32 v200, v164, v200\n"
    "v_lshlrev_b32 v202, v164, v202\n"
    "v_and_b32 v201, v161, v200\n"
    "v_and_b32 v200, v160, v200\n"
    "v_and_b32 v203, v163, v202\n"
    "v_and_b32 v202, v162, v202\n"
    "v_lshlrev_b32 v204, v164, v204\n"
    "v_lshlrev_b32 v206, v164, v206\n"
    "v_and_b32 v205, v161, v204\n"
    "v_and_b32 v204, v160, v204\n"
    "v_and_b32 v207, v163, v206\n"
    "v_and_b32 v206, v162, v206\n"
    "v_lshlrev_b32 v171, 16, v171\n"
    "v_lshlrev_b32 v208, 16, v208\n"
    "v_and_b32 v14, v167, v171\n"
    "v_add_f32 v208, v208, v14\n"
    "v_cvt_pk_bf16_f32 v208, v208, v208\n"
    "v_and_b32 v208, 0xffff, v208\n"
    "v_lshlrev_b32 v210, 16, v210\n"
    "v_and_b32 v14, v168, v171\n"
    "v_add_f32 v210, v210, v14\n"
    "v_cvt_pk_bf16_f32 v210, v210, v210\n"
    "v_and_b32 v210, 0xffff, v210\n"
    "v_lshlrev_b32 v208, v164, v208\n"
    "v_lshlrev_b32 v210, v164, v210\n"
    "v_and_b32 v209, v161, v208\n"
    "v_and_b32 v208, v160, v208\n"
    "v_and_b32 v211, v163, v210\n"
    "v_and_b32 v210, v162, v210\n"
    "s_waitcnt lgkmcnt(14)\n"
    "v_lshlrev_b32 v212, v164, v212\n"
    "s_waitcnt lgkmcnt(13)\n"
    "v_lshlrev_b32 v214, v164, v214\n"
    "v_and_b32 v213, v161, v212\n"
    "v_and_b32 v212, v160, v212\n"
    "v_and_b32 v215, v163, v214\n"
    "v_and_b32 v214, v162, v214\n"
    "s_waitcnt lgkmcnt(12)\n"
    "v_lshlrev_b32 v216, v164, v216\n"
    "s_waitcnt lgkmcnt(11)\n"
    "v_lshlrev_b32 v218, v164, v218\n"
    "v_and_b32 v217, v161, v216\n"
    "v_and_b32 v216, v160, v216\n"
    "v_and_b32 v219, v163, v218\n"
    "v_and_b32 v218, v162, v218\n"
    "s_waitcnt lgkmcnt(9)\n"
    "v_lshlrev_b32 v220, v164, v220\n"
    "s_waitcnt lgkmcnt(8)\n"
    "v_lshlrev_b32 v222, v164, v222\n"
    "v_and_b32 v221, v161, v220\n"
    "v_and_b32 v220, v160, v220\n"
    "v_and_b32 v223, v163, v222\n"
    "v_and_b32 v222, v162, v222\n"
    "s_waitcnt lgkmcnt(7)\n"
    "v_lshlrev_b32 v224, v164, v224\n"
    "s_waitcnt lgkmcnt(6)\n"
    "v_lshlrev_b32 v226, v164, v226\n"
    "v_and_b32 v225, v161, v224\n"
    "v_and_b32 v224, v160, v224\n"
    "v_and_b32 v227, v163, v226\n"
    "v_and_b32 v226, v162, v226\n"
    "v_lshlrev_b32 v172, 16, v172\n"
    "s_waitcnt lgkmcnt(5)\n"
    "v_lshlrev_b32 v228, 16, v228\n"
    "v_and_b32 v14, v167, v172\n"
    "v_add_f32 v228, v228, v14\n"
    "v_cvt_pk_bf16_f32 v228, v228, v228\n"
    "v_and_b32 v228, 0xffff, v228\n"
    "s_waitcnt lgkmcnt(4)\n"
    "v_lshlrev_b32 v230, 16, v230\n"
    "v_and_b32 v14, v168, v172\n"
    "v_add_f32 v230, v230, v14\n"
    "v_cvt_pk_bf16_f32 v230, v230, v230\n"
    "v_and_b32 v230, 0xffff, v230\n"
    "v_lshlrev_b32 v228, v164, v228\n"
    "v_lshlrev_b32 v230, v164, v230\n"
    "v_and_b32 v229, v161, v228\n"
    "v_and_b32 v228, v160, v228\n"
    "v_and_b32 v231, v163, v230\n"
    "v_and_b32 v230, v162, v230\n"
    "s_waitcnt lgkmcnt(3)\n"
    "v_lshlrev_b32 v232, v164, v232\n"
    "s_waitcnt lgkmcnt(2)\n"
    "v_lshlrev_b32 v234, v164, v234\n"
    "v_and_b32 v233, v161, v232\n"
    "v_and_b32 v232, v160, v232\n"
    "v_and_b32 v235, v163, v234\n"
    "v_and_b32 v234, v162, v234\n"
    "s_waitcnt lgkmcnt(1)\n"
    "v_lshlrev_b32 v236, v164, v236\n"
    "s_waitcnt lgkmcnt(0)\n"
    "v_lshlrev_b32 v238, v164, v238\n"
    "v_and_b32 v237, v161, v236\n"
    "v_and_b32 v236, v160, v236\n"
    "v_and_b32 v239, v163, v238\n"
    "v_and_b32 v238, v162, v238\n"
    "s_cmp_ge_u32 s43, 4\n"
    "s_cbranch_scc1 L_now_%=\n"
    "s_add_u32 m0, s54, 0\n"
    "s_mov_b32 s44, s48\n"
    "buffer_load_dwordx4 v6, s[36:39], s44 offen lds\n"
    "s_add_u32 m0, s54, 1024\n"
    "s_add_u32 s44, s44, s47\n"
    "buffer_load_dwordx4 v6, s[36:39], s44 offen lds\n"
    "s_add_u32 m0, s54, 2048\n"
    "s_add_u32 s44, s44, s47\n"
    "buffer_load_dwordx4 v6, s[36:39], s44 offen lds\n"
    "s_add_u32 s48, s48, s49\n"
    "s_cmp_ge_u32 s48, s50\n"
    "s_cselect_b32 s48, s51, s48\n"
    "s_add_u32 m0, s54, 12288\n"
    "s_mov_b32 s44, s48\n"
    "buffer_load_dwordx4 v6, s[36:39], s44 offen lds\n"
    "s_add_u32 m0, s54, 13312\n"
    "s_add_u32 s44, s44, s47\n"
    "buffer_load_dwordx4 v6, s[36:39], s44 offen lds\n"
    "s_add_u32 m0, s54, 14336\n"
    "s_add_u32 s44, s44, s47\n"
    "buffer_load_dwordx4 v6, s[36:39], s44 offen lds\n"
    "s_add_u32 s48, s48, s49\n"
    "s_cmp_ge_u32 s48, s50\n"
    "s_cselect_b32 s48, s51, s48\n"
    "s_mov_b64 s[76:77], %9\n"
    "s_and_b32 s77, s77, 0xffff\n"
    "s_mov_b32 s78, 16384\n"
    "s_mov_b32 s79, 0x20000\n"
    "s_lshl_b32 s44, s43, 12\n"
    "s_add_u32 m0, s44, 0\n"
    "s_nop 0\n"
    "buffer_load_dwordx4 v3, s[76:79], s44 offen offset:0 lds\n"
    "buffer_load_dwordx4 v3, s[76:79], s44 offen offset:1024 lds\n"
    "buffer_load_dwordx4 v3, s[76:79], s44 offen offset:2048 lds\n"
    "buffer_load_dwordx4 v3, s[76:79], s44 offen offset:3072 lds\n"
    "s_add_u32 m0, s54, 24576\n"
    "s_mov_b32 s44, s48\n"
    "buffer_load_dwordx4 v6, s[36:39], s44 offen lds\n"
    "s_add_u32 m0, s54, 25600\n"
    "s_add_u32 s44, s44, s47\n"
    "buffer_load_dwordx4 v6, s[36:39], s44 offen lds\n"
    "s_add_u32 m0, s54, 26624\n"
    "s_add_u32 s44, s44, s47\n"
    "buffer_load_dwordx4 v6, s[36:39], s44 offen lds\n"
    "s_add_u32 s48, s48, s49\n"
    "s_cmp_ge_u32 s48, s50\n"
    "s_cselect_b32 s48, s51, s48\n"
    "s_waitcnt vmcnt(13) lgkmcnt(0)\n"
    "s_branch L_now2_%=\n"
    "L_now_%=:\n"
    "s_waitcnt vmcnt(0) lgkmcnt(0)\n"
    "L_now2_%=:\n"
    "s_barrier\n"
    "s_cmp_ge_u32 s43, 4\n"
    "s_cbranch_scc1 L_roleX_%=\n"
    "s_setprio 3\n"
    "ds_read_b128 v[64:67], v11 offset:0\n"
    "ds_read_b128 v[68:71], v11 offset:32\n"
    "ds_read_b128 v[72:75], v11 offset:64\n"
    "ds_read_b128 v[76:79], v11 offset:96\n"
    "ds_read_b128 v[80:83], v11 offset:128\n"
    "ds_read_b128 v[84:87], v11 offset:160\n"
    "s_waitcnt lgkmcnt(0)\n"
    "s_barrier\n"
    "ds_read_b64 v[104:105], v7 offset:0\n"
    "ds_read_b64 v[106:107], v7 offset:192\n"
    "ds_read_b64 v[108:109], v7 offset:7056\n"
    "ds_read_b64 v[110:111], v7 offset:7248\n"
    "ds_read_b64 v[112:113], v7 offset:14112\n"
    "ds_read_b64 v[114:115], v7 offset:14304\n"
    "ds_read_b64 v[116:117], v7 offset:21168\n"
    "ds_read_b64 v[118:119], v7 offset:21360\n"
    "ds_read_b64 v[120:121], v7 offset:28224\n"
    "ds_read_b64 v[122:123], v7 offset:28416\n"
    "ds_read_b64 v[124:125], v7 offset:35280\n"
    "ds_read_b64 v[126:127], v7 offset:35472\n"
    "v_lshlrev_b32 v16, 16, v64\n"
    "v_and_b32 v17, 0xffff0000, v64\n"
    "v_lshlrev_b32 v18, 16, v65\n"
    "v_and_b32 v19, 0xffff0000, v65\n"
    "v_lshlrev_b32 v20, 16, v66\n"
    "v_and_b32 v21, 0xffff0000, v66\n"
    "v_lshlrev_b32 v22, 16, v67\n"
    "v_and_b32 v23, 0xffff0000, v67\n"
    "v_lshlrev_b32 v24, 16, v68\n"
    "v_and_b32 v25, 0xffff0000, v68\n"
    "v_lshlrev_b32 v26, 16, v69\n"
    "v_and_b32 v27, 0xffff0000, v69\n"
    "v_lshlrev_b32 v28, 16, v70\n"
    "v_and_b32 v29, 0xffff0000, v70\n"
    "v_lshlrev_b32 v30, 16, v71\n"
    "v_and_b32 v31, 0xffff0000, v71\n"
    "v_lshlrev_b32 v32, 16, v72\n"
    "v_and_b32 v33, 0xffff0000, v72\n"
    "v_lshlrev_b32 v34, 16, v73\n"
    "v_and_b32 v35, 0xffff0000, v73\n"
    "v_lshlrev_b32 v36, 16, v74\n"
    "v_and_b32 v37, 0xffff0000, v74\n"
    "v_lshlrev_b32 v38, 16, v75\n"
    "v_and_b32 v39, 0xffff0000, v75\n"
    "v_lshlrev_b32 v40, 16, v76\n"
    "v_and_b32 v41, 0xffff0000, v76\n"
    "v_lshlrev_b32 v42, 16, v77\n"
    "v_and_b32 v43, 0xffff0000, v77\n"
    "v_lshlrev_b32 v44, 16, v78\n"
    "v_and_b32 v45, 0xffff0000, v78\n"
    "v_lshlrev_b32 v46, 16, v79\n"
    "v_and_b32 v47, 0xffff0000, v79\n"
    "v_lshlrev_b32 v48, 16, v80\n"
    "v_and_b32 v49, 0xffff0000, v80\n"
    "v_lshlrev_b32 v50, 16, v81\n"
    "v_and_b32 v51, 0xffff0000, v81\n"
    "v_lshlrev_b32 v52, 16, v82\n"
    "v_and_b32 v53, 0xffff0000, v82\n"
    "v_lshlrev_b32 v54, 16, v83\n"
    "v_and_b32 v55, 0xffff0000, v83\n"
    "v_lshlrev_b32 v56, 16, v84\n"
    "v_and_b32 v57, 0xffff0000, v84\n"
    "v_lshlrev_b32 v58, 16, v85\n"
    "v_and_b32 v59, 0xffff0000, v85\n"
    "v_lshlrev_b32 v60, 16, v86\n"
    "v_and_b32 v61, 0xffff0000, v86\n"
    "v_lshlrev_b32 v62, 16, v87\n"
    "v_and_b32 v63, 0xffff0000, v87\n"
    "s_waitcnt lgkmcnt(10)\n"
    "v_mfma_f32_16x16x32_bf16 v[88:91], v[180:183], v[104:107], 0\n"
    "ds_read_b64 v[248:249], v7 offset:42336\n"
    "ds_read_b64 v[250:251], v7 offset:42528\n"
    "s_waitcnt lgkmcnt(10)\n"
    "v_mfma_f32_16x16x32_bf16 v[88:91], v[184:187], v[108:111], v[88:91]\n"
    "v_mfma_f32_16x16x32_bf16 v[92:95], v[180:183], v[108:111], 0\n"
    "ds_read_b64 v[252:253], v7 offset:49392\n"
    "ds_read_b64 v[254:255], v7 offset:49584\n"
    "s_waitcnt lgkmcnt(10)\n"
    "v_mfma_f32_16x16x32_bf16 v[88:91], v[188:191], v[112:115], v[88:91]\n"
    "v_mfma_f32_16x16x32_bf16 v[92:95], v[184:187], v[112:115], v[92:95]\n"
    "v_mfma_f32_16x16x32_bf16 v[96:99], v[180:183], v[112:115], 0\n"
    "ds_read_b64 v[136:137], v7 offset:8\n"
    "ds_read_b64 v[138:139], v7 offset:200\n"
    "s_waitcnt lgkmcnt(10)\n"
    "v_mfma_f32_16x16x32_bf16 v[88:91], v[192:195], v[116:119], v[88:91]\n"
    "v_mfma_f32_16x16x32_bf16 v[92:95], v[188:191], v[116:119], v[92:95]\n"
    "v_mfma_f32_16x16x32_bf16 v[96:99], v[184:187], v[116:119], v[96:99]\n"
    "v_mfma_f32_16x16x32_bf16 v[100:103], v[180:183], v[116:119], 0\n"
    "ds_read_b64 v[140:141], v7 offset:7064\n"
    "ds_read_b64 v[142:143], v7 offset:7256\n"
    "s_waitcnt lgkmcnt(10)\n"
    "v_mfma_f32_16x16x32_bf16 v[88:91], v[196:199], v[120:123], v[88:91]\n"
    "v_mfma_f32_16x16x32_bf16 v[92:95], v[192:195], v[120:123], v[92:95]\n"
    "v_mfma_f32_16x16x32_bf16 v[96:99], v[188:191], v[120:123], v[96:99]\n"
    "v_mfma_f32_16x16x32_bf16 v[100:103], v[184:187], v[120:123], v[100:103]\n"
    "ds_read_b64 v[144:145], v7 offset:14120\n"
    "ds_read_b64 v[146:147], v7 offset:14312\n"
    "s_waitcnt lgkmcnt(10)\n"
    "v_mfma_f32_16x16x32_bf16 v[92:95], v[196:199], v[124:127], v[92:95]\n"
    "s_setprio 2\n"
    "v_mfma_f32_16x16x32_bf16 v[96:99], v[192:195], v[124:127], v[96:99]\n"
    "v_mfma_f32_16x16x32_bf16 v[100:103], v[188:191], v[124:127], v[100:103]\n"
    "v_cvt_pk_bf16_f32 v64, v88, v89\n"
    "v_cvt_pk_bf16_f32 v65, v90, v91\n"
    "ds_read_b64 v[148:149], v7 offset:21176\n"
    "ds_read_b64 v[150:151], v7 offset:21368\n"
    "s_waitcnt lgkmcnt(10)\n"
    "v_mfma_f32_16x16x32_bf16 v[96:99], v[196:199], v[248:251], v[96:99]\n"
    "v_mfma_f32_16x16x32_bf16 v[100:103], v[192:195], v[248:251], v[100:103]\n"
    "ds_read_b64 v[152:153], v7 offset:28232\n"
    "ds_read_b64 v[154:155], v7 offset:28424\n"
    "s_waitcnt lgkmcnt(10)\n"
    "v_mfma_f32_16x16x32_bf16 v[100:103], v[196:199], v[252:255], v[100:103]\n"
    "v_cvt_pk_bf16_f32 v66, v92, v93\n"
    "v_cvt_pk_bf16_f32 v67, v94, v95\n"
    "ds_read_b64 v[128:129], v7 offset:35288\n"
    "ds_read_b64 v[130:131], v7 offset:35480\n"
    "s_waitcnt lgkmcnt(10)\n"
    "v_mfma_f32_16x16x32_bf16 v[104:107], v[200:203], v[136:139], 0\n"
    "ds_read_b64 v[132:133], v7 offset:42344\n"
    "ds_read_b64 v[134:135], v7 offset:42536\n"
    "s_waitcnt lgkmcnt(10)\n"
    "v_mfma_f32_16x16x32_bf16 v[104:107], v[204:207], v[140:143], v[104:107]\n"
    "v_cvt_pk_bf16_f32 v68, v96, v97\n"
    "v_cvt_pk_bf16_f32 v69, v98, v99\n"
    "v_mfma_f32_16x16x32_bf16 v[108:111], v[200:203], v[140:143], 0\n"
    "v_cvt_pk_bf16_f32 v70, v100, v101\n"
    "v_cvt_pk_bf16_f32 v71, v102, v103\n"
    "ds_read_b64 v[136:137], v7 offset:49400\n"
    "ds_read_b64 v[138:139], v7 offset:49592\n"
    "s_waitcnt lgkmcnt(10)\n"
    "v_mfma_f32_16x16x32_bf16 v[104:107], v[208:211], v[144:147], v[104:107]\n"
    "v_mfma_f32_16x16x32_bf16 v[108:111], v[204:207], v[144:147], v[108:111]\n"
    "ds_write_b64 v9, v[64:65] offset:0\n"
    "v_mfma_f32_16x16x32_bf16 v[112:115], v[200:203], v[144:147], 0\n"
    "ds_read_b64 v[140:141], v7 offset:16\n"
    "ds_read_b64 v[142:143], v7 offset:208\n"
    "s_waitcnt lgkmcnt(11)\n"
    "v_mfma_f32_16x16x32_bf16 v[104:107], v[212:215], v[148:151], v[104:107]\n"
    "ds_write_b64 v9, v[66:67] offset:7056\n"
    "v_mfma_f32_16x16x32_bf16 v[108:111], v[208:211], v[148:151], v[108:111]\n"
    "v_mfma_f32_16x16x32_bf16 v[112:115], v[204:207], v[148:151], v[112:115]\n"
    "ds_write_b64 v9, v[68:69] offset:14112\n"
    "v_mfma_f32_16x16x32_bf16 v[116:119], v[200:203], v[148:151], 0\n"
    "ds_read_b64 v[144:145], v7 offset:7072\n"
    "ds_read_b64 v[146:147], v7 offset:7264\n"
    "s_setprio 1\n"
    "s_waitcnt lgkmcnt(13)\n"
    "v_mfma_f32_16x16x32_bf16 v[104:107], v[216:219], v[152:155], v[104:107]\n"
    "ds_write_b64 v9, v[70:71] offset:21168\n"
    "v_mfma_f32_16x16x32_bf16 v[108:111], v[212:215], v[152:155], v[108:111]\n"
    "v_mfma_f32_16x16x32_bf16 v[112:115], v[208:211], v[152:155], v[112:115]\n"
    "v_mfma_f32_16x16x32_bf16 v[116:119], v[204:207], v[152:155], v[116:119]\n"
    "ds_read_b64 v[148:149], v7 offset:14128\n"
    "s_waitcnt lgkmcnt(14)\n"
    "ds_read_b64 v[150:151], v7 offset:14320\n"
    "s_waitcnt lgkmcnt(14)\n"
    "v_mfma_f32_16x16x32_bf16 v[108:111], v[216:219], v[128:131], v[108:111]\n"
    "v_mfma_f32_16x16x32_bf16 v[112:115], v[212:215], v[128:131], v[112:115]\n"
    "v_mfma_f32_16x16x32_bf16 v[116:119], v[208:211], v[128:131], v[116:119]\n"
    "v_cvt_pk_bf16_f32 v72, v104, v105\n"
    "v_cvt_pk_bf16_f32 v73, v106, v107\n"
    "ds_read_b64 v[152:153], v7 offset:21184\n"
    "s_waitcnt lgkmcnt(14)\n"
    "ds_read_b64 v[154:155], v7 offset:21376\n"
    "s_waitcnt lgkmcnt(14)\n"
    "v_mfma_f32_16x16x32_bf16 v[112:115], v[216:219], v[132:135], v[112:115]\n"
    "v_mfma_f32_16x16x32_bf16 v[116:119], v[212:215], v[132:135], v[116:119]\n"
    "ds_read_b64 v[128:129], v7 offset:28240\n"
    "s_waitcnt lgkmcnt(14)\n"
    "ds_read_b64 v[130:131], v7 offset:28432\n"
    "s_waitcnt lgkmcnt(14)\n"
    "v_mfma_f32_16x16x32_bf16 v[116:119], v[216:219], v[136:139], v[116:119]\n"
    "v_cvt_pk_bf16_f32 v74, v108, v109\n"
    "v_cvt_pk_bf16_f32 v75, v110, v111\n"
    "ds_read_b64 v[132:133], v7 offset:35296\n"
    "s_waitcnt lgkmcnt(14)\n"
    "ds_read_b64 v[134:135], v7 offset:35488\n"
    "s_waitcnt lgkmcnt(13)\n"
    "v_mfma_f32_16x16x32_bf16 v[88:91], v[220:223], v[140:143], 0\n"
    "ds_read_b64 v[136:137], v7 offset:42352\n"
    "ds_read_b64 v[138:139], v7 offset:42544\n"
    "s_waitcnt lgkmcnt(11)\n"
    "v_mfma_f32_16x16x32_bf16 v[88:91], v[224:227], v[144:147], v[88:91]\n"
    "v_cvt_pk_bf16_f32 v76, v112, v113\n"
    "v_cvt_pk_bf16_f32 v77, v114, v115\n"
    "v_mfma_f32_16x16x32_bf16 v[92:95], v[220:223], v[144:147], 0\n"
    "v_cvt_pk_bf16_f32 v78, v116, v117\n"
    "v_cvt_pk_bf16_f32 v79, v118, v119\n"
    "ds_read_b64 v[140:141], v7 offset:49408\n"
    "ds_read_b64 v[142:143], v7 offset:49600\n"
    "s_waitcnt lgkmcnt(10)\n"
    "v_mfma_f32_16x16x32_bf16 v[88:91], v[228:231], v[148:151], v[88:91]\n"
    "v_mfma_f32_16x16x32_bf16 v[92:95], v[224:227], v[148:151], v[92:95]\n"
    "ds_write_b64 v9, v[72:73] offset:8\n"
    "s_setprio 0\n"
    "v_mfma_f32_16x16x32_bf16 v[96:99], v[220:223], v[148:151], 0\n"
    "s_waitcnt lgkmcnt(9)\n"
    "v_mfma_f32_16x16x32_bf16 v[88:91], v[232:235], v[152:155], v[88:91]\n"
    "ds_write_b64 v9, v[74:75] offset:7064\n"
    "v_mfma_f32_16x16x32_bf16 v[92:95], v[228:231], v[152:155], v[92:95]\n"
    "v_mfma_f32_16x16x32_bf16 v[96:99], v[224:227], v[152:155], v[96:99]\n"
    "ds_write_b64 v9, v[76:77] offset:14120\n"
    "v_mfma_f32_16x16x32_bf16 v[100:103], v[220:223], v[152:155], 0\n"
    "s_waitcnt lgkmcnt(9)\n"
    "v_mfma_f32_16x16x32_bf16 v[88:91], v[236:239], v[128:131], v[88:91]\n"
    "ds_write_b64 v9, v[78:79] offset:21176\n"
    "v_mfma_f32_16x16x32_bf16 v[92:95], v[232:235], v[128:131], v[92:95]\n"
    "v_mfma_f32_16x16x32_bf16 v[96:99], v[228:231], v[128:131], v[96:99]\n"
    "v_mfma_f32_16x16x32_bf16 v[100:103], v[224:227], v[128:131], v[100:103]\n"
    "s_waitcnt lgkmcnt(8)\n"
    "v_mfma_f32_16x16x32_bf16 v[92:95], v[236:239], v[132:135], v[92:95]\n"
    "v_mfma_f32_16x16x32_bf16 v[96:99], v[232:235], v[132:135], v[96:99]\n"
    "v_mfma_f32_16x16x32_bf16 v[100:103], v[228:231], v[132:135], v[100:103]\n"
    "v_cvt_pk_bf16_f32 v80, v88, v89\n"
    "v_cvt_pk_bf16_f32 v81, v90, v91\n"
    "s_waitcnt lgkmcnt(6)\n"
    "v_mfma_f32_16x16x32_bf16 v[96:99], v[236:239], v[136:139], v[96:99]\n"
    "v_mfma_f32_16x16x32_bf16 v[100:103], v[232:235], v[136:139], v[100:103]\n"
    "s_waitcnt lgkmcnt(4)\n"
    "v_mfma_f32_16x16x32_bf16 v[100:103], v[236:239], v[140:143], v[100:103]\n"
    "v_cvt_pk_bf16_f32 v82, v92, v93\n"
    "v_cvt_pk_bf16_f32 v83, v94, v95\n"
    "s_nop 2\n"
    "v_cvt_pk_bf16_f32 v84, v96, v97\n"
    "v_cvt_pk_bf16_f32 v85, v98, v99\n"
    "s_nop 0\n"
    "v_cvt_pk_bf16_f32 v86, v100, v101\n"
    "v_cvt_pk_bf16_f32 v87, v102, v103\n"
    "ds_write_b64 v9, v[80:81] offset:16\n"
    "ds_write_b64 v9, v[82:83] offset:7072\n"
    "ds_write_b64 v9, v[84:85] offset:14128\n"
    "ds_write_b64 v9, v[86:87] offset:21184\n"
    "s_waitcnt vmcnt(0)\n"
    "L_loopW_%=:\n"
    "s_waitcnt lgkmcnt(0)\n"
    "s_barrier\n"
    "ds_read_b128 v[64:67], v11 offset:0\n"
    "ds_read_b128 v[68:71], v11 offset:32\n"
    "ds_read_b128 v[72:75], v11 offset:64\n"
    "ds_read_b128 v[76:79], v11 offset:96\n"
    "ds_read_b128 v[80:83], v11 offset:128\n"
    "ds_read_b128 v[84:87], v11 offset:160\n"
    "s_lshl_b32 s45, s57, 7\n"
    "s_add_u32 s45, s45, s55\n"
    "s_add_u32 s45, s45, s43\n"
    "s_lshl_b32 s45, s45, 9\n"
    "s_add_u32 s45, s45, s56\n"
    "s_mul_i32 s60, s45, 192\n"
    "s_setprio 3\n"
    "v_mov_b32 v13, 0x3f803f80\n"
    "s_waitcnt lgkmcnt(5)\n"
    "v_dot2_f32_bf16 v156, v64, v13, 0\n"
    "v_dot2_f32_bf16 v157, v64, v64, 0\n"
    "v_dot2_f32_bf16 v158, v65, v13, 0\n"
    "v_dot2_f32_bf16 v159, v65, v65, 0\n"
    "v_dot2_f32_bf16 v156, v66, v13, v156\n"
    "v_dot2_f32_bf16 v157, v66, v66, v157\n"
    "v_dot2_f32_bf16 v158, v67, v13, v158\n"
    "v_dot2_f32_bf16 v159, v67, v67, v159\n"
    "s_waitcnt lgkmcnt(4)\n"
    "v_dot2_f32_bf16 v156, v68, v13, v156\n"
    "v_dot2_f32_bf16 v157, v68, v68, v157\n"
    "v_dot2_f32_bf16 v158, v69, v13, v158\n"
    "v_dot2_f32_bf16 v159, v69, v69, v159\n"
    "v_dot2_f32_bf16 v156, v70, v13, v156\n"
    "v_dot2_f32_bf16 v157, v70, v70, v157\n"
    "v_dot2_f32_bf16 v158, v71, v13, v158\n"
    "v_dot2_f32_bf16 v159, v71, v71, v159\n"
    "s_waitcnt lgkmcnt(3)\n"
    "v_dot2_f32_bf16 v156, v72, v13, v156\n"
    "v_dot2_f32_bf16 v157, v72, v72, v157\n"
    "v_dot2_f32_bf16 v158, v73, v13, v158\n"
    "v_dot2_f32_bf16 v159, v73, v73, v159\n"
    "v_dot2_f32_bf16 v156, v74, v13, v156\n"
    "v_dot2_f32_bf16 v157, v74, v74, v157\n"
    "v_dot2_f32_bf16 v158, v75, v13, v158\n"
    "v_dot2_f32_bf16 v159, v75, v75, v159\n"
    "s_waitcnt lgkmcnt(2)\n"
    "v_dot2_f32_bf16 v156, v76, v13, v156\n"
    "v_dot2_f32_bf16 v157, v76, v76, v157\n"
    "v_dot2_f32_bf16 v158, v77, v13, v158\n"
    "v_dot2_f32_bf16 v159, v77, v77, v159\n"
    "v_dot2_f32_bf16 v156, v78, v13, v156\n"
    "v_dot2_f32_bf16 v157, v78, v78, v157\n"
    "v_dot2_f32_bf16 v158, v79, v13, v158\n"
    "v_dot2_f32_bf16 v159, v79, v79, v159\n"
    "s_waitcnt lgkmcnt(1)\n"
    "v_dot2_f32_bf16 v156, v80, v13, v156\n"
    "v_dot2_f32_bf16 v157, v80, v80, v157\n"
    "v_dot2_f32_bf16 v158, v81, v13, v158\n"
    "v_dot2_f32_bf16 v159, v81, v81, v159\n"
    "v_dot2_f32_bf16 v156, v82, v13, v156\n"
    "v_dot2_f32_bf16 v157, v82, v82, v157\n"
    "v_dot2_f32_bf16 v158, v83, v13, v158\n"
    "v_dot2_f32_bf16 v159, v83, v83, v159\n"
    "s_waitcnt lgkmcnt(0)\n"
    "v_dot2_f32_bf16 v156, v84, v13, v156\n"
    "v_dot2_f32_bf16 v157, v84, v84, v157\n"
    "v_dot2_f32_bf16 v158, v85, v13, v158\n"
    "v_dot2_f32_bf16 v159, v85, v85, v159\n"
    "v_dot2_f32_bf16 v156, v86, v13, v156\n"
    "v_dot2_f32_bf16 v157, v86, v86, v157\n"
    "v_dot2_f32_bf16 v158, v87, v13, v158\n"
    "v_dot2_f32_bf16 v159, v87, v87, v159\n"
    "s_setprio 2\n"
    "s_nop 0\n"
    "v_add_f32 v156, v156, v158\n"
    "v_add_f32 v157, v157, v159\n"
    "v_mov_b32 v158, v156\n"
    "v_mov_b32 v159, v157\n"
    "s_nop 0\n"
    "v_permlane32_swap_b32 v156, v158\n"
    "v_permlane32_swap_b32 v157, v159\n"
    "v_add_f32 v156, v156, v158\n"
    "v_add_f32 v157, v157, v159\n"
    "v_mul_f32 v156, s65, v156\n"
    "v_mul_f32 v157, s65, v157\n"
    "v_fma_f32 v157, -v156, v156, v157\n"
    "v_max_f32 v157, 0, v157\n"
    "v_add_u32 v158, -1, v157\n"
    "v_add_f32 v157, s66, v157\n"
    "v_cmp_gt_u32 vcc, s68, v158\n"
    "v_rsq_f32 v160, v157\n"
    "s_nop 0\n"
    "v_mul_f32 v160, s67, v160\n"
    "s_nop 1\n"
    "s_or_b64 s[70:71], s[70:71], vcc\n"
    "v_cvt_pk_bf16_f32 v162, v160, 0\n"
    "v_lshlrev_b32 v163, 16, v162\n"
    "v_mul_f32_e64 v161, -v156, v163\n"
    "ds_read_b64_tr_b16 v[136:137], v1 offset:0\n"
    "ds_read_b64_tr_b16 v[138:139], v1 offset:256\n"
    "ds_read_b64_tr_b16 v[140:141], v1 offset:1024\n"
    "ds_read_b64_tr_b16 v[142:143], v1 offset:1280\n"
    "v_dot2_f32_bf16 v104, v64, v162, v161\n"
    "v_dot2_f32_bf16 v105, v64, v163, v161\n"
    "v_dot2_f32_bf16 v106, v65, v162, v161\n"
    "v_dot2_f32_bf16 v107, v65, v163, v161\n"
    "v_dot2_f32_bf16 v108, v66, v162, v161\n"
    "v_dot2_f32_bf16 v109, v66, v163, v161\n"
    "v_dot2_f32_bf16 v110, v67, v162, v161\n"
    "v_dot2_f32_bf16 v111, v67, v163, v161\n"
    "v_dot2_f32_bf16 v112, v68, v162, v161\n"
    "v_dot2_f32_bf16 v113, v68, v163, v161\n"
    "v_dot2_f32_bf16 v114, v69, v162, v161\n"
    "v_dot2_f32_bf16 v115, v69, v163, v161\n"
    "v_dot2_f32_bf16 v116, v70, v162, v161\n"
    "v_dot2_f32_bf16 v117, v70, v163, v161\n"
    "v_dot2_f32_bf16 v118, v71, v162, v161\n"
    "v_dot2_f32_bf16 v119, v71, v163, v161\n"
    "v_cvt_pk_bf16_f32 v64, v104, v105\n"
    "v_cvt_pk_bf16_f32 v65, v106, v107\n"
    "v_cvt_pk_bf16_f32 v66, v108, v109\n"
    "v_cvt_pk_bf16_f32 v67, v110, v111\n"
    "v_cvt_pk_bf16_f32 v68, v112, v113\n"
    "v_cvt_pk_bf16_f32 v69, v114, v115\n"
    "v_cvt_pk_bf16_f32 v70, v116, v117\n"
    "v_cvt_pk_bf16_f32 v71, v118, v119\n"
    "ds_read_b64_tr_b16 v[144:145], v1 offset:2048\n"
    "ds_read_b64_tr_b16 v[146:147], v1 offset:2304\n"
    "ds_read_b64_tr_b16 v[148:149], v1 offset:3072\n"
    "ds_read_b64_tr_b16 v[150:151], v1 offset:3328\n"
    "s_setprio 1\n"
    "v_dot2_f32_bf16 v120, v72, v162, v161\n"
    "v_dot2_f32_bf16 v121, v72, v163, v161\n"
    "v_dot2_f32_bf16 v122, v73, v162, v161\n"
    "v_dot2_f32_bf16 v123, v73, v163, v161\n"
    "v_dot2_f32_bf16 v124, v74, v162, v161\n"
    "v_dot2_f32_bf16 v125, v74, v163, v161\n"
    "v_dot2_f32_bf16 v126, v75, v162, v161\n"
    "v_dot2_f32_bf16 v127, v75, v163, v161\n"
    "v_dot2_f32_bf16 v128, v76, v162, v161\n"
    "v_dot2_f32_bf16 v129, v76, v163, v161\n"
    "v_dot2_f32_bf16 v130, v77, v162, v161\n"
    "v_dot2_f32_bf16 v131, v77, v163, v161\n"
    "v_dot2_f32_bf16 v132, v78, v162, v161\n"
    "v_dot2_f32_bf16 v133, v78, v163, v161\n"
    "v_dot2_f32_bf16 v134, v79, v162, v161\n"
    "v_dot2_f32_bf16 v135, v79, v163, v161\n"
    "v_cvt_pk_bf16_f32 v72, v120, v121\n"
    "v_cvt_pk_bf16_f32 v73, v122, v123\n"
    "v_cvt_pk_bf16_f32 v74, v124, v125\n"
    "v_cvt_pk_bf16_f32 v75, v126, v127\n"
    "v_cvt_pk_bf16_f32 v76, v128, v129\n"
    "v_cvt_pk_bf16_f32 v77, v130, v131\n"
    "v_cvt_pk_bf16_f32 v78, v132, v133\n"
    "v_cvt_pk_bf16_f32 v79, v134, v135\n"
    "ds_read_b64_tr_b16 v[152:153], v1 offset:4096\n"
    "ds_read_b64_tr_b16 v[154:155], v1 offset:4352\n"
    "ds_read_b64_tr_b16 v[248:249], v1 offset:5120\n"
    "ds_read_b64_tr_b16 v[250:251], v1 offset:5376\n"
    "s_setprio 0\n"
    "v_dot2_f32_bf16 v164, v80, v162, v161\n"
    "v_dot2_f32_bf16 v165, v80, v163, v161\n"
    "v_dot2_f32_bf16 v166, v81, v162, v161\n"
    "v_dot2_f32_bf16 v167, v81, v163, v161\n"
    "v_dot2_f32_bf16 v168, v82, v162, v161\n"
    "v_dot2_f32_bf16 v169, v82, v163, v161\n"
    "v_dot2_f32_bf16 v170, v83, v162, v161\n"
    "v_dot2_f32_bf16 v171, v83, v163, v161\n"
    "v_dot2_f32_bf16 v172, v84, v162, v161\n"
    "v_dot2_f32_bf16 v173, v84, v163, v161\n"
    "v_dot2_f32_bf16 v174, v85, v162, v161\n"
    "v_dot2_f32_bf16 v175, v85, v163, v161\n"
    "v_dot2_f32_bf16 v176, v86, v162, v161\n"
    "v_dot2_f32_bf16 v177, v86, v163, v161\n"
    "v_dot2_f32_bf16 v178, v87, v162, v161\n"
    "v_dot2_f32_bf16 v179, v87, v163, v161\n"
    "v_cvt_pk_bf16_f32 v80, v164, v165\n"
    "v_cvt_pk_bf16_f32 v81, v166, v167\n"
    "v_cvt_pk_bf16_f32 v82, v168, v169\n"
    "v_cvt_pk_bf16_f32 v83, v170, v171\n"
    "v_cvt_pk_bf16_f32 v84, v172, v173\n"
    "v_cvt_pk_bf16_f32 v85, v174, v175\n"
    "v_cvt_pk_bf16_f32 v86, v176, v177\n"
    "v_cvt_pk_bf16_f32 v87, v178, v179\n"
    "s_waitcnt lgkmcnt(10)\n"
    "v_mfma_f32_32x32x16_bf16 v[88:103], v[136:139], v[64:67], 0\n"
    "s_waitcnt lgkmcnt(8)\n"
    "v_mfma_f32_32x32x16_bf16 v[88:103], v[140:143], v[68:71], v[88:103]\n"
    "s_waitcnt lgkmcnt(6)\n"
    "v_mfma_f32_32x32x16_bf16 v[88:103], v[144:147], v[72:75], v[88:103]\n"
    "s_waitcnt lgkmcnt(4)\n"
    "v_mfma_f32_32x32x16_bf16 v[88:103], v[148:151], v[76:79], v[88:103]\n"
    "s_waitcnt lgkmcnt(2)\n"
    "v_mfma_f32_32x32x16_bf16 v[88:103], v[152:155], v[80:83], v[88:103]\n"
    "s_waitcnt lgkmcnt(0)\n"
    "v_mfma_f32_32x32x16_bf16 v[88:103], v[248:251], v[84:87], v[88:103]\n"
    "ds_read_b64_tr_b16 v[136:137], v1 offset:12288\n"
    "ds_read_b64_tr_b16 v[138:139], v1 offset:12544\n"
    "ds_read_b64_tr_b16 v[140:141], v1 offset:13312\n"
    "ds_read_b64_tr_b16 v[142:143], v1 offset:13568\n"
    "ds_read_b64_tr_b16 v[144:145], v1 offset:14336\n"
    "ds_read_b64_tr_b16 v[146:147], v1 offset:14592\n"
    "s_setprio 3\n"
    "s_nop 4\n"
    "v_cvt_pk_f16_f32 v165, v94, v95\n"
    "v_cvt_pk_f16_f32 v167, v96, v97\n"
    "v_cvt_pk_f16_f32 v169, v98, v99\n"
    "v_cvt_pk_f16_f32 v171, v100, v101\n"
    "v_cvt_pk_f16_f32 v173, v102, v103\n"
    "s_add_u32 m0, s54, 36864\n"
    "s_mov_b32 s44, s48\n"
    "buffer_load_dwordx4 v6, s[36:39], s44 offen lds\n"
    "s_add_u32 m0, s54, 37888\n"
    "s_add_u32 s44, s44, s47\n"
    "buffer_load_dwordx4 v6, s[36:39], s44 offen lds\n"
    "s_add_u32 m0, s54, 38912\n"
    "s_add_u32 s44, s44, s47\n"
    "buffer_load_dwordx4 v6, s[36:39], s44 offen lds\n"
    "s_add_u32 s48, s48, s49\n"
    "s_cmp_ge_u32 s48, s50\n"
    "s_cselect_b32 s48, s51, s48\n"
    "v_and_b32_e32 v165, s42, v165\n"
    "v_and_b32_e32 v167, s42, v167\n"
    "v_and_b32_e32 v169, s42, v169\n"
    "v_and_b32_e32 v171, s42, v171\n"
    "v_and_b32_e32 v173, s42, v173\n"
    "v_lshrrev_b32_sdwa v164, 2, v165 dst_sel:DWORD dst_unused:UNUSED_PAD src0_sel:DWORD src1_sel:WORD_0\n"
    "v_lshrrev_b32_sdwa v166, 2, v167 dst_sel:DWORD dst_unused:UNUSED_PAD src0_sel:DWORD src1_sel:WORD_0\n"
    "s_waitcnt lgkmcnt(4)\n"
    "v_mfma_f32_32x32x16_bf16 v[104:119], v[136:139], v[64:67], 0\n"
    "ds_read_b64_tr_b16 v[148:149], v1 offset:15360\n"
    "ds_read_b64_tr_b16 v[150:151], v1 offset:15616\n"
    "v_lshrrev_b32_sdwa v168, 2, v169 dst_sel:DWORD dst_unused:UNUSED_PAD src0_sel:DWORD src1_sel:WORD_0\n"
    "v_lshrrev_b32_sdwa v170, 2, v171 dst_sel:DWORD dst_unused:UNUSED_PAD src0_sel:DWORD src1_sel:WORD_0\n"
    "v_lshrrev_b32_sdwa v172, 2, v173 dst_sel:DWORD dst_unused:UNUSED_PAD src0_sel:DWORD src1_sel:WORD_0\n"
    "v_lshrrev_b32_e32 v165, 18, v165\n"
    "v_lshrrev_b32_e32 v167, 18, v167\n"
    "v_lshrrev_b32_e32 v169, 18, v169\n"
    "s_setprio 2\n"
    "v_lshrrev_b32_e32 v171, 18, v171\n"
    "v_lshrrev_b32_e32 v173, 18, v173\n"
    "v_cvt_pk_f16_f32 v156, v88, v89\n"
    "v_cvt_pk_f16_f32 v158, v90, v91\n"
    "s_waitcnt lgkmcnt(4)\n"
    "v_mfma_f32_32x32x16_bf16 v[104:119], v[140:143], v[68:71], v[104:119]\n"
    "ds_read_b64_tr_b16 v[152:153], v1 offset:16384\n"
    "ds_read_b64_tr_b16 v[154:155], v1 offset:16640\n"
    "v_cvt_pk_f16_f32 v160, v92, v93\n"
    "v_pk_fma_f16 v157, v156, v156, s40\n"
    "v_pk_fma_f16 v159, v158, v158, s40\n"
    "ds_read_u16 v164, v164 offset:0\n"
    "v_pk_fma_f16 v161, v160, v160, s40\n"
    "v_pk_mul_f16 v157, v157, v156 neg_lo:[1,0] neg_hi:[1,0]\n"
    "v_pk_mul_f16 v159, v159, v158 neg_lo:[1,0] neg_hi:[1,0]\n"
    "v_pk_mul_f16 v161, v161, v160 neg_lo:[1,0] neg_hi:[1,0]\n"
    "ds_read_u16_d16_hi v165, v165 offset:0\n"
    "v_exp_f16_sdwa v157, v157 dst_sel:WORD_0 dst_unused:UNUSED_PRESERVE src0_sel:WORD_0\n"
    "v_exp_f16_sdwa v159, v159 dst_sel:WORD_0 dst_unused:UNUSED_PRESERVE src0_sel:WORD_0\n"
    "s_waitcnt lgkmcnt(6)\n"
    "v_mfma_f32_32x32x16_bf16 v[104:119], v[144:147], v[72:75], v[104:119]\n"
    "ds_read_u16 v166, v166 offset:0\n"
    "ds_read_b64_tr_b16 v[136:137], v1 offset:17408\n"
    "ds_read_b64_tr_b16 v[138:139], v1 offset:17664\n"
    "v_exp_f16_sdwa v161, v161 dst_sel:WORD_0 dst_unused:UNUSED_PRESERVE src0_sel:WORD_0\n"
    "s_setprio 1\n"
    "v_exp_f16_sdwa v157, v157 dst_sel:WORD_1 dst_unused:UNUSED_PRESERVE src0_sel:WORD_1\n"
    "ds_read_u16_d16_hi v167, v167 offset:0\n"
    "v_exp_f16_sdwa v159, v159 dst_sel:WORD_1 dst_unused:UNUSED_PRESERVE src0_sel:WORD_1\n"
    "v_exp_f16_sdwa v161, v161 dst_sel:WORD_1 dst_unused:UNUSED_PRESERVE src0_sel:WORD_1\n"
    "ds_read_u16 v168, v168 offset:0\n"
    "v_pk_fma_f16 v157, v157, s41, s41\n"
    "v_pk_fma_f16 v159, v159, s41, s41\n"
    "s_waitcnt lgkmcnt(9)\n"
    "v_mfma_f32_32x32x16_bf16 v[104:119], v[148:151], v[76:79], v[104:119]\n"
    "v_pk_fma_f16 v161, v161, s41, s41\n"
    "v_rcp_f16_sdwa v157, v157 dst_sel:WORD_0 dst_unused:UNUSED_PRESERVE src0_sel:WORD_0\n"
    "ds_read_u16_d16_hi v169, v169 offset:0\n"
    "v_rcp_f16_sdwa v159, v159 dst_sel:WORD_0 dst_unused:UNUSED_PRESERVE src0_sel:WORD_0\n"
    "v_rcp_f16_sdwa v161, v161 dst_sel:WORD_0 dst_unused:UNUSED_PRESERVE src0_sel:WORD_0\n"
    "ds_read_u16 v170, v170 offset:0\n"
    "v_rcp_f16_sdwa v157, v157 dst_sel:WORD_1 dst_unused:UNUSED_PRESERVE src0_sel:WORD_1\n"
    "v_rcp_f16_sdwa v159, v159 dst_sel:WORD_1 dst_unused:UNUSED_PRESERVE src0_sel:WORD_1\n"
    "s_setprio 0\n"
    "ds_read_u16_d16_hi v171, v171 offset:0\n"
    "s_waitcnt lgkmcnt(10)\n"
    "v_mfma_f32_32x32x16_bf16 v[104:119], v[152:155], v[80:83], v[104:119]\n"
    "v_rcp_f16_sdwa v161, v161 dst_sel:WORD_1 dst_unused:UNUSED_PRESERVE src0_sel:WORD_1\n"
    "v_fma_mix_f32 v88, v88, v157, 0 op_sel:[0,0,0] op_sel_hi:[0,1,0]\n"
    "ds_read_u16 v172, v172 offset:0\n"
    "v_fma_mix_f32 v90, v90, v159, 0 op_sel:[0,0,0] op_sel_hi:[0,1,0]\n"
    "v_fma_mix_f32 v92, v92, v161, 0 op_sel:[0,0,0] op_sel_hi:[0,1,0]\n"
    "v_fma_mix_f32 v89, v89, v157, 0 op_sel:[0,1,0] op_sel_hi:[0,1,0]\n"
    "v_fma_mix_f32 v91, v91, v159, 0 op_sel:[0,1,0] op_sel_hi:[0,1,0]\n"
    "ds_read_u16_d16_hi v173, v173 offset:0\n"
    "v_fma_mix_f32 v93, v93, v161, 0 op_sel:[0,1,0] op_sel_hi:[0,1,0]\n"
    "v_cvt_pk_bf16_f32 v120, v88, v89\n"
    "v_cvt_pk_bf16_f32 v121, v90, v91\n"
    "v_cvt_pk_bf16_f32 v122, v92, v93\n"
    "s_waitcnt lgkmcnt(7)\n"
    "v_mfma_f32_32x32x16_bf16 v[104:119], v[136:139], v[84:87], v[104:119]\n"
    "v_or_b32_e32 v123, v164, v165\n"
    "s_waitcnt lgkmcnt(6)\n"
    "v_or_b32_e32 v124, v166, v167\n"
    "s_waitcnt lgkmcnt(4)\n"
    "v_or_b32_e32 v125, v168, v169\n"
    "s_waitcnt lgkmcnt(2)\n"
    "v_or_b32_e32 v126, v170, v171\n"
    "s_waitcnt lgkmcnt(0)\n"
    "v_or_b32_e32 v127, v172, v173\n"
    "s_waitcnt vmcnt(3)\n"
    "s_barrier\n"
    "ds_read_b64_tr_b16 v[136:137], v1 offset:24576\n"
    "ds_read_b64_tr_b16 v[138:139], v1 offset:24832\n"
    "ds_read_b64_tr_b16 v[140:141], v2 offset:24576\n"
    "ds_read_b64_tr_b16 v[142:143], v2 offset:26112\n"
    "ds_read_b64_tr_b16 v[144:145], v1 offset:25600\n"
    "ds_read_b64_tr_b16 v[146:147], v1 offset:25856\n"
    "s_setprio 3\n"
    "v_cvt_pk_f16_f32 v165, v110, v111\n"
    "v_cvt_pk_f16_f32 v167, v112, v113\n"
    "v_cvt_pk_f16_f32 v169, v114, v115\n"
    "v_cvt_pk_f16_f32 v171, v116, v117\n"
    "v_cvt_pk_f16_f32 v173, v118, v119\n"
    "s_add_u32 m0, s54, 0\n"
    "s_mov_b32 s44, s48\n"
    "buffer_load_dwordx4 v6, s[36:39], s44 offen lds\n"
    "s_add_u32 m0, s54, 1024\n"
    "s_add_u32 s44, s44, s47\n"
    "buffer_load_dwordx4 v6, s[36:39], s44 offen lds\n"
    "s_add_u32 m0, s54, 2048\n"
    "s_add_u32 s44, s44, s47\n"
    "buffer_load_dwordx4 v6, s[36:39], s44 offen lds\n"
    "s_add_u32 s48, s48, s49\n"
    "s_cmp_ge_u32 s48, s50\n"
    "s_cselect_b32 s48, s51, s48\n"
    "v_and_b32_e32 v165, s42, v165\n"
    "v_and_b32_e32 v167, s42, v167\n"
    "v_and_b32_e32 v169, s42, v169\n"
    "v_and_b32_e32 v171, s42, v171\n"
    "v_and_b32_e32 v173, s42, v173\n"
    "v_lshrrev_b32_sdwa v164, 2, v165 dst_sel:DWORD dst_unused:UNUSED_PAD src0_sel:DWORD src1_sel:WORD_0\n"
    "v_lshrrev_b32_sdwa v166, 2, v167 dst_sel:DWORD dst_unused:UNUSED_PAD src0_sel:DWORD src1_sel:WORD_0\n"
    "s_waitcnt lgkmcnt(4)\n"
    "v_mfma_f32_32x32x16_bf16 v[88:103], v[136:139], v[64:67], 0\n"
    "ds_read_b64_tr_b16 v[148:149], v2 offset:24640\n"
    "ds_read_b64_tr_b16 v[150:151], v2 offset:26176\n"
    "v_lshrrev_b32_sdwa v168, 2, v169 dst_sel:DWORD dst_unused:UNUSED_PAD src0_sel:DWORD src1_sel:WORD_0\n"
    "v_lshrrev_b32_sdwa v170, 2, v171 dst_sel:DWORD dst_unused:UNUSED_PAD src0_sel:DWORD src1_sel:WORD_0\n"
    "v_lshrrev_b32_sdwa v172, 2, v173 dst_sel:DWORD dst_unused:UNUSED_PAD src0_sel:DWORD src1_sel:WORD_0\n"
    "v_lshrrev_b32_e32 v165, 18, v165\n"
    "s_waitcnt lgkmcnt(4)\n"
    "v_mfma_f32_32x32x16_bf16 v[16:31], v[140:143], v[120:123], v[16:31]\n"
    "ds_read_b64_tr_b16 v[152:153], v1 offset:26624\n"
    "ds_read_b64_tr_b16 v[154:155], v1 offset:26880\n"
    "v_lshrrev_b32_e32 v167, 18, v167\n"
    "v_lshrrev_b32_e32 v169, 18, v169\n"
    "s_setprio 2\n"
    "v_lshrrev_b32_e32 v171, 18, v171\n"
    "v_lshrrev_b32_e32 v173, 18, v173\n"
    "v_cvt_pk_f16_f32 v156, v104, v105\n"
    "s_waitcnt lgkmcnt(4)\n"
    "v_mfma_f32_32x32x16_bf16 v[88:103], v[144:147], v[68:71], v[88:103]\n"
    "ds_read_b64_tr_b16 v[136:137], v2 offset:24704\n"
    "ds_read_b64_tr_b16 v[138:139], v2 offset:26240\n"
    "v_cvt_pk_f16_f32 v158, v106, v107\n"
    "v_cvt_pk_f16_f32 v160, v108, v109\n"
    "v_pk_fma_f16 v157, v156, v156, s40\n"
    "v_pk_fma_f16 v159, v158, v158, s40\n"
    "ds_read_u16 v164, v164 offset:0\n"
    "v_pk_fma_f16 v161, v160, v160, s40\n"
    "s_waitcnt lgkmcnt(5)\n"
    "v_mfma_f32_32x32x16_bf16 v[32:47], v[148:151], v[120:123], v[32:47]\n"
    "ds_read_b64_tr_b16 v[140:141], v1 offset:27648\n"
    "ds_read_b64_tr_b16 v[142:143], v1 offset:27904\n"
    "v_pk_mul_f16 v157, v157, v156 neg_lo:[1,0] neg_hi:[1,0]\n"
    "v_pk_mul_f16 v159, v159, v158 neg_lo:[1,0] neg_hi:[1,0]\n"
    "v_pk_mul_f16 v161, v161, v160 neg_lo:[1,0] neg_hi:[1,0]\n"
    "ds_read_u16_d16_hi v165, v165 offset:0\n"
    "v_exp_f16_sdwa v157, v157 dst_sel:WORD_0 dst_unused:UNUSED_PRESERVE src0_sel:WORD_0\n"
    "s_waitcnt lgkmcnt(6)\n"
    "v_mfma_f32_32x32x16_bf16 v[88:103], v[152:155], v[72:75], v[88:103]\n"
    "ds_read_b64_tr_b16 v[144:145], v2 offset:27648\n"
    "ds_read_b64_tr_b16 v[146:147], v2 offset:29184\n"
    "v_exp_f16_sdwa v159, v159 dst_sel:WORD_0 dst_unused:UNUSED_PRESERVE src0_sel:WORD_0\n"
    "ds_read_u16 v166, v166 offset:0\n"
    "v_exp_f16_sdwa v161, v161 dst_sel:WORD_0 dst_unused:UNUSED_PRESERVE src0_sel:WORD_0\n"
    "s_setprio 1\n"
    "v_exp_f16_sdwa v157, v157 dst_sel:WORD_1 dst_unused:UNUSED_PRESERVE src0_sel:WORD_1\n"
    "s_waitcnt lgkmcnt(7)\n"
    "v_mfma_f32_32x32x16_bf16 v[48:63], v[136:139], v[120:123], v[48:63]\n"
    "ds_read_b64_tr_b16 v[148:149], v1 offset:28672\n"
    "ds_read_b64_tr_b16 v[150:151], v1 offset:28928\n"
    "ds_read_u16_d16_hi v167, v167 offset:0\n"
    "v_exp_f16_sdwa v159, v159 dst_sel:WORD_1 dst_unused:UNUSED_PRESERVE src0_sel:WORD_1\n"
    "v_exp_f16_sdwa v161, v161 dst_sel:WORD_1 dst_unused:UNUSED_PRESERVE src0_sel:WORD_1\n"
    "s_waitcnt lgkmcnt(7)\n"
    "v_mfma_f32_32x32x16_bf16 v[88:103], v[140:143], v[76:79], v[88:103]\n"
    "ds_read_b64_tr_b16 v[152:153], v2 offset:27712\n"
    "ds_read_b64_tr_b16 v[154:155], v2 offset:29248\n"
    "ds_read_u16 v168, v168 offset:0\n"
    "v_pk_fma_f16 v157, v157, s41, s41\n"
    "v_pk_fma_f16 v159, v159, s41, s41\n"
    "v_pk_fma_f16 v161, v161, s41, s41\n"
    "v_rcp_f16_sdwa v157, v157 dst_sel:WORD_0 dst_unused:UNUSED_PRESERVE src0_sel:WORD_0\n"
    "ds_read_u16_d16_hi v169, v169 offset:0\n"
    "s_waitcnt lgkmcnt(8)\n"
    "v_mfma_f32_32x32x16_bf16 v[16:31], v[144:147], v[124:127], v[16:31]\n"
    "ds_read_b64_tr_b16 v[136:137], v1 offset:29696\n"
    "ds_read_b64_tr_b16 v[138:139], v1 offset:29952\n"
    "v_rcp_f16_sdwa v159, v159 dst_sel:WORD_0 dst_unused:UNUSED_PRESERVE src0_sel:WORD_0\n"
    "v_rcp_f16_sdwa v161, v161 dst_sel:WORD_0 dst_unused:UNUSED_PRESERVE src0_sel:WORD_0\n"
    "ds_read_u16 v170, v170 offset:0\n"
    "s_waitcnt lgkmcnt(8)\n"
    "v_mfma_f32_32x32x16_bf16 v[88:103], v[148:151], v[80:83], v[88:103]\n"
    "ds_read_b64_tr_b16 v[140:141], v2 offset:27776\n"
    "ds_read_b64_tr_b16 v[142:143], v2 offset:29312\n"
    "v_rcp_f16_sdwa v157, v157 dst_sel:WORD_1 dst_unused:UNUSED_PRESERVE src0_sel:WORD_1\n"
    "v_rcp_f16_sdwa v159, v159 dst_sel:WORD_1 dst_unused:UNUSED_PRESERVE src0_sel:WORD_1\n"
    "s_setprio 0\n"
    "ds_read_u16_d16_hi v171, v171 offset:0\n"
    "v_rcp_f16_sdwa v161, v161 dst_sel:WORD_1 dst_unused:UNUSED_PRESERVE src0_sel:WORD_1\n"
    "s_waitcnt lgkmcnt(8)\n"
    "v_mfma_f32_32x32x16_bf16 v[32:47], v[152:155], v[124:127], v[32:47]\n"
    "v_fma_mix_f32 v104, v104, v157, 0 op_sel:[0,0,0] op_sel_hi:[0,1,0]\n"
    "ds_read_u16 v172, v172 offset:0\n"
    "v_fma_mix_f32 v106, v106, v159, 0 op_sel:[0,0,0] op_sel_hi:[0,1,0]\n"
    "v_fma_mix_f32 v108, v108, v161, 0 op_sel:[0,0,0] op_sel_hi:[0,1,0]\n"
    "v_fma_mix_f32 v105, v105, v157, 0 op_sel:[0,1,0] op_sel_hi:[0,1,0]\n"
    "s_waitcnt lgkmcnt(5)\n"
    "v_mfma_f32_32x32x16_bf16 v[88:103], v[136:139], v[84:87], v[88:103]\n"
    "v_fma_mix_f32 v107, v107, v159, 0 op_sel:[0,1,0] op_sel_hi:[0,1,0]\n"
    "ds_read_u16_d16_hi v173, v173 offset:0\n"
    "v_fma_mix_f32 v109, v109, v161, 0 op_sel:[0,1,0] op_sel_hi:[0,1,0]\n"
    "v_cvt_pk_bf16_f32 v128, v104, v105\n"
    "v_cvt_pk_bf16_f32 v129, v106, v107\n"
    "v_cvt_pk_bf16_f32 v130, v108, v109\n"
    "s_waitcnt lgkmcnt(3)\n"
    "v_mfma_f32_32x32x16_bf16 v[48:63], v[140:143], v[124:127], v[48:63]\n"
    "v_or_b32_e32 v131, v164, v165\n"
    "v_or_b32_e32 v132, v166, v167\n"
    "v_or_b32_e32 v133, v168, v169\n"
    "s_waitcnt lgkmcnt(2)\n"
    "v_or_b32_e32 v134, v170, v171\n"
    "s_waitcnt lgkmcnt(0)\n"
    "v_or_b32_e32 v135, v172, v173\n"
    "s_waitcnt vmcnt(3)\n"
    "s_barrier\n"
    "ds_read_b64_tr_b16 v[136:137], v1 offset:36864\n"
    "ds_read_b64_tr_b16 v[138:139], v1 offset:37120\n"
    "ds_read_b64_tr_b16 v[140:141], v2 offset:36864\n"
    "ds_read_b64_tr_b16 v[142:143], v2 offset:38400\n"
    "ds_read_b64_tr_b16 v[144:145], v1 offset:37888\n"
    "ds_read_b64_tr_b16 v[146:147], v1 offset:38144\n"
    "s_setprio 3\n"
    "v_cvt_pk_f16_f32 v165, v94, v95\n"
    "v_cvt_pk_f16_f32 v167, v96, v97\n"
    "v_cvt_pk_f16_f32 v169, v98, v99\n"
    "v_cvt_pk_f16_f32 v171, v100, v101\n"
    "v_cvt_pk_f16_f32 v173, v102, v103\n"
    "s_add_u32 m0, s54, 12288\n"
    "s_mov_b32 s44, s48\n"
    "buffer_load_dwordx4 v6, s[36:39], s44 offen lds\n"
    "s_add_u32 m0, s54, 13312\n"
    "s_add_u32 s44, s44, s47\n"
    "buffer_load_dwordx4 v6, s[36:39], s44 offen lds\n"
    "s_add_u32 m0, s54, 14336\n"
    "s_add_u32 s44, s44, s47\n"
    "buffer_load_dwordx4 v6, s[36:39], s44 offen lds\n"
    "s_add_u32 s48, s48, s49\n"
    "s_cmp_ge_u32 s48, s50\n"
    "s_cselect_b32 s48, s51, s48\n"
    "v_and_b32_e32 v165, s42, v165\n"
    "v_and_b32_e32 v167, s42, v167\n"
    "v_and_b32_e32 v169, s42, v169\n"
    "v_and_b32_e32 v171, s42, v171\n"
    "v_and_b32_e32 v173, s42, v173\n"
    "v_lshrrev_b32_sdwa v164, 2, v165 dst_sel:DWORD dst_unused:UNUSED_PAD src0_sel:DWORD src1_sel:WORD_0\n"
    "v_lshrrev_b32_sdwa v166, 2, v167 dst_sel:DWORD dst_unused:UNUSED_PAD src0_sel:DWORD src1_sel:WORD_0\n"
    "s_waitcnt lgkmcnt(4)\n"
    "v_mfma_f32_32x32x16_bf16 v[104:119], v[136:139], v[64:67], 0\n"
    "ds_read_b64_tr_b16 v[148:149], v2 offset:36928\n"
    "ds_read_b64_tr_b16 v[150:151], v2 offset:38464\n"
    "v_lshrrev_b32_sdwa v168, 2, v169 dst_sel:DWORD dst_unused:UNUSED_PAD src0_sel:DWORD src1_sel:WORD_0\n"
    "v_lshrrev_b32_sdwa v170, 2, v171 dst_sel:DWORD dst_unused:UNUSED_PAD src0_sel:DWORD src1_sel:WORD_0\n"
    "v_lshrrev_b32_sdwa v172, 2, v173 dst_sel:DWORD dst_unused:UNUSED_PAD src0_sel:DWORD src1_sel:WORD_0\n"
    "v_lshrrev_b32_e32 v165, 18, v165\n"
    "s_waitcnt lgkmcnt(4)\n"
    "v_mfma_f32_32x32x16_bf16 v[16:31], v[140:143], v[128:131], v[16:31]\n"
    "ds_read_b64_tr_b16 v[152:153], v1 offset:38912\n"
    "ds_read_b64_tr_b16 v[154:155], v1 offset:39168\n"
    "v_lshrrev_b32_e32 v167, 18, v167\n"
    "v_lshrrev_b32_e32 v169, 18, v169\n"
    "s_setprio 2\n"
    "v_lshrrev_b32_e32 v171, 18, v171\n"
    "v_lshrrev_b32_e32 v173, 18, v173\n"
    "v_cvt_pk_f16_f32 v156, v88, v89\n"
    "s_waitcnt lgkmcnt(4)\n"
    "v_mfma_f32_32x32x16_bf16 v[104:119], v[144:147], v[68:71], v[104:119]\n"
    "ds_read_b64_tr_b16 v[136:137], v2 offset:36992\n"
    "ds_read_b64_tr_b16 v[138:139], v2 offset:38528\n"
    "v_cvt_pk_f16_f32 v158, v90, v91\n"
    "v_cvt_pk_f16_f32 v160, v92, v93\n"
    "v_pk_fma_f16 v157, v156, v156, s40\n"
    "v_pk_fma_f16 v159, v158, v158, s40\n"
    "ds_read_u16 v164, v164 offset:0\n"
    "v_pk_fma_f16 v161, v160, v160, s40\n"
    "s_waitcnt lgkmcnt(5)\n"
    "v_mfma_f32_32x32x16_bf16 v[32:47], v[148:151], v[128:131], v[32:47]\n"
    "ds_read_b64_tr_b16 v[140:141], v1 offset:39936\n"
    "ds_read_b64_tr_b16 v[142:143], v1 offset:40192\n"
    "v_pk_mul_f16 v157, v157, v156 neg_lo:[1,0] neg_hi:[1,0]\n"
    "v_pk_mul_f16 v159, v159, v158 neg_lo:[1,0] neg_hi:[1,0]\n"
    "v_pk_mul_f16 v161, v161, v160 neg_lo:[1,0] neg_hi:[1,0]\n"
    "ds_read_u16_d16_hi v165, v165 offset:0\n"
    "v_exp_f16_sdwa v157, v157 dst_sel:WORD_0 dst_unused:UNUSED_PRESERVE src0_sel:WORD_0\n"
    "s_waitcnt lgkmcnt(6)\n"
    "v_mfma_f32_32x32x16_bf16 v[104:119], v[152:155], v[72:75], v[104:119]\n"
    "ds_read_b64_tr_b16 v[144:145], v2 offset:39936\n"
    "ds_read_b64_tr_b16 v[146:147], v2 offset:41472\n"
    "v_exp_f16_sdwa v159, v159 dst_sel:WORD_0 dst_unused:UNUSED_PRESERVE src0_sel:WORD_0\n"
    "ds_read_u16 v166, v166 offset:0\n"
    "v_exp_f16_sdwa v161, v161 dst_sel:WORD_0 dst_unused:UNUSED_PRESERVE src0_sel:WORD_0\n"
    "s_setprio 1\n"
    "v_exp_f16_sdwa v157, v157 dst_sel:WORD_1 dst_unused:UNUSED_PRESERVE src0_sel:WORD_1\n"
    "s_waitcnt lgkmcnt(7)\n"
    "v_mfma_f32_32x32x16_bf16 v[48:63], v[136:139], v[128:131], v[48:63]\n"
    "ds_read_b64_tr_b16 v[148:149], v1 offset:40960\n"
    "ds_read_b64_tr_b16 v[150:151], v1 offset:41216\n"
    "ds_read_u16_d16_hi v167, v167 offset:0\n"
    "v_exp_f16_sdwa v159, v159 dst_sel:WORD_1 dst_unused:UNUSED_PRESERVE src0_sel:WORD_1\n"
    "v_exp_f16_sdwa v161, v161 dst_sel:WORD_1 dst_unused:UNUSED_PRESERVE src0_sel:WORD_1\n"
    "s_waitcnt lgkmcnt(7)\n"
    "v_mfma_f32_32x32x16_bf16 v[104:119], v[140:143], v[76:79], v[104:119]\n"
    "ds_read_b64_tr_b16 v[152:153], v2 offset:40000\n"
    "ds_read_b64_tr_b16 v[154:155], v2 offset:41536\n"
    "ds_read_u16 v168, v168 offset:0\n"
    "v_pk_fma_f16 v157, v157, s41, s41\n"
    "v_pk_fma_f16 v159, v159, s41, s41\n"
    "v_pk_fma_f16 v161, v161, s41, s41\n"
    "v_rcp_f16_sdwa v157, v157 dst_sel:WORD_0 dst_unused:UNUSED_PRESERVE src0_sel:WORD_0\n"
    "ds_read_u16_d16_hi v169, v169 offset:0\n"
    "s_waitcnt lgkmcnt(8)\n"
    "v_mfma_f32_32x32x16_bf16 v[16:31], v[144:147], v[132:135], v[16:31]\n"
    "ds_read_b64_tr_b16 v[136:137], v1 offset:41984\n"
    "ds_read_b64_tr_b16 v[138:139], v1 offset:42240\n"
    "v_rcp_f16_sdwa v159, v159 dst_sel:WORD_0 dst_unused:UNUSED_PRESERVE src0_sel:WORD_0\n"
    "v_rcp_f16_sdwa v161, v161 dst_sel:WORD_0 dst_unused:UNUSED_PRESERVE src0_sel:WORD_0\n"
    "ds_read_u16 v170, v170 offset:0\n"
    "s_waitcnt lgkmcnt(8)\n"
    "v_mfma_f32_32x32x16_bf16 v[104:119], v[148:151], v[80:83], v[104:119]\n"
    "ds_read_b64_tr_b16 v[140:141], v2 offset:40064\n"
    "ds_read_b64_tr_b16 v[142:143], v2 offset:41600\n"
    "v_rcp_f16_sdwa v157, v157 dst_sel:WORD_1 dst_unused:UNUSED_PRESERVE src0_sel:WORD_1\n"
    "v_rcp_f16_sdwa v159, v159 dst_sel:WORD_1 dst_unused:UNUSED_PRESERVE src0_sel:WORD_1\n"
    "s_setprio 0\n"
    "ds_read_u16_d16_hi v171, v171 offset:0\n"
    "v_rcp_f16_sdwa v161, v161 dst_sel:WORD_1 dst_unused:UNUSED_PRESERVE src0_sel:WORD_1\n"
    "s_waitcnt lgkmcnt(8)\n"
    "v_mfma_f32_32x32x16_bf16 v[32:47], v[152:155], v[132:135], v[32:47]\n"
    "v_fma_mix_f32 v88, v88, v157, 0 op_sel:[0,0,0] op_sel_hi:[0,1,0]\n"
    "ds_read_u16 v172, v172 offset:0\n"
    "v_fma_mix_f32 v90, v90, v159, 0 op_sel:[0,0,0] op_sel_hi:[0,1,0]\n"
    "v_fma_mix_f32 v92, v92, v161, 0 op_sel:[0,0,0] op_sel_hi:[0,1,0]\n"
    "v_fma_mix_f32 v89, v89, v157, 0 op_sel:[0,1,0] op_sel_hi:[0,1,0]\n"
    "s_waitcnt lgkmcnt(5)\n"
    "v_mfma_f32_32x32x16_bf16 v[104:119], v[136:139], v[84:87], v[104:119]\n"
    "v_fma_mix_f32 v91, v91, v159, 0 op_sel:[0,1,0] op_sel_hi:[0,1,0]\n"
    "ds_read_u16_d16_hi v173, v173 offset:0\n"
    "v_fma_mix_f32 v93, v93, v161, 0 op_sel:[0,1,0] op_sel_hi:[0,1,0]\n"
    "v_cvt_pk_bf16_f32 v120, v88, v89\n"
    "v_cvt_pk_bf16_f32 v121, v90, v91\n"
    "v_cvt_pk_bf16_f32 v122, v92, v93\n"
    "s_waitcnt lgkmcnt(3)\n"
    "v_mfma_f32_32x32x16_bf16 v[48:63], v[140:143], v[132:135], v[48:63]\n"
    "v_or_b32_e32 v123, v164, v165\n"
    "v_or_b32_e32 v124, v166, v167\n"
    "v_or_b32_e32 v125, v168, v169\n"
    "s_waitcnt lgkmcnt(2)\n"
    "v_or_b32_e32 v126, v170, v171\n"
    "s_waitcnt lgkmcnt(0)\n"
    "v_or_b32_e32 v127, v172, v173\n"
    "s_waitcnt vmcnt(3)\n"
    "s_barrier\n"
    "ds_read_b64_tr_b16 v[136:137], v1 offset:0\n"
    "ds_read_b64_tr_b16 v[138:139], v1 offset:256\n"
    "ds_read_b64_tr_b16 v[140:141], v2 offset:0\n"
    "ds_read_b64_tr_b16 v[142:143], v2 offset:1536\n"
    "ds_read_b64_tr_b16 v[144:145], v1 offset:1024\n"
    "ds_read_b64_tr_b16 v[146:147], v1 offset:1280\n"
    "s_setprio 3\n"
    "v_cvt_pk_f16_f32 v165, v110, v111\n"
    "v_cvt_pk_f16_f32 v167, v112, v113\n"
    "v_cvt_pk_f16_f32 v169, v114, v115\n"
    "v_cvt_pk_f16_f32 v171, v116, v117\n"
    "v_cvt_pk_f16_f32 v173, v118, v119\n"
    "s_add_u32 m0, s54, 24576\n"
    "s_mov_b32 s44, s48\n"
    "buffer_load_dwordx4 v6, s[36:39], s44 offen lds\n"
    "s_add_u32 m0, s54, 25600\n"
    "s_add_u32 s44, s44, s47\n"
    "buffer_load_dwordx4 v6, s[36:39], s44 offen lds\n"
    "s_add_u32 m0, s54, 26624\n"
    "s_add_u32 s44, s44, s47\n"
    "buffer_load_dwordx4 v6, s[36:39], s44 offen lds\n"
    "s_add_u32 s48, s48, s49\n"
    "s_cmp_ge_u32 s48, s50\n"
    "s_cselect_b32 s48, s51, s48\n"
    "v_and_b32_e32 v165, s42, v165\n"
    "v_and_b32_e32 v167, s42, v167\n"
    "v_and_b32_e32 v169, s42, v169\n"
    "v_and_b32_e32 v171, s42, v171\n"
    "v_and_b32_e32 v173, s42, v173\n"
    "v_lshrrev_b32_sdwa v164, 2, v165 dst_sel:DWORD dst_unused:UNUSED_PAD src0_sel:DWORD src1_sel:WORD_0\n"
    "v_lshrrev_b32_sdwa v166, 2, v167 dst_sel:DWORD dst_unused:UNUSED_PAD src0_sel:DWORD src1_sel:WORD_0\n"
    "s_waitcnt lgkmcnt(4)\n"
    "v_mfma_f32_32x32x16_bf16 v[88:103], v[136:139], v[64:67], 0\n"
    "ds_read_b64_tr_b16 v[148:149], v2 offset:64\n"
    "ds_read_b64_tr_b16 v[150:151], v2 offset:1600\n"
    "v_lshrrev_b32_sdwa v168, 2, v169 dst_sel:DWORD dst_unused:UNUSED_PAD src0_sel:DWORD src1_sel:WORD_0\n"
    "v_lshrrev_b32_sdwa v170, 2, v171 dst_sel:DWORD dst_unused:UNUSED_PAD src0_sel:DWORD src1_sel:WORD_0\n"
    "v_lshrrev_b32_sdwa v172, 2, v173 dst_sel:DWORD dst_unused:UNUSED_PAD src0_sel:DWORD src1_sel:WORD_0\n"
    "v_lshrrev_b32_e32 v165, 18, v165\n"
    "s_waitcnt lgkmcnt(4)\n"
    "v_mfma_f32_32x32x16_bf16 v[16:31], v[140:143], v[120:123], v[16:31]\n"
    "ds_read_b64_tr_b16 v[152:153], v1 offset:2048\n"
    "ds_read_b64_tr_b16 v[154:155], v1 offset:2304\n"
    "v_lshrrev_b32_e32 v167, 18, v167\n"
    "v_lshrrev_b32_e32 v169, 18, v169\n"
    "s_setprio 2\n"
    "v_lshrrev_b32_e32 v171, 18, v171\n"
    "v_lshrrev_b32_e32 v173, 18, v173\n"
    "v_cvt_pk_f16_f32 v156, v104, v105\n"
    "s_waitcnt lgkmcnt(4)\n"
    "v_mfma_f32_32x32x16_bf16 v[88:103], v[144:147], v[68:71], v[88:103]\n"
    "ds_read_b64_tr_b16 v[136:137], v2 offset:128\n"
    "ds_read_b64_tr_b16 v[138:139], v2 offset:1664\n"
    "v_cvt_pk_f16_f32 v158, v106, v107\n"
    "v_cvt_pk_f16_f32 v160, v108, v109\n"
    "v_pk_fma_f16 v157, v156, v156, s40\n"
    "v_pk_fma_f16 v159, v158, v158, s40\n"
    "ds_read_u16 v164, v164 offset:0\n"
    "v_pk_fma_f16 v161, v160, v160, s40\n"
    "s_waitcnt lgkmcnt(5)\n"
    "v_mfma_f32_32x32x16_bf16 v[32:47], v[148:151], v[120:123], v[32:47]\n"
    "ds_read_b64_tr_b16 v[140:141], v1 offset:3072\n"
    "ds_read_b64_tr_b16 v[142:143], v1 offset:3328\n"
    "v_pk_mul_f16 v157, v157, v156 neg_lo:[1,0] neg_hi:[1,0]\n"
    "v_pk_mul_f16 v159, v159, v158 neg_lo:[1,0] neg_hi:[1,0]\n"
    "v_pk_mul_f16 v161, v161, v160 neg_lo:[1,0] neg_hi:[1,0]\n"
    "ds_read_u16_d16_hi v165, v165 offset:0\n"
    "v_exp_f16_sdwa v157, v157 dst_sel:WORD_0 dst_unused:UNUSED_PRESERVE src0_sel:WORD_0\n"
    "s_waitcnt lgkmcnt(6)\n"
    "v_mfma_f32_32x32x16_bf16 v[88:103], v[152:155], v[72:75], v[88:103]\n"
    "ds_read_b64_tr_b16 v[144:145], v2 offset:3072\n"
    "ds_read_b64_tr_b16 v[146:147], v2 offset:4608\n"
    "v_exp_f16_sdwa v159, v159 dst_sel:WORD_0 dst_unused:UNUSED_PRESERVE src0_sel:WORD_0\n"
    "ds_read_u16 v166, v166 offset:0\n"
    "v_exp_f16_sdwa v161, v161 dst_sel:WORD_0 dst_unused:UNUSED_PRESERVE src0_sel:WORD_0\n"
    "s_setprio 1\n"
    "v_exp_f16_sdwa v157, v157 dst_sel:WORD_1 dst_unused:UNUSED_PRESERVE src0_sel:WORD_1\n"
    "s_waitcnt lgkmcnt(7)\n"
    "v_mfma_f32_32x32x16_bf16 v[48:63], v[136:139], v[120:123], v[48:63]\n"
    "ds_read_b64_tr_b16 v[148:149], v1 offset:4096\n"
    "ds_read_b64_tr_b16 v[150:151], v1 offset:4352\n"
    "ds_read_u16_d16_hi v167, v167 offset:0\n"
    "v_exp_f16_sdwa v159, v159 dst_sel:WORD_1 dst_unused:UNUSED_PRESERVE src0_sel:WORD_1\n"
    "v_exp_f16_sdwa v161, v161 dst_sel:WORD_1 dst_unused:UNUSED_PRESERVE src0_sel:WORD_1\n"
    "s_waitcnt lgkmcnt(7)\n"
    "v_mfma_f32_32x32x16_bf16 v[88:103], v[140:143], v[76:79], v[88:103]\n"
    "ds_read_b64_tr_b16 v[152:153], v2 offset:3136\n"
    "ds_read_b64_tr_b16 v[154:155], v2 offset:4672\n"
    "ds_read_u16 v168, v168 offset:0\n"
    "v_pk_fma_f16 v157, v157, s41, s41\n"
    "v_pk_fma_f16 v159, v159, s41, s41\n"
    "v_pk_fma_f16 v161, v161, s41, s41\n"
    "v_rcp_f16_sdwa v157, v157 dst_sel:WORD_0 dst_unused:UNUSED_PRESERVE src0_sel:WORD_0\n"
    "ds_read_u16_d16_hi v169, v169 offset:0\n"
    "s_waitcnt lgkmcnt(8)\n"
    "v_mfma_f32_32x32x16_bf16 v[16:31], v[144:147], v[124:127], v[16:31]\n"
    "ds_read_b64_tr_b16 v[136:137], v1 offset:5120\n"
    "ds_read_b64_tr_b16 v[138:139], v1 offset:5376\n"
    "v_rcp_f16_sdwa v159, v159 dst_sel:WORD_0 dst_unused:UNUSED_PRESERVE src0_sel:WORD_0\n"
    "v_rcp_f16_sdwa v161, v161 dst_sel:WORD_0 dst_unused:UNUSED_PRESERVE src0_sel:WORD_0\n"
    "ds_read_u16 v170, v170 offset:0\n"
    "s_waitcnt lgkmcnt(8)\n"
    "v_mfma_f32_32x32x16_bf16 v[88:103], v[148:151], v[80:83], v[88:103]\n"
    "ds_read_b64_tr_b16 v[140:141], v2 offset:3200\n"
    "ds_read_b64_tr_b16 v[142:143], v2 offset:4736\n"
    "v_rcp_f16_sdwa v157, v157 dst_sel:WORD_1 dst_unused:UNUSED_PRESERVE src0_sel:WORD_1\n"
    "v_rcp_f16_sdwa v159, v159 dst_sel:WORD_1 dst_unused:UNUSED_PRESERVE src0_sel:WORD_1\n"
    "s_setprio 0\n"
    "ds_read_u16_d16_hi v171, v171 offset:0\n"
    "v_rcp_f16_sdwa v161, v161 dst_sel:WORD_1 dst_unused:UNUSED_PRESERVE src0_sel:WORD_1\n"
    "s_waitcnt lgkmcnt(8)\n"
    "v_mfma_f32_32x32x16_bf16 v[32:47], v[152:155], v[124:127], v[32:47]\n"
    "v_fma_mix_f32 v104, v104, v157, 0 op_sel:[0,0,0] op_sel_hi:[0,1,0]\n"
    "ds_read_u16 v172, v172 offset:0\n"
    "v_fma_mix_f32 v106, v106, v159, 0 op_sel:[0,0,0] op_sel_hi:[0,1,0]\n"
    "v_fma_mix_f32 v108, v108, v161, 0 op_sel:[0,0,0] op_sel_hi:[0,1,0]\n"
    "v_fma_mix_f32 v105, v105, v157, 0 op_sel:[0,1,0] op_sel_hi:[0,1,0]\n"
    "s_waitcnt lgkmcnt(5)\n"
    "v_mfma_f32_32x32x16_bf16 v[88:103], v[136:139], v[84:87], v[88:103]\n"
    "v_fma_mix_f32 v107, v107, v159, 0 op_sel:[0,1,0] op_sel_hi:[0,1,0]\n"
    "ds_read_u16_d16_hi v173, v173 offset:0\n"
    "v_fma_mix_f32 v109, v109, v161, 0 op_sel:[0,1,0] op_sel_hi:[0,1,0]\n"
    "v_cvt_pk_bf16_f32 v128, v104, v105\n"
    "v_cvt_pk_bf16_f32 v129, v106, v107\n"
    "v_cvt_pk_bf16_f32 v130, v108, v109\n"
    "s_waitcnt lgkmcnt(3)\n"
    "v_mfma_f32_32x32x16_bf16 v[48:63], v[140:143], v[124:127], v[48:63]\n"
    "v_or_b32_e32 v131, v164, v165\n"
    "v_or_b32_e32 v132, v166, v167\n"
    "v_or_b32_e32 v133, v168, v169\n"
    "s_waitcnt lgkmcnt(2)\n"
    "v_or_b32_e32 v134, v170, v171\n"
    "s_waitcnt lgkmcnt(0)\n"
    "v_or_b32_e32 v135, v172, v173\n"
    "s_waitcnt vmcnt(3)\n"
    "s_barrier\n"
    "ds_read_b64_tr_b16 v[136:137], v1 offset:12288\n"
    "ds_read_b64_tr_b16 v[138:139], v1 offset:12544\n"
    "ds_read_b64_tr_b16 v[140:141], v2 offset:12288\n"
    "ds_read_b64_tr_b16 v[142:143], v2 offset:13824\n"
    "ds_read_b64_tr_b16 v[144:145], v1 offset:13312\n"
    "ds_read_b64_tr_b16 v[146:147], v1 offset:13568\n"
    "s_setprio 3\n"
    "v_cvt_pk_f16_f32 v165, v94, v95\n"
    "v_cvt_pk_f16_f32 v167, v96, v97\n"
    "v_cvt_pk_f16_f32 v169, v98, v99\n"
    "v_cvt_pk_f16_f32 v171, v100, v101\n"
    "v_cvt_pk_f16_f32 v173, v102, v103\n"
    "s_add_u32 m0, s54, 36864\n"
    "s_mov_b32 s44, s48\n"
    "buffer_load_dwordx4 v6, s[36:39], s44 offen lds\n"
    "s_add_u32 m0, s54, 37888\n"
    "s_add_u32 s44, s44, s47\n"
    "buffer_load_dwordx4 v6, s[36:39], s44 offen lds\n"
    "s_add_u32 m0, s54, 38912\n"
    "s_add_u32 s44, s44, s47\n"
    "buffer_load_dwordx4 v6, s[36:39], s44 offen lds\n"
    "s_add_u32 s48, s48, s49\n"
    "s_cmp_ge_u32 s48, s50\n"
    "s_cselect_b32 s48, s51, s48\n"
    "v_and_b32_e32 v165, s42, v165\n"
    "v_and_b32_e32 v167, s42, v167\n"
    "v_and_b32_e32 v169, s42, v169\n"
    "v_and_b32_e32 v171, s42, v171\n"
    "v_and_b32_e32 v173, s42, v173\n"
    "v_lshrrev_b32_sdwa v164, 2, v165 dst_sel:DWORD dst_unused:UNUSED_PAD src0_sel:DWORD src1_sel:WORD_0\n"
    "v_lshrrev_b32_sdwa v166, 2, v167 dst_sel:DWORD dst_unused:UNUSED_PAD src0_sel:DWORD src1_sel:WORD_0\n"
    "s_waitcnt lgkmcnt(4)\n"
    "v_mfma_f32_32x32x16_bf16 v[104:119], v[136:139], v[64:67], 0\n"
    "ds_read_b64_tr_b16 v[148:149], v2 offset:12352\n"
    "ds_read_b64_tr_b16 v[150:151], v2 offset:13888\n"
    "v_lshrrev_b32_sdwa v168, 2, v169 dst_sel:DWORD dst_unused:UNUSED_PAD src0_sel:DWORD src1_sel:WORD_0\n"
    "v_lshrrev_b32_sdwa v170, 2, v171 dst_sel:DWORD dst_unused:UNUSED_PAD src0_sel:DWORD src1_sel:WORD_0\n"
    "v_lshrrev_b32_sdwa v172, 2, v173 dst_sel:DWORD dst_unused:UNUSED_PAD src0_sel:DWORD src1_sel:WORD_0\n"
    "v_lshrrev_b32_e32 v165, 18, v165\n"
    "s_waitcnt lgkmcnt(4)\n"
    "v_mfma_f32_32x32x16_bf16 v[16:31], v[140:143], v[128:131], v[16:31]\n"
    "ds_read_b64_tr_b16 v[152:153], v1 offset:14336\n"
    "ds_read_b64_tr_b16 v[154:155], v1 offset:14592\n"
    "v_lshrrev_b32_e32 v167, 18, v167\n"
    "v_lshrrev_b32_e32 v169, 18, v169\n"
    "s_setprio 2\n"
    "v_lshrrev_b32_e32 v171, 18, v171\n"
    "v_lshrrev_b32_e32 v173, 18, v173\n"
    "v_cvt_pk_f16_f32 v156, v88, v89\n"
    "s_waitcnt lgkmcnt(4)\n"
    "v_mfma_f32_32x32x16_bf16 v[104:119], v[144:147], v[68:71], v[104:119]\n"
    "ds_read_b64_tr_b16 v[136:137], v2 offset:12416\n"
    "ds_read_b64_tr_b16 v[138:139], v2 offset:13952\n"
    "v_cvt_pk_f16_f32 v158, v90, v91\n"
    "v_cvt_pk_f16_f32 v160, v92, v93\n"
    "v_pk_fma_f16 v157, v156, v156, s40\n"
    "v_pk_fma_f16 v159, v158, v158, s40\n"
    "ds_read_u16 v164, v164 offset:0\n"
    "v_pk_fma_f16 v161, v160, v160, s40\n"
    "s_waitcnt lgkmcnt(5)\n"
    "v_mfma_f32_32x32x16_bf16 v[32:47], v[148:151], v[128:131], v[32:47]\n"
    "ds_read_b64_tr_b16 v[140:141], v1 offset:15360\n"
    "ds_read_b64_tr_b16 v[142:143], v1 offset:15616\n"
    "v_pk_mul_f16 v157, v157, v156 neg_lo:[1,0] neg_hi:[1,0]\n"
    "v_pk_mul_f16 v159, v159, v158 neg_lo:[1,0] neg_hi:[1,0]\n"
    "v_pk_mul_f16 v161, v161, v160 neg_lo:[1,0] neg_hi:[1,0]\n"
    "ds_read_u16_d16_hi v165, v165 offset:0\n"
    "v_exp_f16_sdwa v157, v157 dst_sel:WORD_0 dst_unused:UNUSED_PRESERVE src0_sel:WORD_0\n"
    "s_waitcnt lgkmcnt(6)\n"
    "v_mfma_f32_32x32x16_bf16 v[104:119], v[152:155], v[72:75], v[104:119]\n"
    "ds_read_b64_tr_b16 v[144:145], v2 offset:15360\n"
    "ds_read_b64_tr_b16 v[146:147], v2 offset:16896\n"
    "v_exp_f16_sdwa v159, v159 dst_sel:WORD_0 dst_unused:UNUSED_PRESERVE src0_sel:WORD_0\n"
    "ds_read_u16 v166, v166 offset:0\n"
    "v_exp_f16_sdwa v161, v161 dst_sel:WORD_0 dst_unused:UNUSED_PRESERVE src0_sel:WORD_0\n"
    "s_setprio 1\n"
    "v_exp_f16_sdwa v157, v157 dst_sel:WORD_1 dst_unused:UNUSED_PRESERVE src0_sel:WORD_1\n"
    "s_waitcnt lgkmcnt(7)\n"
    "v_mfma_f32_32x32x16_bf16 v[48:63], v[136:139], v[128:131], v[48:63]\n"
    "ds_read_b64_tr_b16 v[148:149], v1 offset:16384\n"
    "ds_read_b64_tr_b16 v[150:151], v1 offset:16640\n"
    "ds_read_u16_d16_hi v167, v167 offset:0\n"
    "v_exp_f16_sdwa v159, v159 dst_sel:WORD_1 dst_unused:UNUSED_PRESERVE src0_sel:WORD_1\n"
    "v_exp_f16_sdwa v161, v161 dst_sel:WORD_1 dst_unused:UNUSED_PRESERVE src0_sel:WORD_1\n"
    "s_waitcnt lgkmcnt(7)\n"
    "v_mfma_f32_32x32x16_bf16 v[104:119], v[140:143], v[76:79], v[104:119]\n"
    "ds_read_b64_tr_b16 v[152:153], v2 offset:15424\n"
    "ds_read_b64_tr_b16 v[154:155], v2 offset:16960\n"
    "ds_read_u16 v168, v168 offset:0\n"
    "v_pk_fma_f16 v157, v157, s41, s41\n"
    "v_pk_fma_f16 v159, v159, s41, s41\n"
    "v_pk_fma_f16 v161, v161, s41, s41\n"
    "v_rcp_f16_sdwa v157, v157 dst_sel:WORD_0 dst_unused:UNUSED_PRESERVE src0_sel:WORD_0\n"
    "ds_read_u16_d16_hi v169, v169 offset:0\n"
    "s_waitcnt lgkmcnt(8)\n"
    "v_mfma_f32_32x32x16_bf16 v[16:31], v[144:147], v[132:135], v[16:31]\n"
    "ds_read_b64_tr_b16 v[136:137], v1 offset:17408\n"
    "ds_read_b64_tr_b16 v[138:139], v1 offset:17664\n"
    "v_rcp_f16_sdwa v159, v159 dst_sel:WORD_0 dst_unused:UNUSED_PRESERVE src0_sel:WORD_0\n"
    "v_rcp_f16_sdwa v161, v161 dst_sel:WORD_0 dst_unused:UNUSED_PRESERVE src0_sel:WORD_0\n"
    "ds_read_u16 v170, v170 offset:0\n"
    "s_waitcnt lgkmcnt(8)\n"
    "v_mfma_f32_32x32x16_bf16 v[104:119], v[148:151], v[80:83], v[104:119]\n"
    "ds_read_b64_tr_b16 v[140:141], v2 offset:15488\n"
    "ds_read_b64_tr_b16 v[142:143], v2 offset:17024\n"
    "v_rcp_f16_sdwa v157, v157 dst_sel:WORD_1 dst_unused:UNUSED_PRESERVE src0_sel:WORD_1\n"
    "v_rcp_f16_sdwa v159, v159 dst_sel:WORD_1 dst_unused:UNUSED_PRESERVE src0_sel:WORD_1\n"
    "s_setprio 0\n"
    "ds_read_u16_d16_hi v171, v171 offset:0\n"
    "v_rcp_f16_sdwa v161, v161 dst_sel:WORD_1 dst_unused:UNUSED_PRESERVE src0_sel:WORD_1\n"
    "s_waitcnt lgkmcnt(8)\n"
    "v_mfma_f32_32x32x16_bf16 v[32:47], v[152:155], v[132:135], v[32:47]\n"
    "v_fma_mix_f32 v88, v88, v157, 0 op_sel:[0,0,0] op_sel_hi:[0,1,0]\n"
    "ds_read_u16 v172, v172 offset:0\n"
    "v_fma_mix_f32 v90, v90, v159, 0 op_sel:[0,0,0] op_sel_hi:[0,1,0]\n"
    "v_fma_mix_f32 v92, v92, v161, 0 op_sel:[0,0,0] op_sel_hi:[0,1,0]\n"
    "v_fma_mix_f32 v89, v89, v157, 0 op_sel:[0,1,0] op_sel_hi:[0,1,0]\n"
    "s_waitcnt lgkmcnt(5)\n"
    "v_mfma_f32_32x32x16_bf16 v[104:119], v[136:139], v[84:87], v[104:119]\n"
    "v_fma_mix_f32 v91, v91, v159, 0 op_sel:[0,1,0] op_sel_hi:[0,1,0]\n"
    "ds_read_u16_d16_hi v173, v173 offset:0\n"
    "v_fma_mix_f32 v93, v93, v161, 0 op_sel:[0,1,0] op_sel_hi:[0,1,0]\n"
    "v_cvt_pk_bf16_f32 v120, v88, v89\n"
    "v_cvt_pk_bf16_f32 v121, v90, v91\n"
    "v_cvt_pk_bf16_f32 v122, v92, v93\n"
    "s_waitcnt lgkmcnt(3)\n"
    "v_mfma_f32_32x32x16_bf16 v[48:63], v[140:143], v[132:135], v[48:63]\n"
    "v_or_b32_e32 v123, v164, v165\n"
    "v_or_b32_e32 v124, v166, v167\n"
    "v_or_b32_e32 v125, v168, v169\n"
    "s_waitcnt lgkmcnt(2)\n"
    "v_or_b32_e32 v126, v170, v171\n"
    "s_waitcnt lgkmcnt(0)\n"
    "v_or_b32_e32 v127, v172, v173\n"
    "s_waitcnt vmcnt(3)\n"
    "s_barrier\n"
    "ds_read_b64_tr_b16 v[136:137], v1 offset:24576\n"
    "ds_read_b64_tr_b16 v[138:139], v1 offset:24832\n"
    "ds_read_b64_tr_b16 v[140:141], v2 offset:24576\n"
    "ds_read_b64_tr_b16 v[142:143], v2 offset:26112\n"
    "ds_read_b64_tr_b16 v[144:145], v1 offset:25600\n"
    "ds_read_b64_tr_b16 v[146:147], v1 offset:25856\n"
    "s_setprio 3\n"
    "v_cvt_pk_f16_f32 v165, v110, v111\n"
    "v_cvt_pk_f16_f32 v167, v112, v113\n"
    "v_cvt_pk_f16_f32 v169, v114, v115\n"
    "v_cvt_pk_f16_f32 v171, v116, v117\n"
    "v_cvt_pk_f16_f32 v173, v118, v119\n"
    "s_add_u32 m0, s54, 0\n"
    "s_mov_b32 s44, s48\n"
    "buffer_load_dwordx4 v6, s[36:39], s44 offen lds\n"
    "s_add_u32 m0, s54, 1024\n"
    "s_add_u32 s44, s44, s47\n"
    "buffer_load_dwordx4 v6, s[36:39], s44 offen lds\n"
    "s_add_u32 m0, s54, 2048\n"
    "s_add_u32 s44, s44, s47\n"
    "buffer_load_dwordx4 v6, s[36:39], s44 offen lds\n"
    "s_add_u32 s48, s48, s49\n"
    "s_cmp_ge_u32 s48, s50\n"
    "s_cselect_b32 s48, s51, s48\n"
    "v_and_b32_e32 v165, s42, v165\n"
    "v_and_b32_e32 v167, s42, v167\n"
    "v_and_b32_e32 v169, s42, v169\n"
    "v_and_b32_e32 v171, s42, v171\n"
    "v_and_b32_e32 v173, s42, v173\n"
    "v_lshrrev_b32_sdwa v164, 2, v165 dst_sel:DWORD dst_unused:UNUSED_PAD src0_sel:DWORD src1_sel:WORD_0\n"
    "v_lshrrev_b32_sdwa v166, 2, v167 dst_sel:DWORD dst_unused:UNUSED_PAD src0_sel:DWORD src1_sel:WORD_0\n"
    "s_waitcnt lgkmcnt(4)\n"
    "v_mfma_f32_32x32x16_bf16 v[88:103], v[136:139], v[64:67], 0\n"
    "ds_read_b64_tr_b16 v[148:149], v2 offset:24640\n"
    "ds_read_b64_tr_b16 v[150:151], v2 offset:26176\n"
    "v_lshrrev_b32_sdwa v168, 2, v169 dst_sel:DWORD dst_unused:UNUSED_PAD src0_sel:DWORD src1_sel:WORD_0\n"
    "v_lshrrev_b32_sdwa v170, 2, v171 dst_sel:DWORD dst_unused:UNUSED_PAD src0_sel:DWORD src1_sel:WORD_0\n"
    "v_lshrrev_b32_sdwa v172, 2, v173 dst_sel:DWORD dst_unused:UNUSED_PAD src0_sel:DWORD src1_sel:WORD_0\n"
    "v_lshrrev_b32_e32 v165, 18, v165\n"
    "s_waitcnt lgkmcnt(4)\n"
    "v_mfma_f32_32x32x16_bf16 v[16:31], v[140:143], v[120:123], v[16:31]\n"
    "ds_read_b64_tr_b16 v[152:153], v1 offset:26624\n"
    "ds_read_b64_tr_b16 v[154:155], v1 offset:26880\n"
    "v_lshrrev_b32_e32 v167, 18, v167\n"
    "v_lshrrev_b32_e32 v169, 18, v169\n"
    "s_setprio 2\n"
    "v_lshrrev_b32_e32 v171, 18, v171\n"
    "v_lshrrev_b32_e32 v173, 18, v173\n"
    "v_cvt_pk_f16_f32 v156, v104, v105\n"
    "s_waitcnt lgkmcnt(4)\n"
    "v_mfma_f32_32x32x16_bf16 v[88:103], v[144:147], v[68:71], v[88:103]\n"
    "ds_read_b64_tr_b16 v[136:137], v2 offset:24704\n"
    "ds_read_b64_tr_b16 v[138:139], v2 offset:26240\n"
    "v_cvt_pk_f16_f32 v158, v106, v107\n"
    "v_cvt_pk_f16_f32 v160, v108, v109\n"
    "v_pk_fma_f16 v157, v156, v156, s40\n"
    "v_pk_fma_f16 v159, v158, v158, s40\n"
    "ds_read_u16 v164, v164 offset:0\n"
    "v_pk_fma_f16 v161, v160, v160, s40\n"
    "s_waitcnt lgkmcnt(5)\n"
    "v_mfma_f32_32x32x16_bf16 v[32:47], v[148:151], v[120:123], v[32:47]\n"
    "ds_read_b64_tr_b16 v[140:141], v1 offset:27648\n"
    "ds_read_b64_tr_b16 v[142:143], v1 offset:27904\n"
    "v_pk_mul_f16 v157, v157, v156 neg_lo:[1,0] neg_hi:[1,0]\n"
    "v_pk_mul_f16 v159, v159, v158 neg_lo:[1,0] neg_hi:[1,0]\n"
    "v_pk_mul_f16 v161, v161, v160 neg_lo:[1,0] neg_hi:[1,0]\n"
    "ds_read_u16_d16_hi v165, v165 offset:0\n"
    "v_exp_f16_sdwa v157, v157 dst_sel:WORD_0 dst_unused:UNUSED_PRESERVE src0_sel:WORD_0\n"
    "s_waitcnt lgkmcnt(6)\n"
    "v_mfma_f32_32x32x16_bf16 v[88:103], v[152:155], v[72:75], v[88:103]\n"
    "ds_read_b64_tr_b16 v[144:145], v2 offset:27648\n"
    "ds_read_b64_tr_b16 v[146:147], v2 offset:29184\n"
    "v_exp_f16_sdwa v159, v159 dst_sel:WORD_0 dst_unused:UNUSED_PRESERVE src0_sel:WORD_0\n"
    "ds_read_u16 v166, v166 offset:0\n"
    "v_exp_f16_sdwa v161, v161 dst_sel:WORD_0 dst_unused:UNUSED_PRESERVE src0_sel:WORD_0\n"
    "s_setprio 1\n"
    "v_exp_f16_sdwa v157, v157 dst_sel:WORD_1 dst_unused:UNUSED_PRESERVE src0_sel:WORD_1\n"
    "s_waitcnt lgkmcnt(7)\n"
    "v_mfma_f32_32x32x16_bf16 v[48:63], v[136:139], v[120:123], v[48:63]\n"
    "ds_read_b64_tr_b16 v[148:149], v1 offset:28672\n"
    "ds_read_b64_tr_b16 v[150:151], v1 offset:28928\n"
    "ds_read_u16_d16_hi v167, v167 offset:0\n"
    "v_exp_f16_sdwa v159, v159 dst_sel:WORD_1 dst_unused:UNUSED_PRESERVE src0_sel:WORD_1\n"
    "v_exp_f16_sdwa v161, v161 dst_sel:WORD_1 dst_unused:UNUSED_PRESERVE src0_sel:WORD_1\n"
    "s_waitcnt lgkmcnt(7)\n"
    "v_mfma_f32_32x32x16_bf16 v[88:103], v[140:143], v[76:79], v[88:103]\n"
    "ds_read_b64_tr_b16 v[152:153], v2 offset:27712\n"
    "ds_read_b64_tr_b16 v[154:155], v2 offset:29248\n"
    "ds_read_u16 v168, v168 offset:0\n"
    "v_pk_fma_f16 v157, v157, s41, s41\n"
    "v_pk_fma_f16 v159, v159, s41, s41\n"
    "v_pk_fma_f16 v161, v161, s41, s41\n"
    "v_rcp_f16_sdwa v157, v157 dst_sel:WORD_0 dst_unused:UNUSED_PRESERVE src0_sel:WORD_0\n"
    "ds_read_u16_d16_hi v169, v169 offset:0\n"
    "s_waitcnt lgkmcnt(8)\n"
    "v_mfma_f32_32x32x16_bf16 v[16:31], v[144:147], v[124:127], v[16:31]\n"
    "ds_read_b64_tr_b16 v[136:137], v1 offset:29696\n"
    "ds_read_b64_tr_b16 v[138:139], v1 offset:29952\n"
    "v_rcp_f16_sdwa v159, v159 dst_sel:WORD_0 dst_unused:UNUSED_PRESERVE src0_sel:WORD_0\n"
    "v_rcp_f16_sdwa v161, v161 dst_sel:WORD_0 dst_unused:UNUSED_PRESERVE src0_sel:WORD_0\n"
    "ds_read_u16 v170, v170 offset:0\n"
    "s_waitcnt lgkmcnt(8)\n"
    "v_mfma_f32_32x32x16_bf16 v[88:103], v[148:151], v[80:83], v[88:103]\n"
    "ds_read_b64_tr_b16 v[140:141], v2 offset:27776\n"
    "ds_read_b64_tr_b16 v[142:143], v2 offset:29312\n"
    "v_rcp_f16_sdwa v157, v157 dst_sel:WORD_1 dst_unused:UNUSED_PRESERVE src0_sel:WORD_1\n"
    "v_rcp_f16_sdwa v159, v159 dst_sel:WORD_1 dst_unused:UNUSED_PRESERVE src0_sel:WORD_1\n"
    "s_setprio 0\n"
    "ds_read_u16_d16_hi v171, v171 offset:0\n"
    "v_rcp_f16_sdwa v161, v161 dst_sel:WORD_1 dst_unused:UNUSED_PRESERVE src0_sel:WORD_1\n"
    "s_waitcnt lgkmcnt(8)\n"
    "v_mfma_f32_32x32x16_bf16 v[32:47], v[152:155], v[124:127], v[32:47]\n"
    "v_fma_mix_f32 v104, v104, v157, 0 op_sel:[0,0,0] op_sel_hi:[0,1,0]\n"
    "ds_read_u16 v172, v172 offset:0\n"
    "v_fma_mix_f32 v106, v106, v159, 0 op_sel:[0,0,0] op_sel_hi:[0,1,0]\n"
    "v_fma_mix_f32 v108, v108, v161, 0 op_sel:[0,0,0] op_sel_hi:[0,1,0]\n"
    "v_fma_mix_f32 v105, v105, v157, 0 op_sel:[0,1,0] op_sel_hi:[0,1,0]\n"
    "s_waitcnt lgkmcnt(5)\n"
    "v_mfma_f32_32x32x16_bf16 v[88:103], v[136:139], v[84:87], v[88:103]\n"
    "v_fma_mix_f32 v107, v107, v159, 0 op_sel:[0,1,0] op_sel_hi:[0,1,0]\n"
    "ds_read_u16_d16_hi v173, v173 offset:0\n"
    "v_fma_mix_f32 v109, v109, v161, 0 op_sel:[0,1,0] op_sel_hi:[0,1,0]\n"
    "v_cvt_pk_bf16_f32 v128, v104, v105\n"
    "v_cvt_pk_bf16_f32 v129, v106, v107\n"
    "v_cvt_pk_bf16_f32 v130, v108, v109\n"
    "s_waitcnt lgkmcnt(3)\n"
    "v_mfma_f32_32x32x16_bf16 v[48:63], v[140:143], v[124:127], v[48:63]\n"
    "v_or_b32_e32 v131, v164, v165\n"
    "v_or_b32_e32 v132, v166, v167\n"
    "v_or_b32_e32 v133, v168, v169\n"
    "s_waitcnt lgkmcnt(2)\n"
    "v_or_b32_e32 v134, v170, v171\n"
    "s_waitcnt lgkmcnt(0)\n"
    "v_or_b32_e32 v135, v172, v173\n"
    "s_waitcnt vmcnt(3)\n"
    "s_barrier\n"
    "ds_read_b64_tr_b16 v[136:137], v1 offset:36864\n"
    "ds_read_b64_tr_b16 v[138:139], v1 offset:37120\n"
    "ds_read_b64_tr_b16 v[140:141], v2 offset:36864\n"
    "ds_read_b64_tr_b16 v[142:143], v2 offset:38400\n"
    "ds_read_b64_tr_b16 v[144:145], v1 offset:37888\n"
    "ds_read_b64_tr_b16 v[146:147], v1 offset:38144\n"
    "s_setprio 3\n"
    "v_cvt_pk_f16_f32 v165, v94, v95\n"
    "v_cvt_pk_f16_f32 v167, v96, v97\n"
    "v_cvt_pk_f16_f32 v169, v98, v99\n"
    "v_cvt_pk_f16_f32 v171, v100, v101\n"
    "v_cvt_pk_f16_f32 v173, v102, v103\n"
    "s_add_u32 m0, s54, 12288\n"
    "s_mov_b32 s44, s48\n"
    "buffer_load_dwordx4 v6, s[36:39], s44 offen lds\n"
    "s_add_u32 m0, s54, 13312\n"
    "s_add_u32 s44, s44, s47\n"
    "buffer_load_dwordx4 v6, s[36:39], s44 offen lds\n"
    "s_add_u32 m0, s54, 14336\n"
    "s_add_u32 s44, s44, s47\n"
    "buffer_load_dwordx4 v6, s[36:39], s44 offen lds\n"
    "s_add_u32 s48, s48, s49\n"
    "s_cmp_ge_u32 s48, s50\n"
    "s_cselect_b32 s48, s51, s48\n"
    "v_and_b32_e32 v165, s42, v165\n"
    "v_and_b32_e32 v167, s42, v167\n"
    "v_and_b32_e32 v169, s42, v169\n"
    "v_and_b32_e32 v171, s42, v171\n"
    "v_and_b32_e32 v173, s42, v173\n"
    "v_lshrrev_b32_sdwa v164, 2, v165 dst_sel:DWORD dst_unused:UNUSED_PAD src0_sel:DWORD src1_sel:WORD_0\n"
    "v_lshrrev_b32_sdwa v166, 2, v167 dst_sel:DWORD dst_unused:UNUSED_PAD src0_sel:DWORD src1_sel:WORD_0\n"
    "s_waitcnt lgkmcnt(4)\n"
    "v_mfma_f32_32x32x16_bf16 v[104:119], v[136:139], v[64:67], 0\n"
    "ds_read_b64_tr_b16 v[148:149], v2 offset:36928\n"
    "ds_read_b64_tr_b16 v[150:151], v2 offset:38464\n"
    "v_lshrrev_b32_sdwa v168, 2, v169 dst_sel:DWORD dst_unused:UNUSED_PAD src0_sel:DWORD src1_sel:WORD_0\n"
    "v_lshrrev_b32_sdwa v170, 2, v171 dst_sel:DWORD dst_unused:UNUSED_PAD src0_sel:DWORD src1_sel:WORD_0\n"
    "v_lshrrev_b32_sdwa v172, 2, v173 dst_sel:DWORD dst_unused:UNUSED_PAD src0_sel:DWORD src1_sel:WORD_0\n"
    "v_lshrrev_b32_e32 v165, 18, v165\n"
    "s_waitcnt lgkmcnt(4)\n"
    "v_mfma_f32_32x32x16_bf16 v[16:31], v[140:143], v[128:131], v[16:31]\n"
    "ds_read_b64_tr_b16 v[152:153], v1 offset:38912\n"
    "ds_read_b64_tr_b16 v[154:155], v1 offset:39168\n"
    "v_lshrrev_b32_e32 v167, 18, v167\n"
    "v_lshrrev_b32_e32 v169, 18, v169\n"
    "s_setprio 2\n"
    "v_lshrrev_b32_e32 v171, 18, v171\n"
    "v_lshrrev_b32_e32 v173, 18, v173\n"
    "v_cvt_pk_f16_f32 v156, v88, v89\n"
    "s_waitcnt lgkmcnt(4)\n"
    "v_mfma_f32_32x32x16_bf16 v[104:119], v[144:147], v[68:71], v[104:119]\n"
    "ds_read_b64_tr_b16 v[136:137], v2 offset:36992\n"
    "ds_read_b64_tr_b16 v[138:139], v2 offset:38528\n"
    "v_cvt_pk_f16_f32 v158, v90, v91\n"
    "v_cvt_pk_f16_f32 v160, v92, v93\n"
    "v_pk_fma_f16 v157, v156, v156, s40\n"
    "v_pk_fma_f16 v159, v158, v158, s40\n"
    "ds_read_u16 v164, v164 offset:0\n"
    "v_pk_fma_f16 v161, v160, v160, s40\n"
    "s_waitcnt lgkmcnt(5)\n"
    "v_mfma_f32_32x32x16_bf16 v[32:47], v[148:151], v[128:131], v[32:47]\n"
    "ds_read_b64_tr_b16 v[140:141], v1 offset:39936\n"
    "ds_read_b64_tr_b16 v[142:143], v1 offset:40192\n"
    "v_pk_mul_f16 v157, v157, v156 neg_lo:[1,0] neg_hi:[1,0]\n"
    "v_pk_mul_f16 v159, v159, v158 neg_lo:[1,0] neg_hi:[1,0]\n"
    "v_pk_mul_f16 v161, v161, v160 neg_lo:[1,0] neg_hi:[1,0]\n"
    "ds_read_u16_d16_hi v165, v165 offset:0\n"
    "v_exp_f16_sdwa v157, v157 dst_sel:WORD_0 dst_unused:UNUSED_PRESERVE src0_sel:WORD_0\n"
    "s_waitcnt lgkmcnt(6)\n"
    "v_mfma_f32_32x32x16_bf16 v[104:119], v[152:155], v[72:75], v[104:119]\n"
    "ds_read_b64_tr_b16 v[144:145], v2 offset:39936\n"
    "ds_read_b64_tr_b16 v[146:147], v2 offset:41472\n"
    "v_exp_f16_sdwa v159, v159 dst_sel:WORD_0 dst_unused:UNUSED_PRESERVE src0_sel:WORD_0\n"
    "ds_read_u16 v166, v166 offset:0\n"
    "v_exp_f16_sdwa v161, v161 dst_sel:WORD_0 dst_unused:UNUSED_PRESERVE src0_sel:WORD_0\n"
    "s_setprio 1\n"
    "v_exp_f16_sdwa v157, v157 dst_sel:WORD_1 dst_unused:UNUSED_PRESERVE src0_sel:WORD_1\n"
    "s_waitcnt lgkmcnt(7)\n"
    "v_mfma_f32_32x32x16_bf16 v[48:63], v[136:139], v[128:131], v[48:63]\n"
    "ds_read_b64_tr_b16 v[148:149], v1 offset:40960\n"
    "ds_read_b64_tr_b16 v[150:151], v1 offset:41216\n"
    "ds_read_u16_d16_hi v167, v167 offset:0\n"
    "v_exp_f16_sdwa v159, v159 dst_sel:WORD_1 dst_unused:UNUSED_PRESERVE src0_sel:WORD_1\n"
    "v_exp_f16_sdwa v161, v161 dst_sel:WORD_1 dst_unused:UNUSED_PRESERVE src0_sel:WORD_1\n"
    "s_waitcnt lgkmcnt(7)\n"
    "v_mfma_f32_32x32x16_bf16 v[104:119], v[140:143], v[76:79], v[104:119]\n"
    "ds_read_b64_tr_b16 v[152:153], v2 offset:40000\n"
    "ds_read_b64_tr_b16 v[154:155], v2 offset:41536\n"
    "ds_read_u16 v168, v168 offset:0\n"
    "v_pk_fma_f16 v157, v157, s41, s41\n"
    "v_pk_fma_f16 v159, v159, s41, s41\n"
    "v_pk_fma_f16 v161, v161, s41, s41\n"
    "v_rcp_f16_sdwa v157, v157 dst_sel:WORD_0 dst_unused:UNUSED_PRESERVE src0_sel:WORD_0\n"
    "ds_read_u16_d16_hi v169, v169 offset:0\n"
    "s_waitcnt lgkmcnt(8)\n"
    "v_mfma_f32_32x32x16_bf16 v[16:31], v[144:147], v[132:135], v[16:31]\n"
    "ds_read_b64_tr_b16 v[136:137], v1 offset:41984\n"
    "ds_read_b64_tr_b16 v[138:139], v1 offset:42240\n"
    "v_rcp_f16_sdwa v159, v159 dst_sel:WORD_0 dst_unused:UNUSED_PRESERVE src0_sel:WORD_0\n"
    "v_rcp_f16_sdwa v161, v161 dst_sel:WORD_0 dst_unused:UNUSED_PRESERVE src0_sel:WORD_0\n"
    "ds_read_u16 v170, v170 offset:0\n"
    "s_waitcnt lgkmcnt(8)\n"
    "v_mfma_f32_32x32x16_bf16 v[104:119], v[148:151], v[80:83], v[104:119]\n"
    "ds_read_b64_tr_b16 v[140:141], v2 offset:40064\n"
    "ds_read_b64_tr_b16 v[142:143], v2 offset:41600\n"
    "v_rcp_f16_sdwa v157, v157 dst_sel:WORD_1 dst_unused:UNUSED_PRESERVE src0_sel:WORD_1\n"
    "v_rcp_f16_sdwa v159, v159 dst_sel:WORD_1 dst_unused:UNUSED_PRESERVE src0_sel:WORD_1\n"
    "s_setprio 0\n"
    "ds_read_u16_d16_hi v171, v171 offset:0\n"
    "v_rcp_f16_sdwa v161, v161 dst_sel:WORD_1 dst_unused:UNUSED_PRESERVE src0_sel:WORD_1\n"
    "s_waitcnt lgkmcnt(8)\n"
    "v_mfma_f32_32x32x16_bf16 v[32:47], v[152:155], v[132:135], v[32:47]\n"
    "v_fma_mix_f32 v88, v88, v157, 0 op_sel:[0,0,0] op_sel_hi:[0,1,0]\n"
    "ds_read_u16 v172, v172 offset:0\n"
    "v_fma_mix_f32 v90, v90, v159, 0 op_sel:[0,0,0] op_sel_hi:[0,1,0]\n"
    "v_fma_mix_f32 v92, v92, v161, 0 op_sel:[0,0,0] op_sel_hi:[0,1,0]\n"
    "v_fma_mix_f32 v89, v89, v157, 0 op_sel:[0,1,0] op_sel_hi:[0,1,0]\n"
    "s_waitcnt lgkmcnt(5)\n"
    "v_mfma_f32_32x32x16_bf16 v[104:119], v[136:139], v[84:87], v[104:119]\n"
    "v_fma_mix_f32 v91, v91, v159, 0 op_sel:[0,1,0] op_sel_hi:[0,1,0]\n"
    "ds_read_u16_d16_hi v173, v173 offset:0\n"
    "v_fma_mix_f32 v93, v93, v161, 0 op_sel:[0,1,0] op_sel_hi:[0,1,0]\n"
    "v_cvt_pk_bf16_f32 v120, v88, v89\n"
    "v_cvt_pk_bf16_f32 v121, v90, v91\n"
    "v_cvt_pk_bf16_f32 v122, v92, v93\n"
    "s_waitcnt lgkmcnt(3)\n"
    "v_mfma_f32_32x32x16_bf16 v[48:63], v[140:143], v[132:135], v[48:63]\n"
    "v_or_b32_e32 v123, v164, v165\n"
    "v_or_b32_e32 v124, v166, v167\n"
    "v_or_b32_e32 v125, v168, v169\n"
    "s_waitcnt lgkmcnt(2)\n"
    "v_or_b32_e32 v126, v170, v171\n"
    "s_waitcnt lgkmcnt(0)\n"
    "v_or_b32_e32 v127, v172, v173\n"
    "s_waitcnt vmcnt(3)\n"
    "s_barrier\n"
    "ds_read_b64_tr_b16 v[136:137], v1 offset:0\n"
    "ds_read_b64_tr_b16 v[138:139], v1 offset:256\n"
    "ds_read_b64_tr_b16 v[140:141], v2 offset:0\n"
    "ds_read_b64_tr_b16 v[142:143], v2 offset:1536\n"
    "ds_read_b64_tr_b16 v[144:145], v1 offset:1024\n"
    "ds_read_b64_tr_b16 v[146:147], v1 offset:1280\n"
    "s_setprio 3\n"
    "v_cvt_pk_f16_f32 v165, v110, v111\n"
    "v_cvt_pk_f16_f32 v167, v112, v113\n"
    "v_cvt_pk_f16_f32 v169, v114, v115\n"
    "v_cvt_pk_f16_f32 v171, v116, v117\n"
    "v_cvt_pk_f16_f32 v173, v118, v119\n"
    "s_add_u32 m0, s54, 24576\n"
    "s_mov_b32 s44, s48\n"
    "buffer_load_dwordx4 v6, s[36:39], s44 offen lds\n"
    "s_add_u32 m0, s54, 25600\n"
    "s_add_u32 s44, s44, s47\n"
    "buffer_load_dwordx4 v6, s[36:39], s44 offen lds\n"
    "s_add_u32 m0, s54, 26624\n"
    "s_add_u32 s44, s44, s47\n"
    "buffer_load_dwordx4 v6, s[36:39], s44 offen lds\n"
    "s_add_u32 s48, s48, s49\n"
    "s_cmp_ge_u32 s48, s50\n"
    "s_cselect_b32 s48, s51, s48\n"
    "v_and_b32_e32 v165, s42, v165\n"
    "v_and_b32_e32 v167, s42, v167\n"
    "v_and_b32_e32 v169, s42, v169\n"
    "v_and_b32_e32 v171, s42, v171\n"
    "v_and_b32_e32 v173, s42, v173\n"
    "v_lshrrev_b32_sdwa v164, 2, v165 dst_sel:DWORD dst_unused:UNUSED_PAD src0_sel:DWORD src1_sel:WORD_0\n"
    "v_lshrrev_b32_sdwa v166, 2, v167 dst_sel:DWORD dst_unused:UNUSED_PAD src0_sel:DWORD src1_sel:WORD_0\n"
    "s_waitcnt lgkmcnt(4)\n"
    "v_mfma_f32_32x32x16_bf16 v[88:103], v[136:139], v[64:67], 0\n"
    "ds_read_b64_tr_b16 v[148:149], v2 offset:64\n"
    "ds_read_b64_tr_b16 v[150:151], v2 offset:1600\n"
    "v_lshrrev_b32_sdwa v168, 2, v169 dst_sel:DWORD dst_unused:UNUSED_PAD src0_sel:DWORD src1_sel:WORD_0\n"
    "v_lshrrev_b32_sdwa v170, 2, v171 dst_sel:DWORD dst_unused:UNUSED_PAD src0_sel:DWORD src1_sel:WORD_0\n"
    "v_lshrrev_b32_sdwa v172, 2, v173 dst_sel:DWORD dst_unused:UNUSED_PAD src0_sel:DWORD src1_sel:WORD_0\n"
    "v_lshrrev_b32_e32 v165, 18, v165\n"
    "s_waitcnt lgkmcnt(4)\n"
    "v_mfma_f32_32x32x16_bf16 v[16:31], v[140:143], v[120:123], v[16:31]\n"
    "ds_read_b64_tr_b16 v[152:153], v1 offset:2048\n"
    "ds_read_b64_tr_b16 v[154:155], v1 offset:2304\n"
    "v_lshrrev_b32_e32 v167, 18, v167\n"
    "v_lshrrev_b32_e32 v169, 18, v169\n"
    "s_setprio 2\n"
    "v_lshrrev_b32_e32 v171, 18, v171\n"
    "v_lshrrev_b32_e32 v173, 18, v173\n"
    "v_cvt_pk_f16_f32 v156, v104, v105\n"
    "s_waitcnt lgkmcnt(4)\n"
    "v_mfma_f32_32x32x16_bf16 v[88:103], v[144:147], v[68:71], v[88:103]\n"
    "ds_read_b64_tr_b16 v[136:137], v2 offset:128\n"
    "ds_read_b64_tr_b16 v[138:139], v2 offset:1664\n"
    "v_cvt_pk_f16_f32 v158, v106, v107\n"
    "v_cvt_pk_f16_f32 v160, v108, v109\n"
    "v_pk_fma_f16 v157, v156, v156, s40\n"
    "v_pk_fma_f16 v159, v158, v158, s40\n"
    "ds_read_u16 v164, v164 offset:0\n"
    "v_pk_fma_f16 v161, v160, v160, s40\n"
    "s_waitcnt lgkmcnt(5)\n"
    "v_mfma_f32_32x32x16_bf16 v[32:47], v[148:151], v[120:123], v[32:47]\n"
    "ds_read_b64_tr_b16 v[140:141], v1 offset:3072\n"
    "ds_read_b64_tr_b16 v[142:143], v1 offset:3328\n"
    "v_pk_mul_f16 v157, v157, v156 neg_lo:[1,0] neg_hi:[1,0]\n"
    "v_pk_mul_f16 v159, v159, v158 neg_lo:[1,0] neg_hi:[1,0]\n"
    "v_pk_mul_f16 v161, v161, v160 neg_lo:[1,0] neg_hi:[1,0]\n"
    "ds_read_u16_d16_hi v165, v165 offset:0\n"
    "v_exp_f16_sdwa v157, v157 dst_sel:WORD_0 dst_unused:UNUSED_PRESERVE src0_sel:WORD_0\n"
    "s_waitcnt lgkmcnt(6)\n"
    "v_mfma_f32_32x32x16_bf16 v[88:103], v[152:155], v[72:75], v[88:103]\n"
    "ds_read_b64_tr_b16 v[144:145], v2 offset:3072\n"
    "ds_read_b64_tr_b16 v[146:147], v2 offset:4608\n"
    "v_exp_f16_sdwa v159, v159 dst_sel:WORD_0 dst_unused:UNUSED_PRESERVE src0_sel:WORD_0\n"
    "ds_read_u16 v166, v166 offset:0\n"
    "v_exp_f16_sdwa v161, v161 dst_sel:WORD_0 dst_unused:UNUSED_PRESERVE src0_sel:WORD_0\n"
    "s_setprio 1\n"
    "v_exp_f16_sdwa v157, v157 dst_sel:WORD_1 dst_unused:UNUSED_PRESERVE src0_sel:WORD_1\n"
    "s_waitcnt lgkmcnt(7)\n"
    "v_mfma_f32_32x32x16_bf16 v[48:63], v[136:139], v[120:123], v[48:63]\n"
    "ds_read_b64_tr_b16 v[148:149], v1 offset:4096\n"
    "ds_read_b64_tr_b16 v[150:151], v1 offset:4352\n"
    "ds_read_u16_d16_hi v167, v167 offset:0\n"
    "v_exp_f16_sdwa v159, v159 dst_sel:WORD_1 dst_unused:UNUSED_PRESERVE src0_sel:WORD_1\n"
    "v_exp_f16_sdwa v161, v161 dst_sel:WORD_1 dst_unused:UNUSED_PRESERVE src0_sel:WORD_1\n"
    "s_waitcnt lgkmcnt(7)\n"
    "v_mfma_f32_32x32x16_bf16 v[88:103], v[140:143], v[76:79], v[88:103]\n"
    "ds_read_b64_tr_b16 v[152:153], v2 offset:3136\n"
    "ds_read_b64_tr_b16 v[154:155], v2 offset:4672\n"
    "ds_read_u16 v168, v168 offset:0\n"
    "v_pk_fma_f16 v157, v157, s41, s41\n"
    "v_pk_fma_f16 v159, v159, s41, s41\n"
    "v_pk_fma_f16 v161, v161, s41, s41\n"
    "v_rcp_f16_sdwa v157, v157 dst_sel:WORD_0 dst_unused:UNUSED_PRESERVE src0_sel:WORD_0\n"
    "ds_read_u16_d16_hi v169, v169 offset:0\n"
    "s_waitcnt lgkmcnt(8)\n"
    "v_mfma_f32_32x32x16_bf16 v[16:31], v[144:147], v[124:127], v[16:31]\n"
    "ds_read_b64_tr_b16 v[136:137], v1 offset:5120\n"
    "ds_read_b64_tr_b16 v[138:139], v1 offset:5376\n"
    "v_rcp_f16_sdwa v159, v159 dst_sel:WORD_0 dst_unused:UNUSED_PRESERVE src0_sel:WORD_0\n"
    "v_rcp_f16_sdwa v161, v161 dst_sel:WORD_0 dst_unused:UNUSED_PRESERVE src0_sel:WORD_0\n"
    "ds_read_u16 v170, v170 offset:0\n"
    "s_waitcnt lgkmcnt(8)\n"
    "v_mfma_f32_32x32x16_bf16 v[88:103], v[148:151], v[80:83], v[88:103]\n"
    "ds_read_b64_tr_b16 v[140:141], v2 offset:3200\n"
    "ds_read_b64_tr_b16 v[142:143], v2 offset:4736\n"
    "v_rcp_f16_sdwa v157, v157 dst_sel:WORD_1 dst_unused:UNUSED_PRESERVE src0_sel:WORD_1\n"
    "v_rcp_f16_sdwa v159, v159 dst_sel:WORD_1 dst_unused:UNUSED_PRESERVE src0_sel:WORD_1\n"
    "s_setprio 0\n"
    "ds_read_u16_d16_hi v171, v171 offset:0\n"
    "v_rcp_f16_sdwa v161, v161 dst_sel:WORD_1 dst_unused:UNUSED_PRESERVE src0_sel:WORD_1\n"
    "s_waitcnt lgkmcnt(8)\n"
    "v_mfma_f32_32x32x16_bf16 v[32:47], v[152:155], v[124:127], v[32:47]\n"
    "v_fma_mix_f32 v104, v104, v157, 0 op_sel:[0,0,0] op_sel_hi:[0,1,0]\n"
    "ds_read_u16 v172, v172 offset:0\n"
    "v_fma_mix_f32 v106, v106, v159, 0 op_sel:[0,0,0] op_sel_hi:[0,1,0]\n"
    "v_fma_mix_f32 v108, v108, v161, 0 op_sel:[0,0,0] op_sel_hi:[0,1,0]\n"
    "v_fma_mix_f32 v105, v105, v157, 0 op_sel:[0,1,0] op_sel_hi:[0,1,0]\n"
    "s_waitcnt lgkmcnt(5)\n"
    "v_mfma_f32_32x32x16_bf16 v[88:103], v[136:139], v[84:87], v[88:103]\n"
    "v_fma_mix_f32 v107, v107, v159, 0 op_sel:[0,1,0] op_sel_hi:[0,1,0]\n"
    "ds_read_u16_d16_hi v173, v173 offset:0\n"
    "v_fma_mix_f32 v109, v109, v161, 0 op_sel:[0,1,0] op_sel_hi:[0,1,0]\n"
    "v_cvt_pk_bf16_f32 v128, v104, v105\n"
    "v_cvt_pk_bf16_f32 v129, v106, v107\n"
    "v_cvt_pk_bf16_f32 v130, v108, v109\n"
    "s_waitcnt lgkmcnt(3)\n"
    "v_mfma_f32_32x32x16_bf16 v[48:63], v[140:143], v[124:127], v[48:63]\n"
    "v_or_b32_e32 v131, v164, v165\n"
    "v_or_b32_e32 v132, v166, v167\n"
    "v_or_b32_e32 v133, v168, v169\n"
    "s_waitcnt lgkmcnt(2)\n"
    "v_or_b32_e32 v134, v170, v171\n"
    "s_waitcnt lgkmcnt(0)\n"
    "v_or_b32_e32 v135, v172, v173\n"
    "s_waitcnt vmcnt(3)\n"
    "s_barrier\n"
    "ds_read_b64_tr_b16 v[136:137], v1 offset:12288\n"
    "ds_read_b64_tr_b16 v[138:139], v1 offset:12544\n"
    "ds_read_b64_tr_b16 v[140:141], v2 offset:12288\n"
    "ds_read_b64_tr_b16 v[142:143], v2 offset:13824\n"
    "ds_read_b64_tr_b16 v[144:145], v1 offset:13312\n"
    "ds_read_b64_tr_b16 v[146:147], v1 offset:13568\n"
    "s_setprio 3\n"
    "v_cvt_pk_f16_f32 v165, v94, v95\n"
    "v_cvt_pk_f16_f32 v167, v96, v97\n"
    "v_cvt_pk_f16_f32 v169, v98, v99\n"
    "v_cvt_pk_f16_f32 v171, v100, v101\n"
    "v_cvt_pk_f16_f32 v173, v102, v103\n"
    "s_add_u32 m0, s54, 36864\n"
    "s_mov_b32 s44, s48\n"
    "buffer_load_dwordx4 v6, s[36:39], s44 offen lds\n"
    "s_add_u32 m0, s54, 37888\n"
    "s_add_u32 s44, s44, s47\n"
    "buffer_load_dwordx4 v6, s[36:39], s44 offen lds\n"
    "s_add_u32 m0, s54, 38912\n"
    "s_add_u32 s44, s44, s47\n"
    "buffer_load_dwordx4 v6, s[36:39], s44 offen lds\n"
    "s_add_u32 s48, s48, s49\n"
    "s_cmp_ge_u32 s48, s50\n"
    "s_cselect_b32 s48, s51, s48\n"
    "v_and_b32_e32 v165, s42, v165\n"
    "v_and_b32_e32 v167, s42, v167\n"
    "v_and_b32_e32 v169, s42, v169\n"
    "v_and_b32_e32 v171, s42, v171\n"
    "v_and_b32_e32 v173, s42, v173\n"
    "v_lshrrev_b32_sdwa v164, 2, v165 dst_sel:DWORD dst_unused:UNUSED_PAD src0_sel:DWORD src1_sel:WORD_0\n"
    "v_lshrrev_b32_sdwa v166, 2, v167 dst_sel:DWORD dst_unused:UNUSED_PAD src0_sel:DWORD src1_sel:WORD_0\n"
    "s_waitcnt lgkmcnt(4)\n"
    "v_mfma_f32_32x32x16_bf16 v[104:119], v[136:139], v[64:67], 0\n"
    "ds_read_b64_tr_b16 v[148:149], v2 offset:12352\n"
    "ds_read_b64_tr_b16 v[150:151], v2 offset:13888\n"
    "v_lshrrev_b32_sdwa v168, 2, v169 dst_sel:DWORD dst_unused:UNUSED_PAD src0_sel:DWORD src1_sel:WORD_0\n"
    "v_lshrrev_b32_sdwa v170, 2, v171 dst_sel:DWORD dst_unused:UNUSED_PAD src0_sel:DWORD src1_sel:WORD_0\n"
    "v_lshrrev_b32_sdwa v172, 2, v173 dst_sel:DWORD dst_unused:UNUSED_PAD src0_sel:DWORD src1_sel:WORD_0\n"
    "v_lshrrev_b32_e32 v165, 18, v165\n"
    "s_waitcnt lgkmcnt(4)\n"
    "v_mfma_f32_32x32x16_bf16 v[16:31], v[140:143], v[128:131], v[16:31]\n"
    "ds_read_b64_tr_b16 v[152:153], v1 offset:14336\n"
    "ds_read_b64_tr_b16 v[154:155], v1 offset:14592\n"
    "v_lshrrev_b32_e32 v167, 18, v167\n"
    "v_lshrrev_b32_e32 v169, 18, v169\n"
    "s_setprio 2\n"
    "v_lshrrev_b32_e32 v171, 18, v171\n"
    "v_lshrrev_b32_e32 v173, 18, v173\n"
    "v_cvt_pk_f16_f32 v156, v88, v89\n"
    "s_waitcnt lgkmcnt(4)\n"
    "v_mfma_f32_32x32x16_bf16 v[104:119], v[144:147], v[68:71], v[104:119]\n"
    "ds_read_b64_tr_b16 v[136:137], v2 offset:12416\n"
    "ds_read_b64_tr_b16 v[138:139], v2 offset:13952\n"
    "v_cvt_pk_f16_f32 v158, v90, v91\n"
    "v_cvt_pk_f16_f32 v160, v92, v93\n"
    "v_pk_fma_f16 v157, v156, v156, s40\n"
    "v_pk_fma_f16 v159, v158, v158, s40\n"
    "ds_read_u16 v164, v164 offset:0\n"
    "v_pk_fma_f16 v161, v160, v160, s40\n"
    "s_waitcnt lgkmcnt(5)\n"
    "v_mfma_f32_32x32x16_bf16 v[32:47], v[148:151], v[128:131], v[32:47]\n"
    "ds_read_b64_tr_b16 v[140:141], v1 offset:15360\n"
    "ds_read_b64_tr_b16 v[142:143], v1 offset:15616\n"
    "v_pk_mul_f16 v157, v157, v156 neg_lo:[1,0] neg_hi:[1,0]\n"
    "v_pk_mul_f16 v159, v159, v158 neg_lo:[1,0] neg_hi:[1,0]\n"
    "v_pk_mul_f16 v161, v161, v160 neg_lo:[1,0] neg_hi:[1,0]\n"
    "ds_read_u16_d16_hi v165, v165 offset:0\n"
    "v_exp_f16_sdwa v157, v157 dst_sel:WORD_0 dst_unused:UNUSED_PRESERVE src0_sel:WORD_0\n"
    "s_waitcnt lgkmcnt(6)\n"
    "v_mfma_f32_32x32x16_bf16 v[104:119], v[152:155], v[72:75], v[104:119]\n"
    "ds_read_b64_tr_b16 v[144:145], v2 offset:15360\n"
    "ds_read_b64_tr_b16 v[146:147], v2 offset:16896\n"
    "v_exp_f16_sdwa v159, v159 dst_sel:WORD_0 dst_unused:UNUSED_PRESERVE src0_sel:WORD_0\n"
    "ds_read_u16 v166, v166 offset:0\n"
    "v_exp_f16_sdwa v161, v161 dst_sel:WORD_0 dst_unused:UNUSED_PRESERVE src0_sel:WORD_0\n"
    "s_setprio 1\n"
    "v_exp_f16_sdwa v157, v157 dst_sel:WORD_1 dst_unused:UNUSED_PRESERVE src0_sel:WORD_1\n"
    "s_waitcnt lgkmcnt(7)\n"
    "v_mfma_f32_32x32x16_bf16 v[48:63], v[136:139], v[128:131], v[48:63]\n"
    "ds_read_b64_tr_b16 v[148:149], v1 offset:16384\n"
    "ds_read_b64_tr_b16 v[150:151], v1 offset:16640\n"
    "ds_read_u16_d16_hi v167, v167 offset:0\n"
    "v_exp_f16_sdwa v159, v159 dst_sel:WORD_1 dst_unused:UNUSED_PRESERVE src0_sel:WORD_1\n"
    "v_exp_f16_sdwa v161, v161 dst_sel:WORD_1 dst_unused:UNUSED_PRESERVE src0_sel:WORD_1\n"
    "s_waitcnt lgkmcnt(7)\n"
    "v_mfma_f32_32x32x16_bf16 v[104:119], v[140:143], v[76:79], v[104:119]\n"
    "ds_read_b64_tr_b16 v[152:153], v2 offset:15424\n"
    "ds_read_b64_tr_b16 v[154:155], v2 offset:16960\n"
    "ds_read_u16 v168, v168 offset:0\n"
    "v_pk_fma_f16 v157, v157, s41, s41\n"
    "v_pk_fma_f16 v159, v159, s41, s41\n"
    "v_pk_fma_f16 v161, v161, s41, s41\n"
    "v_rcp_f16_sdwa v157, v157 dst_sel:WORD_0 dst_unused:UNUSED_PRESERVE src0_sel:WORD_0\n"
    "ds_read_u16_d16_hi v169, v169 offset:0\n"
    "s_waitcnt lgkmcnt(8)\n"
    "v_mfma_f32_32x32x16_bf16 v[16:31], v[144:147], v[132:135], v[16:31]\n"
    "ds_read_b64_tr_b16 v[136:137], v1 offset:17408\n"
    "ds_read_b64_tr_b16 v[138:139], v1 offset:17664\n"
    "v_rcp_f16_sdwa v159, v159 dst_sel:WORD_0 dst_unused:UNUSED_PRESERVE src0_sel:WORD_0\n"
    "v_rcp_f16_sdwa v161, v161 dst_sel:WORD_0 dst_unused:UNUSED_PRESERVE src0_sel:WORD_0\n"
    "ds_read_u16 v170, v170 offset:0\n"
    "s_waitcnt lgkmcnt(8)\n"
    "v_mfma_f32_32x32x16_bf16 v[104:119], v[148:151], v[80:83], v[104:119]\n"
    "ds_read_b64_tr_b16 v[140:141], v2 offset:15488\n"
    "ds_read_b64_tr_b16 v[142:143], v2 offset:17024\n"
    "v_rcp_f16_sdwa v157, v157 dst_sel:WORD_1 dst_unused:UNUSED_PRESERVE src0_sel:WORD_1\n"
    "v_rcp_f16_sdwa v159, v159 dst_sel:WORD_1 dst_unused:UNUSED_PRESERVE src0_sel:WORD_1\n"
    "s_setprio 0\n"
    "ds_read_u16_d16_hi v171, v171 offset:0\n"
    "v_rcp_f16_sdwa v161, v161 dst_sel:WORD_1 dst_unused:UNUSED_PRESERVE src0_sel:WORD_1\n"
    "s_waitcnt lgkmcnt(8)\n"
    "v_mfma_f32_32x32x16_bf16 v[32:47], v[152:155], v[132:135], v[32:47]\n"
    "v_fma_mix_f32 v88, v88, v157, 0 op_sel:[0,0,0] op_sel_hi:[0,1,0]\n"
    "ds_read_u16 v172, v172 offset:0\n"
    "v_fma_mix_f32 v90, v90, v159, 0 op_sel:[0,0,0] op_sel_hi:[0,1,0]\n"
    "v_fma_mix_f32 v92, v92, v161, 0 op_sel:[0,0,0] op_sel_hi:[0,1,0]\n"
    "v_fma_mix_f32 v89, v89, v157, 0 op_sel:[0,1,0] op_sel_hi:[0,1,0]\n"
    "s_waitcnt lgkmcnt(5)\n"
    "v_mfma_f32_32x32x16_bf16 v[104:119], v[136:139], v[84:87], v[104:119]\n"
    "v_fma_mix_f32 v91, v91, v159, 0 op_sel:[0,1,0] op_sel_hi:[0,1,0]\n"
    "ds_read_u16_d16_hi v173, v173 offset:0\n"
    "v_fma_mix_f32 v93, v93, v161, 0 op_sel:[0,1,0] op_sel_hi:[0,1,0]\n"
    "v_cvt_pk_bf16_f32 v120, v88, v89\n"
    "v_cvt_pk_bf16_f32 v121, v90, v91\n"
    "v_cvt_pk_bf16_f32 v122, v92, v93\n"
    "s_waitcnt lgkmcnt(3)\n"
    "v_mfma_f32_32x32x16_bf16 v[48:63], v[140:143], v[132:135], v[48:63]\n"
    "v_or_b32_e32 v123, v164, v165\n"
    "v_or_b32_e32 v124, v166, v167\n"
    "v_or_b32_e32 v125, v168, v169\n"
    "s_waitcnt lgkmcnt(2)\n"
    "v_or_b32_e32 v126, v170, v171\n"
    "s_waitcnt lgkmcnt(0)\n"
    "v_or_b32_e32 v127, v172, v173\n"
    "s_waitcnt vmcnt(3)\n"
    "s_barrier\n"
    "ds_read_b64_tr_b16 v[136:137], v1 offset:24576\n"
    "ds_read_b64_tr_b16 v[138:139], v1 offset:24832\n"
    "ds_read_b64_tr_b16 v[140:141], v2 offset:24576\n"
    "ds_read_b64_tr_b16 v[142:143], v2 offset:26112\n"
    "ds_read_b64_tr_b16 v[144:145], v1 offset:25600\n"
    "ds_read_b64_tr_b16 v[146:147], v1 offset:25856\n"
    "s_setprio 3\n"
    "v_cvt_pk_f16_f32 v165, v110, v111\n"
    "v_cvt_pk_f16_f32 v167, v112, v113\n"
    "v_cvt_pk_f16_f32 v169, v114, v115\n"
    "v_cvt_pk_f16_f32 v171, v116, v117\n"
    "v_cvt_pk_f16_f32 v173, v118, v119\n"
    "s_add_u32 m0, s54, 0\n"
    "s_mov_b32 s44, s48\n"
    "buffer_load_dwordx4 v6, s[36:39], s44 offen lds\n"
    "s_add_u32 m0, s54, 1024\n"
    "s_add_u32 s44, s44, s47\n"
    "buffer_load_dwordx4 v6, s[36:39], s44 offen lds\n"
    "s_add_u32 m0, s54, 2048\n"
    "s_add_u32 s44, s44, s47\n"
    "buffer_load_dwordx4 v6, s[36:39], s44 offen lds\n"
    "s_add_u32 s48, s48, s49\n"
    "s_cmp_ge_u32 s48, s50\n"
    "s_cselect_b32 s48, s51, s48\n"
    "v_and_b32_e32 v165, s42, v165\n"
    "v_and_b32_e32 v167, s42, v167\n"
    "v_and_b32_e32 v169, s42, v169\n"
    "v_and_b32_e32 v171, s42, v171\n"
    "v_and_b32_e32 v173, s42, v173\n"
    "v_lshrrev_b32_sdwa v164, 2, v165 dst_sel:DWORD dst_unused:UNUSED_PAD src0_sel:DWORD src1_sel:WORD_0\n"
    "v_lshrrev_b32_sdwa v166, 2, v167 dst_sel:DWORD dst_unused:UNUSED_PAD src0_sel:DWORD src1_sel:WORD_0\n"
    "s_waitcnt lgkmcnt(4)\n"
    "v_mfma_f32_32x32x16_bf16 v[88:103], v[136:139], v[64:67], 0\n"
    "ds_read_b64_tr_b16 v[148:149], v2 offset:24640\n"
    "ds_read_b64_tr_b16 v[150:151], v2 offset:26176\n"
    "v_lshrrev_b32_sdwa v168, 2, v169 dst_sel:DWORD dst_unused:UNUSED_PAD src0_sel:DWORD src1_sel:WORD_0\n"
    "v_lshrrev_b32_sdwa v170, 2, v171 dst_sel:DWORD dst_unused:UNUSED_PAD src0_sel:DWORD src1_sel:WORD_0\n"
    "v_lshrrev_b32_sdwa v172, 2, v173 dst_sel:DWORD dst_unused:UNUSED_PAD src0_sel:DWORD src1_sel:WORD_0\n"
    "v_lshrrev_b32_e32 v165, 18, v165\n"
    "s_waitcnt lgkmcnt(4)\n"
    "v_mfma_f32_32x32x16_bf16 v[16:31], v[140:143], v[120:123], v[16:31]\n"
    "ds_read_b64_tr_b16 v[152:153], v1 offset:26624\n"
    "ds_read_b64_tr_b16 v[154:155], v1 offset:26880\n"
    "v_lshrrev_b32_e32 v167, 18, v167\n"
    "v_lshrrev_b32_e32 v169, 18, v169\n"
    "s_setprio 2\n"
    "v_lshrrev_b32_e32 v171, 18, v171\n"
    "v_lshrrev_b32_e32 v173, 18, v173\n"
    "v_cvt_pk_f16_f32 v156, v104, v105\n"
    "s_waitcnt lgkmcnt(4)\n"
    "v_mfma_f32_32x32x16_bf16 v[88:103], v[144:147], v[68:71], v[88:103]\n"
    "ds_read_b64_tr_b16 v[136:137], v2 offset:24704\n"
    "ds_read_b64_tr_b16 v[138:139], v2 offset:26240\n"
    "v_cvt_pk_f16_f32 v158, v106, v107\n"
    "v_cvt_pk_f16_f32 v160, v108, v109\n"
    "v_pk_fma_f16 v157, v156, v156, s40\n"
    "v_pk_fma_f16 v159, v158, v158, s40\n"
    "ds_read_u16 v164, v164 offset:0\n"
    "v_pk_fma_f16 v161, v160, v160, s40\n"
    "s_waitcnt lgkmcnt(5)\n"
    "v_mfma_f32_32x32x16_bf16 v[32:47], v[148:151], v[120:123], v[32:47]\n"
    "ds_read_b64_tr_b16 v[140:141], v1 offset:27648\n"
    "ds_read_b64_tr_b16 v[142:143], v1 offset:27904\n"
    "v_pk_mul_f16 v157, v157, v156 neg_lo:[1,0] neg_hi:[1,0]\n"
    "v_pk_mul_f16 v159, v159, v158 neg_lo:[1,0] neg_hi:[1,0]\n"
    "v_pk_mul_f16 v161, v161, v160 neg_lo:[1,0] neg_hi:[1,0]\n"
    "ds_read_u16_d16_hi v165, v165 offset:0\n"
    "v_exp_f16_sdwa v157, v157 dst_sel:WORD_0 dst_unused:UNUSED_PRESERVE src0_sel:WORD_0\n"
    "s_waitcnt lgkmcnt(6)\n"
    "v_mfma_f32_32x32x16_bf16 v[88:103], v[152:155], v[72:75], v[88:103]\n"
    "ds_read_b64_tr_b16 v[144:145], v2 offset:27648\n"
    "ds_read_b64_tr_b16 v[146:147], v2 offset:29184\n"
    "v_exp_f16_sdwa v159, v159 dst_sel:WORD_0 dst_unused:UNUSED_PRESERVE src0_sel:WORD_0\n"
    "ds_read_u16 v166, v166 offset:0\n"
    "v_exp_f16_sdwa v161, v161 dst_sel:WORD_0 dst_unused:UNUSED_PRESERVE src0_sel:WORD_0\n"
    "s_setprio 1\n"
    "v_exp_f16_sdwa v157, v157 dst_sel:WORD_1 dst_unused:UNUSED_PRESERVE src0_sel:WORD_1\n"
    "s_waitcnt lgkmcnt(7)\n"
    "v_mfma_f32_32x32x16_bf16 v[48:63], v[136:139], v[120:123], v[48:63]\n"
    "ds_read_b64_tr_b16 v[148:149], v1 offset:28672\n"
    "ds_read_b64_tr_b16 v[150:151], v1 offset:28928\n"
    "ds_read_u16_d16_hi v167, v167 offset:0\n"
    "v_exp_f16_sdwa v159, v159 dst_sel:WORD_1 dst_unused:UNUSED_PRESERVE src0_sel:WORD_1\n"
    "v_exp_f16_sdwa v161, v161 dst_sel:WORD_1 dst_unused:UNUSED_PRESERVE src0_sel:WORD_1\n"
    "s_waitcnt lgkmcnt(7)\n"
    "v_mfma_f32_32x32x16_bf16 v[88:103], v[140:143], v[76:79], v[88:103]\n"
    "ds_read_b64_tr_b16 v[152:153], v2 offset:27712\n"
    "ds_read_b64_tr_b16 v[154:155], v2 offset:29248\n"
    "ds_read_u16 v168, v168 offset:0\n"
    "v_pk_fma_f16 v157, v157, s41, s41\n"
    "v_pk_fma_f16 v159, v159, s41, s41\n"
    "v_pk_fma_f16 v161, v161, s41, s41\n"
    "v_rcp_f16_sdwa v157, v157 dst_sel:WORD_0 dst_unused:UNUSED_PRESERVE src0_sel:WORD_0\n"
    "ds_read_u16_d16_hi v169, v169 offset:0\n"
    "s_waitcnt lgkmcnt(8)\n"
    "v_mfma_f32_32x32x16_bf16 v[16:31], v[144:147], v[124:127], v[16:31]\n"
    "ds_read_b64_tr_b16 v[136:137], v1 offset:29696\n"
    "ds_read_b64_tr_b16 v[138:139], v1 offset:29952\n"
    "v_rcp_f16_sdwa v159, v159 dst_sel:WORD_0 dst_unused:UNUSED_PRESERVE src0_sel:WORD_0\n"
    "v_rcp_f16_sdwa v161, v161 dst_sel:WORD_0 dst_unused:UNUSED_PRESERVE src0_sel:WORD_0\n"
    "ds_read_u16 v170, v170 offset:0\n"
    "s_waitcnt lgkmcnt(8)\n"
    "v_mfma_f32_32x32x16_bf16 v[88:103], v[148:151], v[80:83], v[88:103]\n"
    "ds_read_b64_tr_b16 v[140:141], v2 offset:27776\n"
    "ds_read_b64_tr_b16 v[142:143], v2 offset:29312\n"
    "v_rcp_f16_sdwa v157, v157 dst_sel:WORD_1 dst_unused:UNUSED_PRESERVE src0_sel:WORD_1\n"
    "v_rcp_f16_sdwa v159, v159 dst_sel:WORD_1 dst_unused:UNUSED_PRESERVE src0_sel:WORD_1\n"
    "s_setprio 0\n"
    "ds_read_u16_d16_hi v171, v171 offset:0\n"
    "v_rcp_f16_sdwa v161, v161 dst_sel:WORD_1 dst_unused:UNUSED_PRESERVE src0_sel:WORD_1\n"
    "s_waitcnt lgkmcnt(8)\n"
    "v_mfma_f32_32x32x16_bf16 v[32:47], v[152:155], v[124:127], v[32:47]\n"
    "v_fma_mix_f32 v104, v104, v157, 0 op_sel:[0,0,0] op_sel_hi:[0,1,0]\n"
    "ds_read_u16 v172, v172 offset:0\n"
    "v_fma_mix_f32 v106, v106, v159, 0 op_sel:[0,0,0] op_sel_hi:[0,1,0]\n"
    "v_fma_mix_f32 v108, v108, v161, 0 op_sel:[0,0,0] op_sel_hi:[0,1,0]\n"
    "v_fma_mix_f32 v105, v105, v157, 0 op_sel:[0,1,0] op_sel_hi:[0,1,0]\n"
    "s_waitcnt lgkmcnt(5)\n"
    "v_mfma_f32_32x32x16_bf16 v[88:103], v[136:139], v[84:87], v[88:103]\n"
    "v_fma_mix_f32 v107, v107, v159, 0 op_sel:[0,1,0] op_sel_hi:[0,1,0]\n"
    "ds_read_u16_d16_hi v173, v173 offset:0\n"
    "v_fma_mix_f32 v109, v109, v161, 0 op_sel:[0,1,0] op_sel_hi:[0,1,0]\n"
    "v_cvt_pk_bf16_f32 v128, v104, v105\n"
    "v_cvt_pk_bf16_f32 v129, v106, v107\n"
    "v_cvt_pk_bf16_f32 v130, v108, v109\n"
    "s_waitcnt lgkmcnt(3)\n"
    "v_mfma_f32_32x32x16_bf16 v[48:63], v[140:143], v[124:127], v[48:63]\n"
    "v_or_b32_e32 v131, v164, v165\n"
    "v_or_b32_e32 v132, v166, v167\n"
    "v_or_b32_e32 v133, v168, v169\n"
    "s_waitcnt lgkmcnt(2)\n"
    "v_or_b32_e32 v134, v170, v171\n"
    "s_waitcnt lgkmcnt(0)\n"
    "v_or_b32_e32 v135, v172, v173\n"
    "s_waitcnt vmcnt(3)\n"
    "s_barrier\n"
    "ds_read_b64_tr_b16 v[136:137], v1 offset:36864\n"
    "ds_read_b64_tr_b16 v[138:139], v1 offset:37120\n"
    "ds_read_b64_tr_b16 v[140:141], v2 offset:36864\n"
    "ds_read_b64_tr_b16 v[142:143], v2 offset:38400\n"
    "ds_read_b64_tr_b16 v[144:145], v1 offset:37888\n"
    "ds_read_b64_tr_b16 v[146:147], v1 offset:38144\n"
    "s_setprio 3\n"
    "v_cvt_pk_f16_f32 v165, v94, v95\n"
    "v_cvt_pk_f16_f32 v167, v96, v97\n"
    "v_cvt_pk_f16_f32 v169, v98, v99\n"
    "v_cvt_pk_f16_f32 v171, v100, v101\n"
    "v_cvt_pk_f16_f32 v173, v102, v103\n"
    "s_add_u32 m0, s54, 12288\n"
    "s_mov_b32 s44, s48\n"
    "buffer_load_dwordx4 v6, s[36:39], s44 offen lds\n"
    "s_add_u32 m0, s54, 13312\n"
    "s_add_u32 s44, s44, s47\n"
    "buffer_load_dwordx4 v6, s[36:39], s44 offen lds\n"
    "s_add_u32 m0, s54, 14336\n"
    "s_add_u32 s44, s44, s47\n"
    "buffer_load_dwordx4 v6, s[36:39], s44 offen lds\n"
    "s_add_u32 s48, s48, s49\n"
    "s_cmp_ge_u32 s48, s50\n"
    "s_cselect_b32 s48, s51, s48\n"
    "v_and_b32_e32 v165, s42, v165\n"
    "v_and_b32_e32 v167, s42, v167\n"
    "v_and_b32_e32 v169, s42, v169\n"
    "v_and_b32_e32 v171, s42, v171\n"
    "v_and_b32_e32 v173, s42, v173\n"
    "v_lshrrev_b32_sdwa v164, 2, v165 dst_sel:DWORD dst_unused:UNUSED_PAD src0_sel:DWORD src1_sel:WORD_0\n"
    "v_lshrrev_b32_sdwa v166, 2, v167 dst_sel:DWORD dst_unused:UNUSED_PAD src0_sel:DWORD src1_sel:WORD_0\n"
    "s_waitcnt lgkmcnt(4)\n"
    "v_mfma_f32_32x32x16_bf16 v[104:119], v[136:139], v[64:67], 0\n"
    "ds_read_b64_tr_b16 v[148:149], v2 offset:36928\n"
    "ds_read_b64_tr_b16 v[150:151], v2 offset:38464\n"
    "v_lshrrev_b32_sdwa v168, 2, v169 dst_sel:DWORD dst_unused:UNUSED_PAD src0_sel:DWORD src1_sel:WORD_0\n"
    "v_lshrrev_b32_sdwa v170, 2, v171 dst_sel:DWORD dst_unused:UNUSED_PAD src0_sel:DWORD src1_sel:WORD_0\n"
    "v_lshrrev_b32_sdwa v172, 2, v173 dst_sel:DWORD dst_unused:UNUSED_PAD src0_sel:DWORD src1_sel:WORD_0\n"
    "v_lshrrev_b32_e32 v165, 18, v165\n"
    "s_waitcnt lgkmcnt(4)\n"
    "v_mfma_f32_32x32x16_bf16 v[16:31], v[140:143], v[128:131], v[16:31]\n"
    "ds_read_b64_tr_b16 v[152:153], v1 offset:38912\n"
    "ds_read_b64_tr_b16 v[154:155], v1 offset:39168\n"
    "v_lshrrev_b32_e32 v167, 18, v167\n"
    "v_lshrrev_b32_e32 v169, 18, v169\n"
    "s_setprio 2\n"
    "v_lshrrev_b32_e32 v171, 18, v171\n"
    "v_lshrrev_b32_e32 v173, 18, v173\n"
    "v_cvt_pk_f16_f32 v156, v88, v89\n"
    "s_waitcnt lgkmcnt(4)\n"
    "v_mfma_f32_32x32x16_bf16 v[104:119], v[144:147], v[68:71], v[104:119]\n"
    "ds_read_b64_tr_b16 v[136:137], v2 offset:36992\n"
    "ds_read_b64_tr_b16 v[138:139], v2 offset:38528\n"
    "v_cvt_pk_f16_f32 v158, v90, v91\n"
    "v_cvt_pk_f16_f32 v160, v92, v93\n"
    "v_pk_fma_f16 v157, v156, v156, s40\n"
    "v_pk_fma_f16 v159, v158, v158, s40\n"
    "ds_read_u16 v164, v164 offset:0\n"
    "v_pk_fma_f16 v161, v160, v160, s40\n"
    "s_waitcnt lgkmcnt(5)\n"
    "v_mfma_f32_32x32x16_bf16 v[32:47], v[148:151], v[128:131], v[32:47]\n"
    "ds_read_b64_tr_b16 v[140:141], v1 offset:39936\n"
    "ds_read_b64_tr_b16 v[142:143], v1 offset:40192\n"
    "v_pk_mul_f16 v157, v157, v156 neg_lo:[1,0] neg_hi:[1,0]\n"
    "v_pk_mul_f16 v159, v159, v158 neg_lo:[1,0] neg_hi:[1,0]\n"
    "v_pk_mul_f16 v161, v161, v160 neg_lo:[1,0] neg_hi:[1,0]\n"
    "ds_read_u16_d16_hi v165, v165 offset:0\n"
    "v_exp_f16_sdwa v157, v157 dst_sel:WORD_0 dst_unused:UNUSED_PRESERVE src0_sel:WORD_0\n"
    "s_waitcnt lgkmcnt(6)\n"
    "v_mfma_f32_32x32x16_bf16 v[104:119], v[152:155], v[72:75], v[104:119]\n"
    "ds_read_b64_tr_b16 v[144:145], v2 offset:39936\n"
    "ds_read_b64_tr_b16 v[146:147], v2 offset:41472\n"
    "v_exp_f16_sdwa v159, v159 dst_sel:WORD_0 dst_unused:UNUSED_PRESERVE src0_sel:WORD_0\n"
    "ds_read_u16 v166, v166 offset:0\n"
    "v_exp_f16_sdwa v161, v161 dst_sel:WORD_0 dst_unused:UNUSED_PRESERVE src0_sel:WORD_0\n"
    "s_setprio 1\n"
    "v_exp_f16_sdwa v157, v157 dst_sel:WORD_1 dst_unused:UNUSED_PRESERVE src0_sel:WORD_1\n"
    "s_waitcnt lgkmcnt(7)\n"
    "v_mfma_f32_32x32x16_bf16 v[48:63], v[136:139], v[128:131], v[48:63]\n"
    "ds_read_b64_tr_b16 v[148:149], v1 offset:40960\n"
    "ds_read_b64_tr_b16 v[150:151], v1 offset:41216\n"
    "ds_read_u16_d16_hi v167, v167 offset:0\n"
    "v_exp_f16_sdwa v159, v159 dst_sel:WORD_1 dst_unused:UNUSED_PRESERVE src0_sel:WORD_1\n"
    "v_exp_f16_sdwa v161, v161 dst_sel:WORD_1 dst_unused:UNUSED_PRESERVE src0_sel:WORD_1\n"
    "s_waitcnt lgkmcnt(7)\n"
    "v_mfma_f32_32x32x16_bf16 v[104:119], v[140:143], v[76:79], v[104:119]\n"
    "ds_read_b64_tr_b16 v[152:153], v2 offset:40000\n"
    "ds_read_b64_tr_b16 v[154:155], v2 offset:41536\n"
    "ds_read_u16 v168, v168 offset:0\n"
    "v_pk_fma_f16 v157, v157, s41, s41\n"
    "v_pk_fma_f16 v159, v159, s41, s41\n"
    "v_pk_fma_f16 v161, v161, s41, s41\n"
    "v_rcp_f16_sdwa v157, v157 dst_sel:WORD_0 dst_unused:UNUSED_PRESERVE src0_sel:WORD_0\n"
    "ds_read_u16_d16_hi v169, v169 offset:0\n"
    "s_waitcnt lgkmcnt(8)\n"
    "v_mfma_f32_32x32x16_bf16 v[16:31], v[144:147], v[132:135], v[16:31]\n"
    "ds_read_b64_tr_b16 v[136:137], v1 offset:41984\n"
    "ds_read_b64_tr_b16 v[138:139], v1 offset:42240\n"
    "v_rcp_f16_sdwa v159, v159 dst_sel:WORD_0 dst_unused:UNUSED_PRESERVE src0_sel:WORD_0\n"
    "v_rcp_f16_sdwa v161, v161 dst_sel:WORD_0 dst_unused:UNUSED_PRESERVE src0_sel:WORD_0\n"
    "ds_read_u16 v170, v170 offset:0\n"
    "s_waitcnt lgkmcnt(8)\n"
    "v_mfma_f32_32x32x16_bf16 v[104:119], v[148:151], v[80:83], v[104:119]\n"
    "ds_read_b64_tr_b16 v[140:141], v2 offset:40064\n"
    "ds_read_b64_tr_b16 v[142:143], v2 offset:41600\n"
    "v_rcp_f16_sdwa v157, v157 dst_sel:WORD_1 dst_unused:UNUSED_PRESERVE src0_sel:WORD_1\n"
    "v_rcp_f16_sdwa v159, v159 dst_sel:WORD_1 dst_unused:UNUSED_PRESERVE src0_sel:WORD_1\n"
    "s_setprio 0\n"
    "ds_read_u16_d16_hi v171, v171 offset:0\n"
    "v_rcp_f16_sdwa v161, v161 dst_sel:WORD_1 dst_unused:UNUSED_PRESERVE src0_sel:WORD_1\n"
    "s_waitcnt lgkmcnt(8)\n"
    "v_mfma_f32_32x32x16_bf16 v[32:47], v[152:155], v[132:135], v[32:47]\n"
    "v_fma_mix_f32 v88, v88, v157, 0 op_sel:[0,0,0] op_sel_hi:[0,1,0]\n"
    "ds_read_u16 v172, v172 offset:0\n"
    "v_fma_mix_f32 v90, v90, v159, 0 op_sel:[0,0,0] op_sel_hi:[0,1,0]\n"
    "v_fma_mix_f32 v92, v92, v161, 0 op_sel:[0,0,0] op_sel_hi:[0,1,0]\n"
    "v_fma_mix_f32 v89, v89, v157, 0 op_sel:[0,1,0] op_sel_hi:[0,1,0]\n"
    "s_waitcnt lgkmcnt(5)\n"
    "v_mfma_f32_32x32x16_bf16 v[104:119], v[136:139], v[84:87], v[104:119]\n"
    "v_fma_mix_f32 v91, v91, v159, 0 op_sel:[0,1,0] op_sel_hi:[0,1,0]\n"
    "ds_read_u16_d16_hi v173, v173 offset:0\n"
    "v_fma_mix_f32 v93, v93, v161, 0 op_sel:[0,1,0] op_sel_hi:[0,1,0]\n"
    "v_cvt_pk_bf16_f32 v120, v88, v89\n"
    "v_cvt_pk_bf16_f32 v121, v90, v91\n"
    "v_cvt_pk_bf16_f32 v122, v92, v93\n"
    "s_waitcnt lgkmcnt(3)\n"
    "v_mfma_f32_32x32x16_bf16 v[48:63], v[140:143], v[132:135], v[48:63]\n"
    "v_or_b32_e32 v123, v164, v165\n"
    "v_or_b32_e32 v124, v166, v167\n"
    "v_or_b32_e32 v125, v168, v169\n"
    "s_waitcnt lgkmcnt(2)\n"
    "v_or_b32_e32 v126, v170, v171\n"
    "s_waitcnt lgkmcnt(0)\n"
    "v_or_b32_e32 v127, v172, v173\n"
    "s_waitcnt vmcnt(3)\n"
    "s_barrier\n"
    "ds_read_b128 v[64:67], v11 offset:0\n"
    "ds_read_b128 v[68:71], v11 offset:32\n"
    "ds_read_b128 v[72:75], v11 offset:64\n"
    "ds_read_b128 v[76:79], v11 offset:96\n"
    "ds_read_b128 v[80:83], v11 offset:128\n"
    "ds_read_b128 v[84:87], v11 offset:160\n"
    "ds_read_b64_tr_b16 v[136:137], v2 offset:0\n"
    "ds_read_b64_tr_b16 v[138:139], v2 offset:1536\n"
    "ds_read_b64_tr_b16 v[140:141], v2 offset:64\n"
    "ds_read_b64_tr_b16 v[142:143], v2 offset:1600\n"
    "ds_read_b64_tr_b16 v[144:145], v2 offset:128\n"
    "ds_read_b64_tr_b16 v[146:147], v2 offset:1664\n"
    "s_setprio 3\n"
    "v_cvt_pk_f16_f32 v165, v110, v111\n"
    "v_cvt_pk_f16_f32 v167, v112, v113\n"
    "v_cvt_pk_f16_f32 v169, v114, v115\n"
    "v_cvt_pk_f16_f32 v171, v116, v117\n"
    "v_cvt_pk_f16_f32 v173, v118, v119\n"
    "s_add_u32 m0, s54, 24576\n"
    "s_mov_b32 s44, s48\n"
    "buffer_load_dwordx4 v6, s[36:39], s44 offen lds\n"
    "s_add_u32 m0, s54, 25600\n"
    "s_add_u32 s44, s44, s47\n"
    "buffer_load_dwordx4 v6, s[36:39], s44 offen lds\n"
    "s_add_u32 m0, s54, 26624\n"
    "s_add_u32 s44, s44, s47\n"
    "buffer_load_dwordx4 v6, s[36:39], s44 offen lds\n"
    "s_add_u32 s48, s48, s49\n"
    "s_cmp_ge_u32 s48, s50\n"
    "s_cselect_b32 s48, s51, s48\n"
    "v_and_b32_e32 v165, s42, v165\n"
    "v_and_b32_e32 v167, s42, v167\n"
    "v_and_b32_e32 v169, s42, v169\n"
    "v_and_b32_e32 v171, s42, v171\n"
    "v_and_b32_e32 v173, s42, v173\n"
    "v_lshrrev_b32_sdwa v164, 2, v165 dst_sel:DWORD dst_unused:UNUSED_PAD src0_sel:DWORD src1_sel:WORD_0\n"
    "v_lshrrev_b32_sdwa v166, 2, v167 dst_sel:DWORD dst_unused:UNUSED_PAD src0_sel:DWORD src1_sel:WORD_0\n"
    "s_waitcnt lgkmcnt(4)\n"
    "v_mfma_f32_32x32x16_bf16 v[16:31], v[136:139], v[120:123], v[16:31]\n"
    "ds_read_b64_tr_b16 v[148:149], v2 offset:3072\n"
    "ds_read_b64_tr_b16 v[150:151], v2 offset:4608\n"
    "v_lshrrev_b32_sdwa v168, 2, v169 dst_sel:DWORD dst_unused:UNUSED_PAD src0_sel:DWORD src1_sel:WORD_0\n"
    "v_lshrrev_b32_sdwa v170, 2, v171 dst_sel:DWORD dst_unused:UNUSED_PAD src0_sel:DWORD src1_sel:WORD_0\n"
    "v_lshrrev_b32_sdwa v172, 2, v173 dst_sel:DWORD dst_unused:UNUSED_PAD src0_sel:DWORD src1_sel:WORD_0\n"
    "v_lshrrev_b32_e32 v165, 18, v165\n"
    "v_lshrrev_b32_e32 v167, 18, v167\n"
    "v_lshrrev_b32_e32 v169, 18, v169\n"
    "s_setprio 2\n"
    "v_lshrrev_b32_e32 v171, 18, v171\n"
    "v_lshrrev_b32_e32 v173, 18, v173\n"
    "v_cvt_pk_f16_f32 v156, v104, v105\n"
    "v_cvt_pk_f16_f32 v158, v106, v107\n"
    "s_waitcnt lgkmcnt(4)\n"
    "v_mfma_f32_32x32x16_bf16 v[32:47], v[140:143], v[120:123], v[32:47]\n"
    "ds_read_b64_tr_b16 v[152:153], v2 offset:3136\n"
    "ds_read_b64_tr_b16 v[154:155], v2 offset:4672\n"
    "v_cvt_pk_f16_f32 v160, v108, v109\n"
    "v_pk_fma_f16 v157, v156, v156, s40\n"
    "v_pk_fma_f16 v159, v158, v158, s40\n"
    "ds_read_u16 v164, v164 offset:0\n"
    "v_pk_fma_f16 v161, v160, v160, s40\n"
    "v_pk_mul_f16 v157, v157, v156 neg_lo:[1,0] neg_hi:[1,0]\n"
    "v_pk_mul_f16 v159, v159, v158 neg_lo:[1,0] neg_hi:[1,0]\n"
    "v_pk_mul_f16 v161, v161, v160 neg_lo:[1,0] neg_hi:[1,0]\n"
    "ds_read_u16_d16_hi v165, v165 offset:0\n"
    "v_exp_f16_sdwa v157, v157 dst_sel:WORD_0 dst_unused:UNUSED_PRESERVE src0_sel:WORD_0\n"
    "v_exp_f16_sdwa v159, v159 dst_sel:WORD_0 dst_unused:UNUSED_PRESERVE src0_sel:WORD_0\n"
    "s_waitcnt lgkmcnt(6)\n"
    "v_mfma_f32_32x32x16_bf16 v[48:63], v[144:147], v[120:123], v[48:63]\n"
    "ds_read_u16 v166, v166 offset:0\n"
    "ds_read_b64_tr_b16 v[136:137], v2 offset:3200\n"
    "ds_read_b64_tr_b16 v[138:139], v2 offset:4736\n"
    "v_exp_f16_sdwa v161, v161 dst_sel:WORD_0 dst_unused:UNUSED_PRESERVE src0_sel:WORD_0\n"
    "s_setprio 1\n"
    "v_exp_f16_sdwa v157, v157 dst_sel:WORD_1 dst_unused:UNUSED_PRESERVE src0_sel:WORD_1\n"
    "ds_read_u16_d16_hi v167, v167 offset:0\n"
    "v_exp_f16_sdwa v159, v159 dst_sel:WORD_1 dst_unused:UNUSED_PRESERVE src0_sel:WORD_1\n"
    "v_exp_f16_sdwa v161, v161 dst_sel:WORD_1 dst_unused:UNUSED_PRESERVE src0_sel:WORD_1\n"
    "ds_read_u16 v168, v168 offset:0\n"
    "v_pk_fma_f16 v157, v157, s41, s41\n"
    "v_pk_fma_f16 v159, v159, s41, s41\n"
    "s_waitcnt lgkmcnt(9)\n"
    "v_mfma_f32_32x32x16_bf16 v[16:31], v[148:151], v[124:127], v[16:31]\n"
    "v_pk_fma_f16 v161, v161, s41, s41\n"
    "v_rcp_f16_sdwa v157, v157 dst_sel:WORD_0 dst_unused:UNUSED_PRESERVE src0_sel:WORD_0\n"
    "ds_read_u16_d16_hi v169, v169 offset:0\n"
    "v_rcp_f16_sdwa v159, v159 dst_sel:WORD_0 dst_unused:UNUSED_PRESERVE src0_sel:WORD_0\n"
    "v_rcp_f16_sdwa v161, v161 dst_sel:WORD_0 dst_unused:UNUSED_PRESERVE src0_sel:WORD_0\n"
    "ds_read_u16 v170, v170 offset:0\n"
    "v_rcp_f16_sdwa v157, v157 dst_sel:WORD_1 dst_unused:UNUSED_PRESERVE src0_sel:WORD_1\n"
    "v_rcp_f16_sdwa v159, v159 dst_sel:WORD_1 dst_unused:UNUSED_PRESERVE src0_sel:WORD_1\n"
    "s_setprio 0\n"
    "ds_read_u16_d16_hi v171, v171 offset:0\n"
    "s_waitcnt lgkmcnt(10)\n"
    "v_mfma_f32_32x32x16_bf16 v[32:47], v[152:155], v[124:127], v[32:47]\n"
    "v_rcp_f16_sdwa v161, v161 dst_sel:WORD_1 dst_unused:UNUSED_PRESERVE src0_sel:WORD_1\n"
    "v_fma_mix_f32 v104, v104, v157, 0 op_sel:[0,0,0] op_sel_hi:[0,1,0]\n"
    "ds_read_u16 v172, v172 offset:0\n"
    "v_fma_mix_f32 v106, v106, v159, 0 op_sel:[0,0,0] op_sel_hi:[0,1,0]\n"
    "v_fma_mix_f32 v108, v108, v161, 0 op_sel:[0,0,0] op_sel_hi:[0,1,0]\n"
    "v_fma_mix_f32 v105, v105, v157, 0 op_sel:[0,1,0] op_sel_hi:[0,1,0]\n"
    "v_fma_mix_f32 v107, v107, v159, 0 op_sel:[0,1,0] op_sel_hi:[0,1,0]\n"
    "ds_read_u16_d16_hi v173, v173 offset:0\n"
    "v_fma_mix_f32 v109, v109, v161, 0 op_sel:[0,1,0] op_sel_hi:[0,1,0]\n"
    "v_cvt_pk_bf16_f32 v128, v104, v105\n"
    "v_cvt_pk_bf16_f32 v129, v106, v107\n"
    "v_cvt_pk_bf16_f32 v130, v108, v109\n"
    "s_waitcnt lgkmcnt(7)\n"
    "v_mfma_f32_32x32x16_bf16 v[48:63], v[136:139], v[124:127], v[48:63]\n"
    "v_or_b32_e32 v131, v164, v165\n"
    "s_waitcnt lgkmcnt(6)\n"
    "v_or_b32_e32 v132, v166, v167\n"
    "s_waitcnt lgkmcnt(4)\n"
    "v_or_b32_e32 v133, v168, v169\n"
    "s_waitcnt lgkmcnt(2)\n"
    "v_or_b32_e32 v134, v170, v171\n"
    "s_waitcnt lgkmcnt(0)\n"
    "v_or_b32_e32 v135, v172, v173\n"
    "s_waitcnt vmcnt(3)\n"
    "s_nop 0\n"
    "s_barrier\n"
    "s_add_u32 s57, s57, 1\n"
    "s_cmp_ge_u32 s57, 4\n"
    "s_cbranch_scc1 L_tailW_%=\n"
    "s_setprio 3\n"
    "ds_read_b64_tr_b16 v[136:137], v2 offset:12288\n"
    "ds_read_b64_tr_b16 v[138:139], v2 offset:13824\n"
    "ds_read_b64_tr_b16 v[140:141], v2 offset:12352\n"
    "ds_read_b64_tr_b16 v[142:143], v2 offset:13888\n"
    "ds_read_b64_tr_b16 v[144:145], v2 offset:12416\n"
    "ds_read_b64_tr_b16 v[146:147], v2 offset:13952\n"
    "ds_read_b64_tr_b16 v[148:149], v2 offset:15360\n"
    "ds_read_b64_tr_b16 v[150:151], v2 offset:16896\n"
    "ds_read_b64_tr_b16 v[152:153], v2 offset:15424\n"
    "ds_read_b64_tr_b16 v[154:155], v2 offset:16960\n"
    "ds_read_b64 v[104:105], v7 offset:0\n"
    "ds_read_b64 v[106:107], v7 offset:192\n"
    "ds_read_b64 v[108:109], v7 offset:7056\n"
    "ds_read_b64 v[110:111], v7 offset:7248\n"
    "s_waitcnt lgkmcnt(12)\n"
    "v_mfma_f32_32x32x16_bf16 v[16:31], v[136:139], v[128:131], v[16:31]\n"
    "ds_read_b64_tr_b16 v[136:137], v2 offset:15488\n"
    "ds_read_b64_tr_b16 v[138:139], v2 offset:17024\n"
    "ds_read_b64 v[112:113], v7 offset:14112\n"
    "s_waitcnt lgkmcnt(14)\n"
    "ds_read_b64 v[114:115], v7 offset:14304\n"
    "s_waitcnt lgkmcnt(14)\n"
    "ds_read_b64 v[116:117], v7 offset:21168\n"
    "s_waitcnt lgkmcnt(14)\n"
    "ds_read_b64 v[118:119], v7 offset:21360\n"
    "s_waitcnt lgkmcnt(14)\n"
    "ds_read_b64 v[120:121], v7 offset:28224\n"
    "s_waitcnt lgkmcnt(14)\n"
    "ds_read_b64 v[122:123], v7 offset:28416\n"
    "s_waitcnt lgkmcnt(14)\n"
    "ds_read_b64 v[124:125], v7 offset:35280\n"
    "s_waitcnt lgkmcnt(14)\n"
    "ds_read_b64 v[126:127], v7 offset:35472\n"
    "v_mfma_f32_32x32x16_bf16 v[32:47], v[140:143], v[128:131], v[32:47]\n"
    "s_waitcnt lgkmcnt(12)\n"
    "v_mfma_f32_16x16x32_bf16 v[88:91], v[180:183], v[104:107], 0\n"
    "ds_read_b64 v[248:249], v7 offset:42336\n"
    "ds_read_b64 v[250:251], v7 offset:42528\n"
    "v_mfma_f32_32x32x16_bf16 v[48:63], v[144:147], v[128:131], v[48:63]\n"
    "s_waitcnt lgkmcnt(12)\n"
    "v_mfma_f32_16x16x32_bf16 v[88:91], v[184:187], v[108:111], v[88:91]\n"
    "v_mfma_f32_32x32x16_bf16 v[16:31], v[148:151], v[132:135], v[16:31]\n"
    "v_mfma_f32_16x16x32_bf16 v[92:95], v[180:183], v[108:111], 0\n"
    "ds_read_b64 v[252:253], v7 offset:49392\n"
    "ds_read_b64 v[254:255], v7 offset:49584\n"
    "v_mfma_f32_32x32x16_bf16 v[32:47], v[152:155], v[132:135], v[32:47]\n"
    "s_waitcnt lgkmcnt(10)\n"
    "v_mfma_f32_16x16x32_bf16 v[88:91], v[188:191], v[112:115], v[88:91]\n"
    "v_mfma_f32_32x32x16_bf16 v[48:63], v[136:139], v[132:135], v[48:63]\n"
    "v_mfma_f32_16x16x32_bf16 v[92:95], v[184:187], v[112:115], v[92:95]\n"
    "v_mfma_f32_16x16x32_bf16 v[96:99], v[180:183], v[112:115], 0\n"
    "ds_read_b64 v[136:137], v7 offset:8\n"
    "ds_read_b64 v[138:139], v7 offset:200\n"
    "s_waitcnt lgkmcnt(10)\n"
    "v_mfma_f32_16x16x32_bf16 v[88:91], v[192:195], v[116:119], v[88:91]\n"
    "v_mfma_f32_16x16x32_bf16 v[92:95], v[188:191], v[116:119], v[92:95]\n"
    "v_cmp_u_f32 vcc, v16, v16\n"
    "v_cvt_pk_bf16_f32 v156, v16, v17\n"
    "v_cvt_pk_bf16_f32 v157, v18, v19\n"
    "s_nop 1\n"
    "s_or_b64 s[70:71], s[70:71], vcc\n"
    "v_cvt_pk_bf16_f32 v158, v20, v21\n"
    "v_cvt_pk_bf16_f32 v159, v22, v23\n"
    "v_cvt_pk_bf16_f32 v160, v24, v25\n"
    "v_cvt_pk_bf16_f32 v161, v26, v27\n"
    "v_cvt_pk_bf16_f32 v162, v28, v29\n"
    "v_cvt_pk_bf16_f32 v163, v30, v31\n"
    "v_permlane16_swap_b32 v156, v160\n"
    "v_permlane16_swap_b32 v157, v161\n"
    "v_permlane16_swap_b32 v158, v162\n"
    "v_permlane16_swap_b32 v159, v163\n"
    "v_mfma_f32_16x16x32_bf16 v[96:99], v[184:187], v[116:119], v[96:99]\n"
    "v_cvt_pk_bf16_f32 v164, v32, v33\n"
    "v_cvt_pk_bf16_f32 v165, v34, v35\n"
    "v_cvt_pk_bf16_f32 v166, v36, v37\n"
    "v_cvt_pk_bf16_f32 v167, v38, v39\n"
    "v_cvt_pk_bf16_f32 v168, v40, v41\n"
    "v_cvt_pk_bf16_f32 v169, v42, v43\n"
    "v_cvt_pk_bf16_f32 v170, v44, v45\n"
    "v_cvt_pk_bf16_f32 v171, v46, v47\n"
    "v_permlane16_swap_b32 v164, v168\n"
    "v_permlane16_swap_b32 v165, v169\n"
    "v_permlane16_swap_b32 v166, v170\n"
    "v_permlane16_swap_b32 v167, v171\n"
    "v_lshlrev_b32 v16, 16, v64\n"
    "v_and_b32 v17, 0xffff0000, v64\n"
    "v_lshlrev_b32 v18, 16, v65\n"
    "v_and_b32 v19, 0xffff0000, v65\n"
    "v_lshlrev_b32 v20, 16, v66\n"
    "v_and_b32 v21, 0xffff0000, v66\n"
    "v_lshlrev_b32 v22, 16, v67\n"
    "v_and_b32 v23, 0xffff0000, v67\n"
    "v_lshlrev_b32 v24, 16, v68\n"
    "v_and_b32 v25, 0xffff0000, v68\n"
    "v_lshlrev_b32 v26, 16, v69\n"
    "v_and_b32 v27, 0xffff0000, v69\n"
    "v_lshlrev_b32 v28, 16, v70\n"
    "v_and_b32 v29, 0xffff0000, v70\n"
    "v_lshlrev_b32 v30, 16, v71\n"
    "v_and_b32 v31, 0xffff0000, v71\n"
    "v_mfma_f32_16x16x32_bf16 v[100:103], v[180:183], v[116:119], 0\n"
    "ds_read_b64 v[140:141], v7 offset:7064\n"
    "ds_read_b64 v[142:143], v7 offset:7256\n"
    "v_cvt_pk_bf16_f32 v172, v48, v49\n"
    "v_cvt_pk_bf16_f32 v173, v50, v51\n"
    "v_cvt_pk_bf16_f32 v174, v52, v53\n"
    "v_cvt_pk_bf16_f32 v175, v54, v55\n"
    "v_cvt_pk_bf16_f32 v176, v56, v57\n"
    "v_cvt_pk_bf16_f32 v177, v58, v59\n"
    "v_cvt_pk_bf16_f32 v178, v60, v61\n"
    "v_cvt_pk_bf16_f32 v179, v62, v63\n"
    "v_permlane16_swap_b32 v172, v176\n"
    "v_permlane16_swap_b32 v173, v177\n"
    "v_permlane16_swap_b32 v174, v178\n"
    "v_permlane16_swap_b32 v175, v179\n"
    "v_lshlrev_b32 v32, 16, v72\n"
    "v_and_b32 v33, 0xffff0000, v72\n"
    "v_lshlrev_b32 v34, 16, v73\n"
    "v_and_b32 v35, 0xffff0000, v73\n"
    "v_lshlrev_b32 v36, 16, v74\n"
    "v_and_b32 v37, 0xffff0000, v74\n"
    "v_lshlrev_b32 v38, 16, v75\n"
    "v_and_b32 v39, 0xffff0000, v75\n"
    "v_lshlrev_b32 v40, 16, v76\n"
    "v_and_b32 v41, 0xffff0000, v76\n"
    "v_lshlrev_b32 v42, 16, v77\n"
    "v_and_b32 v43, 0xffff0000, v77\n"
    "v_lshlrev_b32 v44, 16, v78\n"
    "v_and_b32 v45, 0xffff0000, v78\n"
    "v_lshlrev_b32 v46, 16, v79\n"
    "v_and_b32 v47, 0xffff0000, v79\n"
    "s_waitcnt lgkmcnt(10)\n"
    "v_mfma_f32_16x16x32_bf16 v[88:91], v[196:199], v[120:123], v[88:91]\n"
    "v_lshlrev_b32 v48, 16, v80\n"
    "v_and_b32 v49, 0xffff0000, v80\n"
    "v_lshlrev_b32 v50, 16, v81\n"
    "v_and_b32 v51, 0xffff0000, v81\n"
    "v_lshlrev_b32 v52, 16, v82\n"
    "v_and_b32 v53, 0xffff0000, v82\n"
    "v_lshlrev_b32 v54, 16, v83\n"
    "v_and_b32 v55, 0xffff0000, v83\n"
    "v_lshlrev_b32 v56, 16, v84\n"
    "v_and_b32 v57, 0xffff0000, v84\n"
    "v_lshlrev_b32 v58, 16, v85\n"
    "v_and_b32 v59, 0xffff0000, v85\n"
    "v_lshlrev_b32 v60, 16, v86\n"
    "v_and_b32 v61, 0xffff0000, v86\n"
    "v_lshlrev_b32 v62, 16, v87\n"
    "v_and_b32 v63, 0xffff0000, v87\n"
    "v_mfma_f32_16x16x32_bf16 v[92:95], v[192:195], v[120:123], v[92:95]\n"
    "v_mfma_f32_16x16x32_bf16 v[96:99], v[188:191], v[120:123], v[96:99]\n"
    "buffer_store_dwordx4 v[156:159], v12, s[32:35], s60 offen offset:0 nt\n"
    "v_mfma_f32_16x16x32_bf16 v[100:103], v[184:187], v[120:123], v[100:103]\n"
    "ds_read_b64 v[144:145], v7 offset:14120\n"
    "ds_read_b64 v[146:147], v7 offset:14312\n"
    "s_waitcnt lgkmcnt(10)\n"
    "v_mfma_f32_16x16x32_bf16 v[92:95], v[196:199], v[124:127], v[92:95]\n"
    "s_setprio 2\n"
    "v_mfma_f32_16x16x32_bf16 v[96:99], v[192:195], v[124:127], v[96:99]\n"
    "v_mfma_f32_16x16x32_bf16 v[100:103], v[188:191], v[124:127], v[100:103]\n"
    "v_cvt_pk_bf16_f32 v64, v88, v89\n"
    "v_cvt_pk_bf16_f32 v65, v90, v91\n"
    "ds_read_b64 v[148:149], v7 offset:21176\n"
    "ds_read_b64 v[150:151], v7 offset:21368\n"
    "s_waitcnt lgkmcnt(10)\n"
    "v_mfma_f32_16x16x32_bf16 v[96:99], v[196:199], v[248:251], v[96:99]\n"
    "v_mfma_f32_16x16x32_bf16 v[100:103], v[192:195], v[248:251], v[100:103]\n"
    "ds_read_b64 v[152:153], v7 offset:28232\n"
    "ds_read_b64 v[154:155], v7 offset:28424\n"
    "buffer_store_dwordx4 v[160:163], v10, s[32:35], s60 offen offset:0 nt\n"
    "s_waitcnt lgkmcnt(10)\n"
    "v_mfma_f32_16x16x32_bf16 v[100:103], v[196:199], v[252:255], v[100:103]\n"
    "v_cvt_pk_bf16_f32 v66, v92, v93\n"
    "v_cvt_pk_bf16_f32 v67, v94, v95\n"
    "ds_read_b64 v[128:129], v7 offset:35288\n"
    "ds_read_b64 v[130:131], v7 offset:35480\n"
    "s_waitcnt lgkmcnt(10)\n"
    "v_mfma_f32_16x16x32_bf16 v[104:107], v[200:203], v[136:139], 0\n"
    "ds_read_b64 v[132:133], v7 offset:42344\n"
    "ds_read_b64 v[134:135], v7 offset:42536\n"
    "s_waitcnt lgkmcnt(10)\n"
    "v_mfma_f32_16x16x32_bf16 v[104:107], v[204:207], v[140:143], v[104:107]\n"
    "v_cvt_pk_bf16_f32 v68, v96, v97\n"
    "v_cvt_pk_bf16_f32 v69, v98, v99\n"
    "v_mfma_f32_16x16x32_bf16 v[108:111], v[200:203], v[140:143], 0\n"
    "v_cvt_pk_bf16_f32 v70, v100, v101\n"
    "v_cvt_pk_bf16_f32 v71, v102, v103\n"
    "ds_read_b64 v[136:137], v7 offset:49400\n"
    "ds_read_b64 v[138:139], v7 offset:49592\n"
    "s_waitcnt lgkmcnt(10)\n"
    "v_mfma_f32_16x16x32_bf16 v[104:107], v[208:211], v[144:147], v[104:107]\n"
    "v_mfma_f32_16x16x32_bf16 v[108:111], v[204:207], v[144:147], v[108:111]\n"
    "buffer_store_dwordx4 v[164:167], v12, s[32:35], s60 offen offset:64 nt\n"
    "ds_write_b64 v9, v[64:65] offset:0\n"
    "v_mfma_f32_16x16x32_bf16 v[112:115], v[200:203], v[144:147], 0\n"
    "ds_read_b64 v[140:141], v7 offset:16\n"
    "ds_read_b64 v[142:143], v7 offset:208\n"
    "s_waitcnt lgkmcnt(11)\n"
    "v_mfma_f32_16x16x32_bf16 v[104:107], v[212:215], v[148:151], v[104:107]\n"
    "ds_write_b64 v9, v[66:67] offset:7056\n"
    "v_mfma_f32_16x16x32_bf16 v[108:111], v[208:211], v[148:151], v[108:111]\n"
    "v_mfma_f32_16x16x32_bf16 v[112:115], v[204:207], v[148:151], v[112:115]\n"
    "ds_write_b64 v9, v[68:69] offset:14112\n"
    "v_mfma_f32_16x16x32_bf16 v[116:119], v[200:203], v[148:151], 0\n"
    "ds_read_b64 v[144:145], v7 offset:7072\n"
    "ds_read_b64 v[146:147], v7 offset:7264\n"
    "s_setprio 1\n"
    "s_waitcnt lgkmcnt(13)\n"
    "v_mfma_f32_16x16x32_bf16 v[104:107], v[216:219], v[152:155], v[104:107]\n"
    "buffer_store_dwordx4 v[168:171], v10, s[32:35], s60 offen offset:64 nt\n"
    "ds_write_b64 v9, v[70:71] offset:21168\n"
    "v_mfma_f32_16x16x32_bf16 v[108:111], v[212:215], v[152:155], v[108:111]\n"
    "v_mfma_f32_16x16x32_bf16 v[112:115], v[208:211], v[152:155], v[112:115]\n"
    "v_mfma_f32_16x16x32_bf16 v[116:119], v[204:207], v[152:155], v[116:119]\n"
    "ds_read_b64 v[148:149], v7 offset:14128\n"
    "s_waitcnt lgkmcnt(14)\n"
    "ds_read_b64 v[150:151], v7 offset:14320\n"
    "s_waitcnt lgkmcnt(14)\n"
    "v_mfma_f32_16x16x32_bf16 v[108:111], v[216:219], v[128:131], v[108:111]\n"
    "v_mfma_f32_16x16x32_bf16 v[112:115], v[212:215], v[128:131], v[112:115]\n"
    "v_mfma_f32_16x16x32_bf16 v[116:119], v[208:211], v[128:131], v[116:119]\n"
    "v_cvt_pk_bf16_f32 v72, v104, v105\n"
    "v_cvt_pk_bf16_f32 v73, v106, v107\n"
    "ds_read_b64 v[152:153], v7 offset:21184\n"
    "s_waitcnt lgkmcnt(14)\n"
    "ds_read_b64 v[154:155], v7 offset:21376\n"
    "buffer_store_dwordx4 v[172:175], v12, s[32:35], s60 offen offset:128 nt\n"
    "s_waitcnt lgkmcnt(14)\n"
    "v_mfma_f32_16x16x32_bf16 v[112:115], v[216:219], v[132:135], v[112:115]\n"
    "v_mfma_f32_16x16x32_bf16 v[116:119], v[212:215], v[132:135], v[116:119]\n"
    "ds_read_b64 v[128:129], v7 offset:28240\n"
    "s_waitcnt lgkmcnt(14)\n"
    "ds_read_b64 v[130:131], v7 offset:28432\n"
    "s_waitcnt lgkmcnt(14)\n"
    "v_mfma_f32_16x16x32_bf16 v[116:119], v[216:219], v[136:139], v[116:119]\n"
    "v_cvt_pk_bf16_f32 v74, v108, v109\n"
    "v_cvt_pk_bf16_f32 v75, v110, v111\n"
    "ds_read_b64 v[132:133], v7 offset:35296\n"
    "s_waitcnt lgkmcnt(14)\n"
    "ds_read_b64 v[134:135], v7 offset:35488\n"
    "s_waitcnt lgkmcnt(13)\n"
    "v_mfma_f32_16x16x32_bf16 v[88:91], v[220:223], v[140:143], 0\n"
    "ds_read_b64 v[136:137], v7 offset:42352\n"
    "ds_read_b64 v[138:139], v7 offset:42544\n"
    "s_waitcnt lgkmcnt(11)\n"
    "v_mfma_f32_16x16x32_bf16 v[88:91], v[224:227], v[144:147], v[88:91]\n"
    "v_cvt_pk_bf16_f32 v76, v112, v113\n"
    "v_cvt_pk_bf16_f32 v77, v114, v115\n"
    "v_mfma_f32_16x16x32_bf16 v[92:95], v[220:223], v[144:147], 0\n"
    "v_cvt_pk_bf16_f32 v78, v116, v117\n"
    "v_cvt_pk_bf16_f32 v79, v118, v119\n"
    "ds_read_b64 v[140:141], v7 offset:49408\n"
    "ds_read_b64 v[142:143], v7 offset:49600\n"
    "buffer_store_dwordx4 v[176:179], v10, s[32:35], s60 offen offset:128 nt\n"
    "s_waitcnt lgkmcnt(10)\n"
    "v_mfma_f32_16x16x32_bf16 v[88:91], v[228:231], v[148:151], v[88:91]\n"
    "v_mfma_f32_16x16x32_bf16 v[92:95], v[224:227], v[148:151], v[92:95]\n"
    "ds_write_b64 v9, v[72:73] offset:8\n"
    "s_setprio 0\n"
    "v_mfma_f32_16x16x32_bf16 v[96:99], v[220:223], v[148:151], 0\n"
    "s_waitcnt lgkmcnt(9)\n"
    "v_mfma_f32_16x16x32_bf16 v[88:91], v[232:235], v[152:155], v[88:91]\n"
    "ds_write_b64 v9, v[74:75] offset:7064\n"
    "v_mfma_f32_16x16x32_bf16 v[92:95], v[228:231], v[152:155], v[92:95]\n"
    "v_mfma_f32_16x16x32_bf16 v[96:99], v[224:227], v[152:155], v[96:99]\n"
    "ds_write_b64 v9, v[76:77] offset:14120\n"
    "v_mfma_f32_16x16x32_bf16 v[100:103], v[220:223], v[152:155], 0\n"
    "s_waitcnt lgkmcnt(9)\n"
    "v_mfma_f32_16x16x32_bf16 v[88:91], v[236:239], v[128:131], v[88:91]\n"
    "ds_write_b64 v9, v[78:79] offset:21176\n"
    "v_mfma_f32_16x16x32_bf16 v[92:95], v[232:235], v[128:131], v[92:95]\n"
    "v_mfma_f32_16x16x32_bf16 v[96:99], v[228:231], v[128:131], v[96:99]\n"
    "v_mfma_f32_16x16x32_bf16 v[100:103], v[224:227], v[128:131], v[100:103]\n"
    "s_waitcnt lgkmcnt(8)\n"
    "v_mfma_f32_16x16x32_bf16 v[92:95], v[236:239], v[132:135], v[92:95]\n"
    "v_mfma_f32_16x16x32_bf16 v[96:99], v[232:235], v[132:135], v[96:99]\n"
    "v_mfma_f32_16x16x32_bf16 v[100:103], v[228:231], v[132:135], v[100:103]\n"
    "v_cvt_pk_bf16_f32 v80, v88, v89\n"
    "v_cvt_pk_bf16_f32 v81, v90, v91\n"
    "s_waitcnt lgkmcnt(6)\n"
    "v_mfma_f32_16x16x32_bf16 v[96:99], v[236:239], v[136:139], v[96:99]\n"
    "v_mfma_f32_16x16x32_bf16 v[100:103], v[232:235], v[136:139], v[100:103]\n"
    "s_waitcnt lgkmcnt(4)\n"
    "v_mfma_f32_16x16x32_bf16 v[100:103], v[236:239], v[140:143], v[100:103]\n"
    "v_cvt_pk_bf16_f32 v82, v92, v93\n"
    "v_cvt_pk_bf16_f32 v83, v94, v95\n"
    "s_nop 2\n"
    "v_cvt_pk_bf16_f32 v84, v96, v97\n"
    "v_cvt_pk_bf16_f32 v85, v98, v99\n"
    "s_nop 0\n"
    "v_cvt_pk_bf16_f32 v86, v100, v101\n"
    "v_cvt_pk_bf16_f32 v87, v102, v103\n"
    "ds_write_b64 v9, v[80:81] offset:16\n"
    "ds_write_b64 v9, v[82:83] offset:7072\n"
    "ds_write_b64 v9, v[84:85] offset:14128\n"
    "ds_write_b64 v9, v[86:87] offset:21184\n"
    "s_branch L_loopW_%=\n"
    "s_waitcnt lgkmcnt(0)\n"
    "L_tailW_%=:\n"
    "ds_read_b64_tr_b16 v[136:137], v2 offset:12288\n"
    "ds_read_b64_tr_b16 v[138:139], v2 offset:13824\n"
    "ds_read_b64_tr_b16 v[140:141], v2 offset:12352\n"
    "ds_read_b64_tr_b16 v[142:143], v2 offset:13888\n"
    "ds_read_b64_tr_b16 v[144:145], v2 offset:12416\n"
    "ds_read_b64_tr_b16 v[146:147], v2 offset:13952\n"
    "ds_read_b64_tr_b16 v[148:149], v2 offset:15360\n"
    "ds_read_b64_tr_b16 v[150:151], v2 offset:16896\n"
    "ds_read_b64_tr_b16 v[152:153], v2 offset:15424\n"
    "ds_read_b64_tr_b16 v[154:155], v2 offset:16960\n"
    "s_waitcnt lgkmcnt(8)\n"
    "v_mfma_f32_32x32x16_bf16 v[16:31], v[136:139], v[128:131], v[16:31]\n"
    "ds_read_b64_tr_b16 v[136:137], v2 offset:15488\n"
    "ds_read_b64_tr_b16 v[138:139], v2 offset:17024\n"
    "s_waitcnt lgkmcnt(8)\n"
    "v_mfma_f32_32x32x16_bf16 v[32:47], v[140:143], v[128:131], v[32:47]\n"
    "s_waitcnt lgkmcnt(6)\n"
    "v_mfma_f32_32x32x16_bf16 v[48:63], v[144:147], v[128:131], v[48:63]\n"
    "s_waitcnt lgkmcnt(4)\n"
    "v_mfma_f32_32x32x16_bf16 v[16:31], v[148:151], v[132:135], v[16:31]\n"
    "s_waitcnt lgkmcnt(2)\n"
    "v_mfma_f32_32x32x16_bf16 v[32:47], v[152:155], v[132:135], v[32:47]\n"
    "s_waitcnt lgkmcnt(0)\n"
    "v_mfma_f32_32x32x16_bf16 v[48:63], v[136:139], v[132:135], v[48:63]\n"
    "s_nop 7\n"
    "v_cmp_u_f32 vcc, v16, v16\n"
    "v_cvt_pk_bf16_f32 v156, v16, v17\n"
    "v_cvt_pk_bf16_f32 v157, v18, v19\n"
    "v_cvt_pk_bf16_f32 v158, v20, v21\n"
    "v_cvt_pk_bf16_f32 v159, v22, v23\n"
    "v_cvt_pk_bf16_f32 v160, v24, v25\n"
    "v_cvt_pk_bf16_f32 v161, v26, v27\n"
    "v_cvt_pk_bf16_f32 v162, v28, v29\n"
    "v_cvt_pk_bf16_f32 v163, v30, v31\n"
    "v_permlane16_swap_b32 v156, v160\n"
    "v_permlane16_swap_b32 v157, v161\n"
    "v_permlane16_swap_b32 v158, v162\n"
    "v_permlane16_swap_b32 v159, v163\n"
    "s_or_b64 s[70:71], s[70:71], vcc\n"
    "buffer_store_dwordx4 v[156:159], v12, s[32:35], s60 offen offset:0 nt\n"
    "buffer_store_dwordx4 v[160:163], v10, s[32:35], s60 offen offset:0 nt\n"
    "v_cvt_pk_bf16_f32 v164, v32, v33\n"
    "v_cvt_pk_bf16_f32 v165, v34, v35\n"
    "v_cvt_pk_bf16_f32 v166, v36, v37\n"
    "v_cvt_pk_bf16_f32 v167, v38, v39\n"
    "v_cvt_pk_bf16_f32 v168, v40, v41\n"
    "v_cvt_pk_bf16_f32 v169, v42, v43\n"
    "v_cvt_pk_bf16_f32 v170, v44, v45\n"
    "v_cvt_pk_bf16_f32 v171, v46, v47\n"
    "v_permlane16_swap_b32 v164, v168\n"
    "v_permlane16_swap_b32 v165, v169\n"
    "v_permlane16_swap_b32 v166, v170\n"
    "v_permlane16_swap_b32 v167, v171\n"
    "buffer_store_dwordx4 v[164:167], v12, s[32:35], s60 offen offset:64 nt\n"
    "buffer_store_dwordx4 v[168:171], v10, s[32:35], s60 offen offset:64 nt\n"
    "v_cvt_pk_bf16_f32 v172, v48, v49\n"
    "v_cvt_pk_bf16_f32 v173, v50, v51\n"
    "v_cvt_pk_bf16_f32 v174, v52, v53\n"
    "v_cvt_pk_bf16_f32 v175, v54, v55\n"
    "v_cvt_pk_bf16_f32 v176, v56, v57\n"
    "v_cvt_pk_bf16_f32 v177, v58, v59\n"
    "v_cvt_pk_bf16_f32 v178, v60, v61\n"
    "v_cvt_pk_bf16_f32 v179, v62, v63\n"
    "v_permlane16_swap_b32 v172, v176\n"
    "v_permlane16_swap_b32 v173, v177\n"
    "v_permlane16_swap_b32 v174, v178\n"
    "v_permlane16_swap_b32 v175, v179\n"
    "buffer_store_dwordx4 v[172:175], v12, s[32:35], s60 offen offset:128 nt\n"
    "buffer_store_dwordx4 v[176:179], v10, s[32:35], s60 offen offset:128 nt\n"
    "s_branch L_done_%=\n"
    "L_roleX_%=:\n"
    "s_setprio 3\n"
    "ds_read_b128 v[64:67], v11 offset:0\n"
    "ds_read_b128 v[68:71], v11 offset:32\n"
    "ds_read_b128 v[72:75], v11 offset:64\n"
    "ds_read_b128 v[76:79], v11 offset:96\n"
    "ds_read_b128 v[80:83], v11 offset:128\n"
    "ds_read_b128 v[84:87], v11 offset:160\n"
    "s_waitcnt lgkmcnt(0)\n"
    "s_barrier\n"
    "ds_read_b64 v[104:105], v7 offset:0\n"
    "ds_read_b64 v[106:107], v7 offset:192\n"
    "ds_read_b64 v[108:109], v7 offset:7056\n"
    "ds_read_b64 v[110:111], v7 offset:7248\n"
    "ds_read_b64 v[112:113], v7 offset:14112\n"
    "ds_read_b64 v[114:115], v7 offset:14304\n"
    "ds_read_b64 v[116:117], v7 offset:21168\n"
    "ds_read_b64 v[118:119], v7 offset:21360\n"
    "ds_read_b64 v[120:121], v7 offset:28224\n"
    "ds_read_b64 v[122:123], v7 offset:28416\n"
    "ds_read_b64 v[124:125], v7 offset:35280\n"
    "ds_read_b64 v[126:127], v7 offset:35472\n"
    "v_lshlrev_b32 v16, 16, v64\n"
    "v_and_b32 v17, 0xffff0000, v64\n"
    "v_lshlrev_b32 v18, 16, v65\n"
    "v_and_b32 v19, 0xffff0000, v65\n"
    "v_lshlrev_b32 v20, 16, v66\n"
    "v_and_b32 v21, 0xffff0000, v66\n"
    "v_lshlrev_b32 v22, 16, v67\n"
    "v_and_b32 v23, 0xffff0000, v67\n"
    "v_lshlrev_b32 v24, 16, v68\n"
    "v_and_b32 v25, 0xffff0000, v68\n"
    "v_lshlrev_b32 v26, 16, v69\n"
    "v_and_b32 v27, 0xffff0000, v69\n"
    "v_lshlrev_b32 v28, 16, v70\n"
    "v_and_b32 v29, 0xffff0000, v70\n"
    "v_lshlrev_b32 v30, 16, v71\n"
    "v_and_b32 v31, 0xffff0000, v71\n"
    "v_lshlrev_b32 v32, 16, v72\n"
    "v_and_b32 v33, 0xffff0000, v72\n"
    "v_lshlrev_b32 v34, 16, v73\n"
    "v_and_b32 v35, 0xffff0000, v73\n"
    "v_lshlrev_b32 v36, 16, v74\n"
    "v_and_b32 v37, 0xffff0000, v74\n"
    "v_lshlrev_b32 v38, 16, v75\n"
    "v_and_b32 v39, 0xffff0000, v75\n"
    "v_lshlrev_b32 v40, 16, v76\n"
    "v_and_b32 v41, 0xffff0000, v76\n"
    "v_lshlrev_b32 v42, 16, v77\n"
    "v_and_b32 v43, 0xffff0000, v77\n"
    "v_lshlrev_b32 v44, 16, v78\n"
    "v_and_b32 v45, 0xffff0000, v78\n"
    "v_lshlrev_b32 v46, 16, v79\n"
    "v_and_b32 v47, 0xffff0000, v79\n"
    "v_lshlrev_b32 v48, 16, v80\n"
    "v_and_b32 v49, 0xffff0000, v80\n"
    "v_lshlrev_b32 v50, 16, v81\n"
    "v_and_b32 v51, 0xffff0000, v81\n"
    "v_lshlrev_b32 v52, 16, v82\n"
    "v_and_b32 v53, 0xffff0000, v82\n"
    "v_lshlrev_b32 v54, 16, v83\n"
    "v_and_b32 v55, 0xffff0000, v83\n"
    "v_lshlrev_b32 v56, 16, v84\n"
    "v_and_b32 v57, 0xffff0000, v84\n"
    "v_lshlrev_b32 v58, 16, v85\n"
    "v_and_b32 v59, 0xffff0000, v85\n"
    "v_lshlrev_b32 v60, 16, v86\n"
    "v_and_b32 v61, 0xffff0000, v86\n"
    "v_lshlrev_b32 v62, 16, v87\n"
    "v_and_b32 v63, 0xffff0000, v87\n"
    "s_waitcnt lgkmcnt(10)\n"
    "v_mfma_f32_16x16x32_bf16 v[88:91], v[180:183], v[104:107], 0\n"
    "ds_read_b64 v[248:249], v7 offset:42336\n"
    "ds_read_b64 v[250:251], v7 offset:42528\n"
    "s_waitcnt lgkmcnt(10)\n"
    "v_mfma_f32_16x16x32_bf16 v[88:91], v[184:187], v[108:111], v[88:91]\n"
    "v_mfma_f32_16x16x32_bf16 v[92:95], v[180:183], v[108:111], 0\n"
    "ds_read_b64 v[252:253], v7 offset:49392\n"
    "ds_read_b64 v[254:255], v7 offset:49584\n"
    "s_waitcnt lgkmcnt(10)\n"
    "v_mfma_f32_16x16x32_bf16 v[88:91], v[188:191], v[112:115], v[88:91]\n"
    "v_mfma_f32_16x16x32_bf16 v[92:95], v[184:187], v[112:115], v[92:95]\n"
    "v_mfma_f32_16x16x32_bf16 v[96:99], v[180:183], v[112:115], 0\n"
    "ds_read_b64 v[136:137], v7 offset:8\n"
    "ds_read_b64 v[138:139], v7 offset:200\n"
    "s_waitcnt lgkmcnt(10)\n"
    "v_mfma_f32_16x16x32_bf16 v[88:91], v[192:195], v[116:119], v[88:91]\n"
    "v_mfma_f32_16x16x32_bf16 v[92:95], v[188:191], v[116:119], v[92:95]\n"
    "v_mfma_f32_16x16x32_bf16 v[96:99], v[184:187], v[116:119], v[96:99]\n"
    "v_mfma_f32_16x16x32_bf16 v[100:103], v[180:183], v[116:119], 0\n"
    "ds_read_b64 v[140:141], v7 offset:7064\n"
    "ds_read_b64 v[142:143], v7 offset:7256\n"
    "s_waitcnt lgkmcnt(10)\n"
    "v_mfma_f32_16x16x32_bf16 v[88:91], v[196:199], v[120:123], v[88:91]\n"
    "v_mfma_f32_16x16x32_bf16 v[92:95], v[192:195], v[120:123], v[92:95]\n"
    "v_mfma_f32_16x16x32_bf16 v[96:99], v[188:191], v[120:123], v[96:99]\n"
    "v_mfma_f32_16x16x32_bf16 v[100:103], v[184:187], v[120:123], v[100:103]\n"
    "ds_read_b64 v[144:145], v7 offset:14120\n"
    "ds_read_b64 v[146:147], v7 offset:14312\n"
    "s_waitcnt lgkmcnt(10)\n"
    "v_mfma_f32_16x16x32_bf16 v[92:95], v[196:199], v[124:127], v[92:95]\n"
    "s_setprio 2\n"
    "v_mfma_f32_16x16x32_bf16 v[96:99], v[192:195], v[124:127], v[96:99]\n"
    "v_mfma_f32_16x16x32_bf16 v[100:103], v[188:191], v[124:127], v[100:103]\n"
    "v_cvt_pk_bf16_f32 v64, v88, v89\n"
    "v_cvt_pk_bf16_f32 v65, v90, v91\n"
    "ds_read_b64 v[148:149], v7 offset:21176\n"
    "ds_read_b64 v[150:151], v7 offset:21368\n"
    "s_waitcnt lgkmcnt(10)\n"
    "v_mfma_f32_16x16x32_bf16 v[96:99], v[196:199], v[248:251], v[96:99]\n"
    "v_mfma_f32_16x16x32_bf16 v[100:103], v[192:195], v[248:251], v[100:103]\n"
    "ds_read_b64 v[152:153], v7 offset:28232\n"
    "ds_read_b64 v[154:155], v7 offset:28424\n"
    "s_waitcnt lgkmcnt(10)\n"
    "v_mfma_f32_16x16x32_bf16 v[100:103], v[196:199], v[252:255], v[100:103]\n"
    "v_cvt_pk_bf16_f32 v66, v92, v93\n"
    "v_cvt_pk_bf16_f32 v67, v94, v95\n"
    "ds_read_b64 v[128:129], v7 offset:35288\n"
    "ds_read_b64 v[130:131], v7 offset:35480\n"
    "s_waitcnt lgkmcnt(10)\n"
    "v_mfma_f32_16x16x32_bf16 v[104:107], v[200:203], v[136:139], 0\n"
    "ds_read_b64 v[132:133], v7 offset:42344\n"
    "ds_read_b64 v[134:135], v7 offset:42536\n"
    "s_waitcnt lgkmcnt(10)\n"
    "v_mfma_f32_16x16x32_bf16 v[104:107], v[204:207], v[140:143], v[104:107]\n"
    "v_cvt_pk_bf16_f32 v68, v96, v97\n"
    "v_cvt_pk_bf16_f32 v69, v98, v99\n"
    "v_mfma_f32_16x16x32_bf16 v[108:111], v[200:203], v[140:143], 0\n"
    "v_cvt_pk_bf16_f32 v70, v100, v101\n"
    "v_cvt_pk_bf16_f32 v71, v102, v103\n"
    "ds_read_b64 v[136:137], v7 offset:49400\n"
    "ds_read_b64 v[138:139], v7 offset:49592\n"
    "s_waitcnt lgkmcnt(10)\n"
    "v_mfma_f32_16x16x32_bf16 v[104:107], v[208:211], v[144:147], v[104:107]\n"
    "v_mfma_f32_16x16x32_bf16 v[108:111], v[204:207], v[144:147], v[108:111]\n"
    "ds_write_b64 v9, v[64:65] offset:0\n"
    "v_mfma_f32_16x16x32_bf16 v[112:115], v[200:203], v[144:147], 0\n"
    "ds_read_b64 v[140:141], v7 offset:16\n"
    "ds_read_b64 v[142:143], v7 offset:208\n"
    "s_waitcnt lgkmcnt(11)\n"
    "v_mfma_f32_16x16x32_bf16 v[104:107], v[212:215], v[148:151], v[104:107]\n"
    "ds_write_b64 v9, v[66:67] offset:7056\n"
    "v_mfma_f32_16x16x32_bf16 v[108:111], v[208:211], v[148:151], v[108:111]\n"
    "v_mfma_f32_16x16x32_bf16 v[112:115], v[204:207], v[148:151], v[112:115]\n"
    "ds_write_b64 v9, v[68:69] offset:14112\n"
    "v_mfma_f32_16x16x32_bf16 v[116:119], v[200:203], v[148:151], 0\n"
    "ds_read_b64 v[144:145], v7 offset:7072\n"
    "ds_read_b64 v[146:147], v7 offset:7264\n"
    "s_setprio 1\n"
    "s_waitcnt lgkmcnt(13)\n"
    "v_mfma_f32_16x16x32_bf16 v[104:107], v[216:219], v[152:155], v[104:107]\n"
    "ds_write_b64 v9, v[70:71] offset:21168\n"
    "v_mfma_f32_16x16x32_bf16 v[108:111], v[212:215], v[152:155], v[108:111]\n"
    "v_mfma_f32_16x16x32_bf16 v[112:115], v[208:211], v[152:155], v[112:115]\n"
    "v_mfma_f32_16x16x32_bf16 v[116:119], v[204:207], v[152:155], v[116:119]\n"
    "ds_read_b64 v[148:149], v7 offset:14128\n"
    "s_waitcnt lgkmcnt(14)\n"
    "ds_read_b64 v[150:151], v7 offset:14320\n"
    "s_waitcnt lgkmcnt(14)\n"
    "v_mfma_f32_16x16x32_bf16 v[108:111], v[216:219], v[128:131], v[108:111]\n"
    "v_mfma_f32_16x16x32_bf16 v[112:115], v[212:215], v[128:131], v[112:115]\n"
    "v_mfma_f32_16x16x32_bf16 v[116:119], v[208:211], v[128:131], v[116:119]\n"
    "v_cvt_pk_bf16_f32 v72, v104, v105\n"
    "v_cvt_pk_bf16_f32 v73, v106, v107\n"
    "ds_read_b64 v[152:153], v7 offset:21184\n"
    "s_waitcnt lgkmcnt(14)\n"
    "ds_read_b64 v[154:155], v7 offset:21376\n"
    "s_waitcnt lgkmcnt(14)\n"
    "v_mfma_f32_16x16x32_bf16 v[112:115], v[216:219], v[132:135], v[112:115]\n"
    "v_mfma_f32_16x16x32_bf16 v[116:119], v[212:215], v[132:135], v[116:119]\n"
    "ds_read_b64 v[128:129], v7 offset:28240\n"
    "s_waitcnt lgkmcnt(14)\n"
    "ds_read_b64 v[130:131], v7 offset:28432\n"
    "s_waitcnt lgkmcnt(14)\n"
    "v_mfma_f32_16x16x32_bf16 v[116:119], v[216:219], v[136:139], v[116:119]\n"
    "v_cvt_pk_bf16_f32 v74, v108, v109\n"
    "v_cvt_pk_bf16_f32 v75, v110, v111\n"
    "ds_read_b64 v[132:133], v7 offset:35296\n"
    "s_waitcnt lgkmcnt(14)\n"
    "ds_read_b64 v[134:135], v7 offset:35488\n"
    "s_waitcnt lgkmcnt(13)\n"
    "v_mfma_f32_16x16x32_bf16 v[88:91], v[220:223], v[140:143], 0\n"
    "ds_read_b64 v[136:137], v7 offset:42352\n"
    "ds_read_b64 v[138:139], v7 offset:42544\n"
    "s_waitcnt lgkmcnt(11)\n"
    "v_mfma_f32_16x16x32_bf16 v[88:91], v[224:227], v[144:147], v[88:91]\n"
    "v_cvt_pk_bf16_f32 v76, v112, v113\n"
    "v_cvt_pk_bf16_f32 v77, v114, v115\n"
    "v_mfma_f32_16x16x32_bf16 v[92:95], v[220:223], v[144:147], 0\n"
    "v_cvt_pk_bf16_f32 v78, v116, v117\n"
    "v_cvt_pk_bf16_f32 v79, v118, v119\n"
    "ds_read_b64 v[140:141], v7 offset:49408\n"
    "ds_read_b64 v[142:143], v7 offset:49600\n"
    "s_waitcnt lgkmcnt(10)\n"
    "v_mfma_f32_16x16x32_bf16 v[88:91], v[228:231], v[148:151], v[88:91]\n"
    "v_mfma_f32_16x16x32_bf16 v[92:95], v[224:227], v[148:151], v[92:95]\n"
    "ds_write_b64 v9, v[72:73] offset:8\n"
    "s_setprio 0\n"
    "v_mfma_f32_16x16x32_bf16 v[96:99], v[220:223], v[148:151], 0\n"
    "s_waitcnt lgkmcnt(9)\n"
    "v_mfma_f32_16x16x32_bf16 v[88:91], v[232:235], v[152:155], v[88:91]\n"
    "ds_write_b64 v9, v[74:75] offset:7064\n"
    "v_mfma_f32_16x16x32_bf16 v[92:95], v[228:231], v[152:155], v[92:95]\n"
    "v_mfma_f32_16x16x32_bf16 v[96:99], v[224:227], v[152:155], v[96:99]\n"
    "ds_write_b64 v9, v[76:77] offset:14120\n"
    "v_mfma_f32_16x16x32_bf16 v[100:103], v[220:223], v[152:155], 0\n"
    "s_waitcnt lgkmcnt(9)\n"
    "v_mfma_f32_16x16x32_bf16 v[88:91], v[236:239], v[128:131], v[88:91]\n"
    "ds_write_b64 v9, v[78:79] offset:21176\n"
    "v_mfma_f32_16x16x32_bf16 v[92:95], v[232:235], v[128:131], v[92:95]\n"
    "v_mfma_f32_16x16x32_bf16 v[96:99], v[228:231], v[128:131], v[96:99]\n"
    "v_mfma_f32_16x16x32_bf16 v[100:103], v[224:227], v[128:131], v[100:103]\n"
    "s_waitcnt lgkmcnt(8)\n"
    "v_mfma_f32_16x16x32_bf16 v[92:95], v[236:239], v[132:135], v[92:95]\n"
    "v_mfma_f32_16x16x32_bf16 v[96:99], v[232:235], v[132:135], v[96:99]\n"
    "v_mfma_f32_16x16x32_bf16 v[100:103], v[228:231], v[132:135], v[100:103]\n"
    "v_cvt_pk_bf16_f32 v80, v88, v89\n"
    "v_cvt_pk_bf16_f32 v81, v90, v91\n"
    "s_waitcnt lgkmcnt(6)\n"
    "v_mfma_f32_16x16x32_bf16 v[96:99], v[236:239], v[136:139], v[96:99]\n"
    "v_mfma_f32_16x16x32_bf16 v[100:103], v[232:235], v[136:139], v[100:103]\n"
    "s_waitcnt lgkmcnt(4)\n"
    "v_mfma_f32_16x16x32_bf16 v[100:103], v[236:239], v[140:143], v[100:103]\n"
    "v_cvt_pk_bf16_f32 v82, v92, v93\n"
    "v_cvt_pk_bf16_f32 v83, v94, v95\n"
    "s_nop 2\n"
    "v_cvt_pk_bf16_f32 v84, v96, v97\n"
    "v_cvt_pk_bf16_f32 v85, v98, v99\n"
    "s_nop 0\n"
    "v_cvt_pk_bf16_f32 v86, v100, v101\n"
    "v_cvt_pk_bf16_f32 v87, v102, v103\n"
    "ds_write_b64 v9, v[80:81] offset:16\n"
    "ds_write_b64 v9, v[82:83] offset:7072\n"
    "ds_write_b64 v9, v[84:85] offset:14128\n"
    "ds_write_b64 v9, v[86:87] offset:21184\n"
    "L_loopX_%=:\n"
    "s_waitcnt lgkmcnt(0)\n"
    "s_barrier\n"
    "ds_read_b128 v[64:67], v11 offset:0\n"
    "ds_read_b128 v[68:71], v11 offset:32\n"
    "ds_read_b128 v[72:75], v11 offset:64\n"
    "ds_read_b128 v[76:79], v11 offset:96\n"
    "ds_read_b128 v[80:83], v11 offset:128\n"
    "ds_read_b128 v[84:87], v11 offset:160\n"
    "s_lshl_b32 s45, s57, 7\n"
    "s_add_u32 s45, s45, s55\n"
    "s_add_u32 s45, s45, s43\n"
    "s_lshl_b32 s45, s45, 9\n"
    "s_add_u32 s45, s45, s56\n"
    "s_mul_i32 s60, s45, 192\n"
    "s_add_u32 s45, s57, 1\n"
    "s_cmp_lt_u32 s45, 4\n"
    "s_cselect_b32 s61, 50331648, 0\n"
    "s_lshl_b32 s45, s45, 7\n"
    "s_add_u32 s45, s45, s55\n"
    "s_and_b32 s46, s43, 3\n"
    "s_mul_i32 s46, s46, 3\n"
    "s_add_u32 s58, s45, s46\n"
    "s_sub_u32 s58, s58, 2\n"
    "s_setprio 3\n"
    "v_mov_b32 v13, 0x3f803f80\n"
    "s_waitcnt lgkmcnt(5)\n"
    "v_dot2_f32_bf16 v156, v64, v13, 0\n"
    "v_dot2_f32_bf16 v157, v64, v64, 0\n"
    "v_dot2_f32_bf16 v158, v65, v13, 0\n"
    "v_dot2_f32_bf16 v159, v65, v65, 0\n"
    "v_dot2_f32_bf16 v156, v66, v13, v156\n"
    "v_dot2_f32_bf16 v157, v66, v66, v157\n"
    "v_dot2_f32_bf16 v158, v67, v13, v158\n"
    "v_dot2_f32_bf16 v159, v67, v67, v159\n"
    "s_waitcnt lgkmcnt(4)\n"
    "v_dot2_f32_bf16 v156, v68, v13, v156\n"
    "v_dot2_f32_bf16 v157, v68, v68, v157\n"
    "v_dot2_f32_bf16 v158, v69, v13, v158\n"
    "v_dot2_f32_bf16 v159, v69, v69, v159\n"
    "v_dot2_f32_bf16 v156, v70, v13, v156\n"
    "v_dot2_f32_bf16 v157, v70, v70, v157\n"
    "v_dot2_f32_bf16 v158, v71, v13, v158\n"
    "v_dot2_f32_bf16 v159, v71, v71, v159\n"
    "s_waitcnt lgkmcnt(3)\n"
    "v_dot2_f32_bf16 v156, v72, v13, v156\n"
    "v_dot2_f32_bf16 v157, v72, v72, v157\n"
    "v_dot2_f32_bf16 v158, v73, v13, v158\n"
    "v_dot2_f32_bf16 v159, v73, v73, v159\n"
    "v_dot2_f32_bf16 v156, v74, v13, v156\n"
    "v_dot2_f32_bf16 v157, v74, v74, v157\n"
    "v_dot2_f32_bf16 v158, v75, v13, v158\n"
    "v_dot2_f32_bf16 v159, v75, v75, v159\n"
    "s_waitcnt lgkmcnt(2)\n"
    "v_dot2_f32_bf16 v156, v76, v13, v156\n"
    "v_dot2_f32_bf16 v157, v76, v76, v157\n"
    "v_dot2_f32_bf16 v158, v77, v13, v158\n"
    "v_dot2_f32_bf16 v159, v77, v77, v159\n"
    "v_dot2_f32_bf16 v156, v78, v13, v156\n"
    "v_dot2_f32_bf16 v157, v78, v78, v157\n"
    "v_dot2_f32_bf16 v158, v79, v13, v158\n"
    "v_dot2_f32_bf16 v159, v79, v79, v159\n"
    "s_waitcnt lgkmcnt(1)\n"
    "v_dot2_f32_bf16 v156, v80, v13, v156\n"
    "v_dot2_f32_bf16 v157, v80, v80, v157\n"
    "v_dot2_f32_bf16 v158, v81, v13, v158\n"
    "v_dot2_f32_bf16 v159, v81, v81, v159\n"
    "v_dot2_f32_bf16 v156, v82, v13, v156\n"
    "v_dot2_f32_bf16 v157, v82, v82, v157\n"
    "v_dot2_f32_bf16 v158, v83, v13, v158\n"
    "v_dot2_f32_bf16 v159, v83, v83, v159\n"
    "s_waitcnt lgkmcnt(0)\n"
    "v_dot2_f32_bf16 v156, v84, v13, v156\n"
    "v_dot2_f32_bf16 v157, v84, v84, v157\n"
    "v_dot2_f32_bf16 v158, v85, v13, v158\n"
    "v_dot2_f32_bf16 v159, v85, v85, v159\n"
    "v_dot2_f32_bf16 v156, v86, v13, v156\n"
    "v_dot2_f32_bf16 v157, v86, v86, v157\n"
    "v_dot2_f32_bf16 v158, v87, v13, v158\n"
    "v_dot2_f32_bf16 v159, v87, v87, v159\n"
    "s_setprio 2\n"
    "s_nop 0\n"
    "v_add_f32 v156, v156, v158\n"
    "v_add_f32 v157, v157, v159\n"
    "v_mov_b32 v158, v156\n"
    "v_mov_b32 v159, v157\n"
    "s_nop 0\n"
    "v_permlane32_swap_b32 v156, v158\n"
    "v_permlane32_swap_b32 v157, v159\n"
    "v_add_f32 v156, v156, v158\n"
    "v_add_f32 v157, v157, v159\n"
    "v_mul_f32 v156, s65, v156\n"
    "v_mul_f32 v157, s65, v157\n"
    "v_fma_f32 v157, -v156, v156, v157\n"
    "v_max_f32 v157, 0, v157\n"
    "v_add_u32 v158, -1, v157\n"
    "v_add_f32 v157, s66, v157\n"
    "v_cmp_gt_u32 vcc, s68, v158\n"
    "v_rsq_f32 v160, v157\n"
    "s_nop 0\n"
    "v_mul_f32 v160, s67, v160\n"
    "s_nop 1\n"
    "s_or_b64 s[70:71], s[70:71], vcc\n"
    "v_cvt_pk_bf16_f32 v162, v160, 0\n"
    "v_lshlrev_b32 v163, 16, v162\n"
    "v_mul_f32_e64 v161, -v156, v163\n"
    "ds_read_b64_tr_b16 v[136:137], v1 offset:0\n"
    "ds_read_b64_tr_b16 v[138:139], v1 offset:256\n"
    "ds_read_b64_tr_b16 v[140:141], v1 offset:1024\n"
    "ds_read_b64_tr_b16 v[142:143], v1 offset:1280\n"
    "v_dot2_f32_bf16 v104, v64, v162, v161\n"
    "v_dot2_f32_bf16 v105, v64, v163, v161\n"
    "v_dot2_f32_bf16 v106, v65, v162, v161\n"
    "v_dot2_f32_bf16 v107, v65, v163, v161\n"
    "v_dot2_f32_bf16 v108, v66, v162, v161\n"
    "v_dot2_f32_bf16 v109, v66, v163, v161\n"
    "v_dot2_f32_bf16 v110, v67, v162, v161\n"
    "v_dot2_f32_bf16 v111, v67, v163, v161\n"
    "v_dot2_f32_bf16 v112, v68, v162, v161\n"
    "v_dot2_f32_bf16 v113, v68, v163, v161\n"
    "v_dot2_f32_bf16 v114, v69, v162, v161\n"
    "v_dot2_f32_bf16 v115, v69, v163, v161\n"
    "v_dot2_f32_bf16 v116, v70, v162, v161\n"
    "v_dot2_f32_bf16 v117, v70, v163, v161\n"
    "v_dot2_f32_bf16 v118, v71, v162, v161\n"
    "v_dot2_f32_bf16 v119, v71, v163, v161\n"
    "v_cvt_pk_bf16_f32 v64, v104, v105\n"
    "v_cvt_pk_bf16_f32 v65, v106, v107\n"
    "v_cvt_pk_bf16_f32 v66, v108, v109\n"
    "v_cvt_pk_bf16_f32 v67, v110, v111\n"
    "v_cvt_pk_bf16_f32 v68, v112, v113\n"
    "v_cvt_pk_bf16_f32 v69, v114, v115\n"
    "v_cvt_pk_bf16_f32 v70, v116, v117\n"
    "v_cvt_pk_bf16_f32 v71, v118, v119\n"
    "ds_read_b64_tr_b16 v[144:145], v1 offset:2048\n"
    "ds_read_b64_tr_b16 v[146:147], v1 offset:2304\n"
    "ds_read_b64_tr_b16 v[148:149], v1 offset:3072\n"
    "ds_read_b64_tr_b16 v[150:151], v1 offset:3328\n"
    "s_setprio 1\n"
    "v_dot2_f32_bf16 v120, v72, v162, v161\n"
    "v_dot2_f32_bf16 v121, v72, v163, v161\n"
    "v_dot2_f32_bf16 v122, v73, v162, v161\n"
    "v_dot2_f32_bf16 v123, v73, v163, v161\n"
    "v_dot2_f32_bf16 v124, v74, v162, v161\n"
    "v_dot2_f32_bf16 v125, v74, v163, v161\n"
    "v_dot2_f32_bf16 v126, v75, v162, v161\n"
    "v_dot2_f32_bf16 v127, v75, v163, v161\n"
    "v_dot2_f32_bf16 v128, v76, v162, v161\n"
    "v_dot2_f32_bf16 v129, v76, v163, v161\n"
    "v_dot2_f32_bf16 v130, v77, v162, v161\n"
    "v_dot2_f32_bf16 v131, v77, v163, v161\n"
    "v_dot2_f32_bf16 v132, v78, v162, v161\n"
    "v_dot2_f32_bf16 v133, v78, v163, v161\n"
    "v_dot2_f32_bf16 v134, v79, v162, v161\n"
    "v_dot2_f32_bf16 v135, v79, v163, v161\n"
    "v_cvt_pk_bf16_f32 v72, v120, v121\n"
    "v_cvt_pk_bf16_f32 v73, v122, v123\n"
    "v_cvt_pk_bf16_f32 v74, v124, v125\n"
    "v_cvt_pk_bf16_f32 v75, v126, v127\n"
    "v_cvt_pk_bf16_f32 v76, v128, v129\n"
    "v_cvt_pk_bf16_f32 v77, v130, v131\n"
    "v_cvt_pk_bf16_f32 v78, v132, v133\n"
    "v_cvt_pk_bf16_f32 v79, v134, v135\n"
    "ds_read_b64_tr_b16 v[152:153], v1 offset:4096\n"
    "ds_read_b64_tr_b16 v[154:155], v1 offset:4352\n"
    "ds_read_b64_tr_b16 v[248:249], v1 offset:5120\n"
    "ds_read_b64_tr_b16 v[250:251], v1 offset:5376\n"
    "s_setprio 0\n"
    "v_dot2_f32_bf16 v164, v80, v162, v161\n"
    "v_dot2_f32_bf16 v165, v80, v163, v161\n"
    "v_dot2_f32_bf16 v166, v81, v162, v161\n"
    "v_dot2_f32_bf16 v167, v81, v163, v161\n"
    "v_dot2_f32_bf16 v168, v82, v162, v161\n"
    "v_dot2_f32_bf16 v169, v82, v163, v161\n"
    "v_dot2_f32_bf16 v170, v83, v162, v161\n"
    "v_dot2_f32_bf16 v171, v83, v163, v161\n"
    "v_dot2_f32_bf16 v172, v84, v162, v161\n"
    "v_dot2_f32_bf16 v173, v84, v163, v161\n"
    "v_dot2_f32_bf16 v174, v85, v162, v161\n"
    "v_dot2_f32_bf16 v175, v85, v163, v161\n"
    "v_dot2_f32_bf16 v176, v86, v162, v161\n"
    "v_dot2_f32_bf16 v177, v86, v163, v161\n"
    "v_dot2_f32_bf16 v178, v87, v162, v161\n"
    "v_dot2_f32_bf16 v179, v87, v163, v161\n"
    "v_cvt_pk_bf16_f32 v80, v164, v165\n"
    "v_cvt_pk_bf16_f32 v81, v166, v167\n"
    "v_cvt_pk_bf16_f32 v82, v168, v169\n"
    "v_cvt_pk_bf16_f32 v83, v170, v171\n"
    "v_cvt_pk_bf16_f32 v84, v172, v173\n"
    "v_cvt_pk_bf16_f32 v85, v174, v175\n"
    "v_cvt_pk_bf16_f32 v86, v176, v177\n"
    "v_cvt_pk_bf16_f32 v87, v178, v179\n"
    "s_waitcnt lgkmcnt(10)\n"
    "v_mfma_f32_32x32x16_bf16 v[88:103], v[136:139], v[64:67], 0\n"
    "s_waitcnt lgkmcnt(8)\n"
    "v_mfma_f32_32x32x16_bf16 v[88:103], v[140:143], v[68:71], v[88:103]\n"
    "s_waitcnt lgkmcnt(6)\n"
    "v_mfma_f32_32x32x16_bf16 v[88:103], v[144:147], v[72:75], v[88:103]\n"
    "s_waitcnt lgkmcnt(4)\n"
    "v_mfma_f32_32x32x16_bf16 v[88:103], v[148:151], v[76:79], v[88:103]\n"
    "s_waitcnt lgkmcnt(2)\n"
    "v_mfma_f32_32x32x16_bf16 v[88:103], v[152:155], v[80:83], v[88:103]\n"
    "s_waitcnt lgkmcnt(0)\n"
    "v_mfma_f32_32x32x16_bf16 v[88:103], v[248:251], v[84:87], v[88:103]\n"
    "ds_read_b64_tr_b16 v[136:137], v1 offset:12288\n"
    "ds_read_b64_tr_b16 v[138:139], v1 offset:12544\n"
    "ds_read_b64_tr_b16 v[140:141], v1 offset:13312\n"
    "ds_read_b64_tr_b16 v[142:143], v1 offset:13568\n"
    "ds_read_b64_tr_b16 v[144:145], v1 offset:14336\n"
    "ds_read_b64_tr_b16 v[146:147], v1 offset:14592\n"
    "s_setprio 1\n"
    "s_nop 4\n"
    "v_cvt_pk_f16_f32 v165, v94, v95\n"
    "v_cvt_pk_f16_f32 v167, v96, v97\n"
    "v_cvt_pk_f16_f32 v169, v98, v99\n"
    "v_cvt_pk_f16_f32 v171, v100, v101\n"
    "v_cvt_pk_f16_f32 v173, v102, v103\n"
    "v_and_b32_e32 v165, s42, v165\n"
    "v_and_b32_e32 v167, s42, v167\n"
    "v_and_b32_e32 v169, s42, v169\n"
    "v_and_b32_e32 v171, s42, v171\n"
    "v_and_b32_e32 v173, s42, v173\n"
    "v_lshrrev_b32_sdwa v164, 2, v165 dst_sel:DWORD dst_unused:UNUSED_PAD src0_sel:DWORD src1_sel:WORD_0\n"
    "v_lshrrev_b32_sdwa v166, 2, v167 dst_sel:DWORD dst_unused:UNUSED_PAD src0_sel:DWORD src1_sel:WORD_0\n"
    "s_waitcnt lgkmcnt(4)\n"
    "v_mfma_f32_32x32x16_bf16 v[104:119], v[136:139], v[64:67], 0\n"
    "ds_read_b64_tr_b16 v[148:149], v1 offset:15360\n"
    "ds_read_b64_tr_b16 v[150:151], v1 offset:15616\n"
    "v_lshrrev_b32_sdwa v168, 2, v169 dst_sel:DWORD dst_unused:UNUSED_PAD src0_sel:DWORD src1_sel:WORD_0\n"
    "v_lshrrev_b32_sdwa v170, 2, v171 dst_sel:DWORD dst_unused:UNUSED_PAD src0_sel:DWORD src1_sel:WORD_0\n"
    "v_lshrrev_b32_sdwa v172, 2, v173 dst_sel:DWORD dst_unused:UNUSED_PAD src0_sel:DWORD src1_sel:WORD_0\n"
    "v_lshrrev_b32_e32 v165, 18, v165\n"
    "v_lshrrev_b32_e32 v167, 18, v167\n"
    "v_lshrrev_b32_e32 v169, 18, v169\n"
    "s_setprio 0\n"
    "v_lshrrev_b32_e32 v171, 18, v171\n"
    "v_lshrrev_b32_e32 v173, 18, v173\n"
    "v_cvt_pk_f16_f32 v156, v88, v89\n"
    "v_cvt_pk_f16_f32 v158, v90, v91\n"
    "s_waitcnt lgkmcnt(4)\n"
    "v_mfma_f32_32x32x16_bf16 v[104:119], v[140:143], v[68:71], v[104:119]\n"
    "ds_read_b64_tr_b16 v[152:153], v1 offset:16384\n"
    "ds_read_b64_tr_b16 v[154:155], v1 offset:16640\n"
    "v_cvt_pk_f16_f32 v160, v92, v93\n"
    "v_pk_fma_f16 v157, v156, v156, s40\n"
    "v_pk_fma_f16 v159, v158, v158, s40\n"
    "ds_read_u16 v164, v164 offset:0\n"
    "v_pk_fma_f16 v161, v160, v160, s40\n"
    "v_pk_mul_f16 v157, v157, v156 neg_lo:[1,0] neg_hi:[1,0]\n"
    "v_pk_mul_f16 v159, v159, v158 neg_lo:[1,0] neg_hi:[1,0]\n"
    "v_pk_mul_f16 v161, v161, v160 neg_lo:[1,0] neg_hi:[1,0]\n"
    "ds_read_u16_d16_hi v165, v165 offset:0\n"
    "v_exp_f16_sdwa v157, v157 dst_sel:WORD_0 dst_unused:UNUSED_PRESERVE src0_sel:WORD_0\n"
    "v_exp_f16_sdwa v159, v159 dst_sel:WORD_0 dst_unused:UNUSED_PRESERVE src0_sel:WORD_0\n"
    "s_waitcnt lgkmcnt(6)\n"
    "v_mfma_f32_32x32x16_bf16 v[104:119], v[144:147], v[72:75], v[104:119]\n"
    "ds_read_u16 v166, v166 offset:0\n"
    "ds_read_b64_tr_b16 v[136:137], v1 offset:17408\n"
    "ds_read_b64_tr_b16 v[138:139], v1 offset:17664\n"
    "v_exp_f16_sdwa v161, v161 dst_sel:WORD_0 dst_unused:UNUSED_PRESERVE src0_sel:WORD_0\n"
    "v_exp_f16_sdwa v157, v157 dst_sel:WORD_1 dst_unused:UNUSED_PRESERVE src0_sel:WORD_1\n"
    "s_barrier\n"
    "s_setprio 3\n"
    "ds_read_u16_d16_hi v167, v167 offset:0\n"
    "v_exp_f16_sdwa v159, v159 dst_sel:WORD_1 dst_unused:UNUSED_PRESERVE src0_sel:WORD_1\n"
    "v_exp_f16_sdwa v161, v161 dst_sel:WORD_1 dst_unused:UNUSED_PRESERVE src0_sel:WORD_1\n"
    "ds_read_u16 v168, v168 offset:0\n"
    "v_pk_fma_f16 v157, v157, s41, s41\n"
    "v_pk_fma_f16 v159, v159, s41, s41\n"
    "s_waitcnt lgkmcnt(9)\n"
    "v_mfma_f32_32x32x16_bf16 v[104:119], v[148:151], v[76:79], v[104:119]\n"
    "v_pk_fma_f16 v161, v161, s41, s41\n"
    "v_rcp_f16_sdwa v157, v157 dst_sel:WORD_0 dst_unused:UNUSED_PRESERVE src0_sel:WORD_0\n"
    "ds_read_u16_d16_hi v169, v169 offset:0\n"
    "v_rcp_f16_sdwa v159, v159 dst_sel:WORD_0 dst_unused:UNUSED_PRESERVE src0_sel:WORD_0\n"
    "v_rcp_f16_sdwa v161, v161 dst_sel:WORD_0 dst_unused:UNUSED_PRESERVE src0_sel:WORD_0\n"
    "ds_read_u16 v170, v170 offset:0\n"
    "v_rcp_f16_sdwa v157, v157 dst_sel:WORD_1 dst_unused:UNUSED_PRESERVE src0_sel:WORD_1\n"
    "v_rcp_f16_sdwa v159, v159 dst_sel:WORD_1 dst_unused:UNUSED_PRESERVE src0_sel:WORD_1\n"
    "ds_read_u16_d16_hi v171, v171 offset:0\n"
    "s_setprio 2\n"
    "s_waitcnt lgkmcnt(10)\n"
    "v_mfma_f32_32x32x16_bf16 v[104:119], v[152:155], v[80:83], v[104:119]\n"
    "v_rcp_f16_sdwa v161, v161 dst_sel:WORD_1 dst_unused:UNUSED_PRESERVE src0_sel:WORD_1\n"
    "v_fma_mix_f32 v88, v88, v157, 0 op_sel:[0,0,0] op_sel_hi:[0,1,0]\n"
    "ds_read_u16 v172, v172 offset:0\n"
    "v_fma_mix_f32 v90, v90, v159, 0 op_sel:[0,0,0] op_sel_hi:[0,1,0]\n"
    "v_fma_mix_f32 v92, v92, v161, 0 op_sel:[0,0,0] op_sel_hi:[0,1,0]\n"
    "v_fma_mix_f32 v89, v89, v157, 0 op_sel:[0,1,0] op_sel_hi:[0,1,0]\n"
    "v_fma_mix_f32 v91, v91, v159, 0 op_sel:[0,1,0] op_sel_hi:[0,1,0]\n"
    "ds_read_u16_d16_hi v173, v173 offset:0\n"
    "v_fma_mix_f32 v93, v93, v161, 0 op_sel:[0,1,0] op_sel_hi:[0,1,0]\n"
    "v_cvt_pk_bf16_f32 v120, v88, v89\n"
    "v_cvt_pk_bf16_f32 v121, v90, v91\n"
    "v_cvt_pk_bf16_f32 v122, v92, v93\n"
    "s_waitcnt lgkmcnt(7)\n"
    "v_mfma_f32_32x32x16_bf16 v[104:119], v[136:139], v[84:87], v[104:119]\n"
    "v_or_b32_e32 v123, v164, v165\n"
    "s_waitcnt lgkmcnt(6)\n"
    "v_or_b32_e32 v124, v166, v167\n"
    "s_waitcnt lgkmcnt(4)\n"
    "v_or_b32_e32 v125, v168, v169\n"
    "s_waitcnt lgkmcnt(2)\n"
    "v_or_b32_e32 v126, v170, v171\n"
    "s_waitcnt lgkmcnt(0)\n"
    "v_or_b32_e32 v127, v172, v173\n"
    "ds_read_b64_tr_b16 v[136:137], v1 offset:24576\n"
    "ds_read_b64_tr_b16 v[138:139], v1 offset:24832\n"
    "ds_read_b64_tr_b16 v[140:141], v2 offset:24576\n"
    "ds_read_b64_tr_b16 v[142:143], v2 offset:26112\n"
    "ds_read_b64_tr_b16 v[144:145], v1 offset:25600\n"
    "ds_read_b64_tr_b16 v[146:147], v1 offset:25856\n"
    "s_setprio 1\n"
    "v_cvt_pk_f16_f32 v165, v110, v111\n"
    "v_cvt_pk_f16_f32 v167, v112, v113\n"
    "v_cvt_pk_f16_f32 v169, v114, v115\n"
    "v_cvt_pk_f16_f32 v171, v116, v117\n"
    "v_cvt_pk_f16_f32 v173, v118, v119\n"
    "s_mov_b32 exec_hi, 0xffff\n"
    "s_add_u32 s45, s58, 0\n"
    "s_cmp_lt_u32 s45, 512\n"
    "s_cselect_b32 s30, s61, 0\n"
    "s_cselect_b32 s45, s45, 0\n"
    "s_mul_i32 s46, s45, 98304\n"
    "s_add_u32 m0, s59, 0\n"
    "s_add_u32 s44, s46, s64\n"
    "buffer_load_dwordx4 v4, s[28:31], s44 offen nt lds\n"
    "s_add_u32 m0, s59, 784\n"
    "s_add_u32 s44, s46, s63\n"
    "buffer_load_dwordx4 v3, s[28:31], s44 offen nt lds\n"
    "s_add_u32 m0, s59, 1568\n"
    "s_add_u32 s44, s44, 768\n"
    "buffer_load_dwordx4 v3, s[28:31], s44 offen nt lds\n"
    "s_add_u32 m0, s59, 2352\n"
    "s_add_u32 s44, s44, 768\n"
    "buffer_load_dwordx4 v3, s[28:31], s44 offen nt lds\n"
    "s_mov_b32 exec_hi, -1\n"
    "v_and_b32_e32 v165, s42, v165\n"
    "v_and_b32_e32 v167, s42, v167\n"
    "v_and_b32_e32 v169, s42, v169\n"
    "v_and_b32_e32 v171, s42, v171\n"
    "v_and_b32_e32 v173, s42, v173\n"
    "v_lshrrev_b32_sdwa v164, 2, v165 dst_sel:DWORD dst_unused:UNUSED_PAD src0_sel:DWORD src1_sel:WORD_0\n"
    "v_lshrrev_b32_sdwa v166, 2, v167 dst_sel:DWORD dst_unused:UNUSED_PAD src0_sel:DWORD src1_sel:WORD_0\n"
    "s_waitcnt lgkmcnt(4)\n"
    "v_mfma_f32_32x32x16_bf16 v[88:103], v[136:139], v[64:67], 0\n"
    "ds_read_b64_tr_b16 v[148:149], v2 offset:24640\n"
    "ds_read_b64_tr_b16 v[150:151], v2 offset:26176\n"
    "v_lshrrev_b32_sdwa v168, 2, v169 dst_sel:DWORD dst_unused:UNUSED_PAD src0_sel:DWORD src1_sel:WORD_0\n"
    "v_lshrrev_b32_sdwa v170, 2, v171 dst_sel:DWORD dst_unused:UNUSED_PAD src0_sel:DWORD src1_sel:WORD_0\n"
    "v_lshrrev_b32_sdwa v172, 2, v173 dst_sel:DWORD dst_unused:UNUSED_PAD src0_sel:DWORD src1_sel:WORD_0\n"
    "v_lshrrev_b32_e32 v165, 18, v165\n"
    "s_waitcnt lgkmcnt(4)\n"
    "v_mfma_f32_32x32x16_bf16 v[16:31], v[140:143], v[120:123], v[16:31]\n"
    "ds_read_b64_tr_b16 v[152:153], v1 offset:26624\n"
    "ds_read_b64_tr_b16 v[154:155], v1 offset:26880\n"
    "v_lshrrev_b32_e32 v167, 18, v167\n"
    "v_lshrrev_b32_e32 v169, 18, v169\n"
    "s_setprio 0\n"
    "v_lshrrev_b32_e32 v171, 18, v171\n"
    "v_lshrrev_b32_e32 v173, 18, v173\n"
    "v_cvt_pk_f16_f32 v156, v104, v105\n"
    "s_waitcnt lgkmcnt(4)\n"
    "v_mfma_f32_32x32x16_bf16 v[88:103], v[144:147], v[68:71], v[88:103]\n"
    "ds_read_b64_tr_b16 v[136:137], v2 offset:24704\n"
    "ds_read_b64_tr_b16 v[138:139], v2 offset:26240\n"
    "v_cvt_pk_f16_f32 v158, v106, v107\n"
    "v_cvt_pk_f16_f32 v160, v108, v109\n"
    "v_pk_fma_f16 v157, v156, v156, s40\n"
    "v_pk_fma_f16 v159, v158, v158, s40\n"
    "ds_read_u16 v164, v164 offset:0\n"
    "v_pk_fma_f16 v161, v160, v160, s40\n"
    "s_waitcnt lgkmcnt(5)\n"
    "v_mfma_f32_32x32x16_bf16 v[32:47], v[148:151], v[120:123], v[32:47]\n"
    "ds_read_b64_tr_b16 v[140:141], v1 offset:27648\n"
    "ds_read_b64_tr_b16 v[142:143], v1 offset:27904\n"
    "v_pk_mul_f16 v157, v157, v156 neg_lo:[1,0] neg_hi:[1,0]\n"
    "v_pk_mul_f16 v159, v159, v158 neg_lo:[1,0] neg_hi:[1,0]\n"
    "v_pk_mul_f16 v161, v161, v160 neg_lo:[1,0] neg_hi:[1,0]\n"
    "ds_read_u16_d16_hi v165, v165 offset:0\n"
    "v_exp_f16_sdwa v157, v157 dst_sel:WORD_0 dst_unused:UNUSED_PRESERVE src0_sel:WORD_0\n"
    "s_waitcnt lgkmcnt(6)\n"
    "v_mfma_f32_32x32x16_bf16 v[88:103], v[152:155], v[72:75], v[88:103]\n"
    "ds_read_b64_tr_b16 v[144:145], v2 offset:27648\n"
    "ds_read_b64_tr_b16 v[146:147], v2 offset:29184\n"
    "v_exp_f16_sdwa v159, v159 dst_sel:WORD_0 dst_unused:UNUSED_PRESERVE src0_sel:WORD_0\n"
    "ds_read_u16 v166, v166 offset:0\n"
    "v_exp_f16_sdwa v161, v161 dst_sel:WORD_0 dst_unused:UNUSED_PRESERVE src0_sel:WORD_0\n"
    "v_exp_f16_sdwa v157, v157 dst_sel:WORD_1 dst_unused:UNUSED_PRESERVE src0_sel:WORD_1\n"
    "s_waitcnt lgkmcnt(7)\n"
    "v_mfma_f32_32x32x16_bf16 v[48:63], v[136:139], v[120:123], v[48:63]\n"
    "s_barrier\n"
    "s_setprio 3\n"
    "ds_read_b64_tr_b16 v[148:149], v1 offset:28672\n"
    "ds_read_b64_tr_b16 v[150:151], v1 offset:28928\n"
    "ds_read_u16_d16_hi v167, v167 offset:0\n"
    "v_exp_f16_sdwa v159, v159 dst_sel:WORD_1 dst_unused:UNUSED_PRESERVE src0_sel:WORD_1\n"
    "v_exp_f16_sdwa v161, v161 dst_sel:WORD_1 dst_unused:UNUSED_PRESERVE src0_sel:WORD_1\n"
    "s_waitcnt lgkmcnt(7)\n"
    "v_mfma_f32_32x32x16_bf16 v[88:103], v[140:143], v[76:79], v[88:103]\n"
    "ds_read_b64_tr_b16 v[152:153], v2 offset:27712\n"
    "ds_read_b64_tr_b16 v[154:155], v2 offset:29248\n"
    "ds_read_u16 v168, v168 offset:0\n"
    "v_pk_fma_f16 v157, v157, s41, s41\n"
    "v_pk_fma_f16 v159, v159, s41, s41\n"
    "v_pk_fma_f16 v161, v161, s41, s41\n"
    "v_rcp_f16_sdwa v157, v157 dst_sel:WORD_0 dst_unused:UNUSED_PRESERVE src0_sel:WORD_0\n"
    "ds_read_u16_d16_hi v169, v169 offset:0\n"
    "s_waitcnt lgkmcnt(8)\n"
    "v_mfma_f32_32x32x16_bf16 v[16:31], v[144:147], v[124:127], v[16:31]\n"
    "ds_read_b64_tr_b16 v[136:137], v1 offset:29696\n"
    "ds_read_b64_tr_b16 v[138:139], v1 offset:29952\n"
    "v_rcp_f16_sdwa v159, v159 dst_sel:WORD_0 dst_unused:UNUSED_PRESERVE src0_sel:WORD_0\n"
    "v_rcp_f16_sdwa v161, v161 dst_sel:WORD_0 dst_unused:UNUSED_PRESERVE src0_sel:WORD_0\n"
    "ds_read_u16 v170, v170 offset:0\n"
    "s_waitcnt lgkmcnt(8)\n"
    "v_mfma_f32_32x32x16_bf16 v[88:103], v[148:151], v[80:83], v[88:103]\n"
    "ds_read_b64_tr_b16 v[140:141], v2 offset:27776\n"
    "ds_read_b64_tr_b16 v[142:143], v2 offset:29312\n"
    "v_rcp_f16_sdwa v157, v157 dst_sel:WORD_1 dst_unused:UNUSED_PRESERVE src0_sel:WORD_1\n"
    "v_rcp_f16_sdwa v159, v159 dst_sel:WORD_1 dst_unused:UNUSED_PRESERVE src0_sel:WORD_1\n"
    "ds_read_u16_d16_hi v171, v171 offset:0\n"
    "s_setprio 2\n"
    "v_rcp_f16_sdwa v161, v161 dst_sel:WORD_1 dst_unused:UNUSED_PRESERVE src0_sel:WORD_1\n"
    "s_waitcnt lgkmcnt(8)\n"
    "v_mfma_f32_32x32x16_bf16 v[32:47], v[152:155], v[124:127], v[32:47]\n"
    "v_fma_mix_f32 v104, v104, v157, 0 op_sel:[0,0,0] op_sel_hi:[0,1,0]\n"
    "ds_read_u16 v172, v172 offset:0\n"
    "v_fma_mix_f32 v106, v106, v159, 0 op_sel:[0,0,0] op_sel_hi:[0,1,0]\n"
    "v_fma_mix_f32 v108, v108, v161, 0 op_sel:[0,0,0] op_sel_hi:[0,1,0]\n"
    "v_fma_mix_f32 v105, v105, v157, 0 op_sel:[0,1,0] op_sel_hi:[0,1,0]\n"
    "s_waitcnt lgkmcnt(5)\n"
    "v_mfma_f32_32x32x16_bf16 v[88:103], v[136:139], v[84:87], v[88:103]\n"
    "v_fma_mix_f32 v107, v107, v159, 0 op_sel:[0,1,0] op_sel_hi:[0,1,0]\n"
    "ds_read_u16_d16_hi v173, v173 offset:0\n"
    "v_fma_mix_f32 v109, v109, v161, 0 op_sel:[0,1,0] op_sel_hi:[0,1,0]\n"
    "v_cvt_pk_bf16_f32 v128, v104, v105\n"
    "v_cvt_pk_bf16_f32 v129, v106, v107\n"
    "v_cvt_pk_bf16_f32 v130, v108, v109\n"
    "s_waitcnt lgkmcnt(3)\n"
    "v_mfma_f32_32x32x16_bf16 v[48:63], v[140:143], v[124:127], v[48:63]\n"
    "v_or_b32_e32 v131, v164, v165\n"
    "v_or_b32_e32 v132, v166, v167\n"
    "v_or_b32_e32 v133, v168, v169\n"
    "s_waitcnt lgkmcnt(2)\n"
    "v_or_b32_e32 v134, v170, v171\n"
    "s_waitcnt lgkmcnt(0)\n"
    "v_or_b32_e32 v135, v172, v173\n"
    "ds_read_b64_tr_b16 v[136:137], v1 offset:36864\n"
    "ds_read_b64_tr_b16 v[138:139], v1 offset:37120\n"
    "ds_read_b64_tr_b16 v[140:141], v2 offset:36864\n"
    "ds_read_b64_tr_b16 v[142:143], v2 offset:38400\n"
    "ds_read_b64_tr_b16 v[144:145], v1 offset:37888\n"
    "ds_read_b64_tr_b16 v[146:147], v1 offset:38144\n"
    "s_setprio 1\n"
    "v_cvt_pk_f16_f32 v165, v94, v95\n"
    "v_cvt_pk_f16_f32 v167, v96, v97\n"
    "v_cvt_pk_f16_f32 v169, v98, v99\n"
    "v_cvt_pk_f16_f32 v171, v100, v101\n"
    "v_cvt_pk_f16_f32 v173, v102, v103\n"
    "s_mov_b32 exec_hi, 0xffff\n"
    "s_add_u32 m0, s59, 3136\n"
    "s_add_u32 s44, s44, 768\n"
    "buffer_load_dwordx4 v3, s[28:31], s44 offen nt lds\n"
    "s_add_u32 m0, s59, 3920\n"
    "s_add_u32 s44, s44, 768\n"
    "buffer_load_dwordx4 v3, s[28:31], s44 offen nt lds\n"
    "s_add_u32 m0, s59, 4704\n"
    "s_add_u32 s44, s44, 768\n"
    "buffer_load_dwordx4 v3, s[28:31], s44 offen nt lds\n"
    "s_add_u32 m0, s59, 5488\n"
    "s_add_u32 s44, s44, 768\n"
    "buffer_load_dwordx4 v3, s[28:31], s44 offen nt lds\n"
    "s_mov_b32 exec_hi, -1\n"
    "v_and_b32_e32 v165, s42, v165\n"
    "v_and_b32_e32 v167, s42, v167\n"
    "v_and_b32_e32 v169, s42, v169\n"
    "v_and_b32_e32 v171, s42, v171\n"
    "v_and_b32_e32 v173, s42, v173\n"
    "v_lshrrev_b32_sdwa v164, 2, v165 dst_sel:DWORD dst_unused:UNUSED_PAD src0_sel:DWORD src1_sel:WORD_0\n"
    "v_lshrrev_b32_sdwa v166, 2, v167 dst_sel:DWORD dst_unused:UNUSED_PAD src0_sel:DWORD src1_sel:WORD_0\n"
    "s_waitcnt lgkmcnt(4)\n"
    "v_mfma_f32_32x32x16_bf16 v[104:119], v[136:139], v[64:67], 0\n"
    "ds_read_b64_tr_b16 v[148:149], v2 offset:36928\n"
    "ds_read_b64_tr_b16 v[150:151], v2 offset:38464\n"
    "v_lshrrev_b32_sdwa v168, 2, v169 dst_sel:DWORD dst_unused:UNUSED_PAD src0_sel:DWORD src1_sel:WORD_0\n"
    "v_lshrrev_b32_sdwa v170, 2, v171 dst_sel:DWORD dst_unused:UNUSED_PAD src0_sel:DWORD src1_sel:WORD_0\n"
    "v_lshrrev_b32_sdwa v172, 2, v173 dst_sel:DWORD dst_unused:UNUSED_PAD src0_sel:DWORD src1_sel:WORD_0\n"
    "v_lshrrev_b32_e32 v165, 18, v165\n"
    "s_waitcnt lgkmcnt(4)\n"
    "v_mfma_f32_32x32x16_bf16 v[16:31], v[140:143], v[128:131], v[16:31]\n"
    "ds_read_b64_tr_b16 v[152:153], v1 offset:38912\n"
    "ds_read_b64_tr_b16 v[154:155], v1 offset:39168\n"
    "v_lshrrev_b32_e32 v167, 18, v167\n"
    "v_lshrrev_b32_e32 v169, 18, v169\n"
    "s_setprio 0\n"
    "v_lshrrev_b32_e32 v171, 18, v171\n"
    "v_lshrrev_b32_e32 v173, 18, v173\n"
    "v_cvt_pk_f16_f32 v156, v88, v89\n"
    "s_waitcnt lgkmcnt(4)\n"
    "v_mfma_f32_32x32x16_bf16 v[104:119], v[144:147], v[68:71], v[104:119]\n"
    "ds_read_b64_tr_b16 v[136:137], v2 offset:36992\n"
    "ds_read_b64_tr_b16 v[138:139], v2 offset:38528\n"
    "v_cvt_pk_f16_f32 v158, v90, v91\n"
    "v_cvt_pk_f16_f32 v160, v92, v93\n"
    "v_pk_fma_f16 v157, v156, v156, s40\n"
    "v_pk_fma_f16 v159, v158, v158, s40\n"
    "ds_read_u16 v164, v164 offset:0\n"
    "v_pk_fma_f16 v161, v160, v160, s40\n"
    "s_waitcnt lgkmcnt(5)\n"
    "v_mfma_f32_32x32x16_bf16 v[32:47], v[148:151], v[128:131], v[32:47]\n"
    "ds_read_b64_tr_b16 v[140:141], v1 offset:39936\n"
    "ds_read_b64_tr_b16 v[142:143], v1 offset:40192\n"
    "v_pk_mul_f16 v157, v157, v156 neg_lo:[1,0] neg_hi:[1,0]\n"
    "v_pk_mul_f16 v159, v159, v158 neg_lo:[1,0] neg_hi:[1,0]\n"
    "v_pk_mul_f16 v161, v161, v160 neg_lo:[1,0] neg_hi:[1,0]\n"
    "ds_read_u16_d16_hi v165, v165 offset:0\n"
    "v_exp_f16_sdwa v157, v157 dst_sel:WORD_0 dst_unused:UNUSED_PRESERVE src0_sel:WORD_0\n"
    "s_waitcnt lgkmcnt(6)\n"
    "v_mfma_f32_32x32x16_bf16 v[104:119], v[152:155], v[72:75], v[104:119]\n"
    "ds_read_b64_tr_b16 v[144:145], v2 offset:39936\n"
    "ds_read_b64_tr_b16 v[146:147], v2 offset:41472\n"
    "v_exp_f16_sdwa v159, v159 dst_sel:WORD_0 dst_unused:UNUSED_PRESERVE src0_sel:WORD_0\n"
    "ds_read_u16 v166, v166 offset:0\n"
    "v_exp_f16_sdwa v161, v161 dst_sel:WORD_0 dst_unused:UNUSED_PRESERVE src0_sel:WORD_0\n"
    "v_exp_f16_sdwa v157, v157 dst_sel:WORD_1 dst_unused:UNUSED_PRESERVE src0_sel:WORD_1\n"
    "s_waitcnt lgkmcnt(7)\n"
    "v_mfma_f32_32x32x16_bf16 v[48:63], v[136:139], v[128:131], v[48:63]\n"
    "s_barrier\n"
    "s_setprio 3\n"
    "ds_read_b64_tr_b16 v[148:149], v1 offset:40960\n"
    "ds_read_b64_tr_b16 v[150:151], v1 offset:41216\n"
    "ds_read_u16_d16_hi v167, v167 offset:0\n"
    "v_exp_f16_sdwa v159, v159 dst_sel:WORD_1 dst_unused:UNUSED_PRESERVE src0_sel:WORD_1\n"
    "v_exp_f16_sdwa v161, v161 dst_sel:WORD_1 dst_unused:UNUSED_PRESERVE src0_sel:WORD_1\n"
    "s_waitcnt lgkmcnt(7)\n"
    "v_mfma_f32_32x32x16_bf16 v[104:119], v[140:143], v[76:79], v[104:119]\n"
    "ds_read_b64_tr_b16 v[152:153], v2 offset:40000\n"
    "ds_read_b64_tr_b16 v[154:155], v2 offset:41536\n"
    "ds_read_u16 v168, v168 offset:0\n"
    "v_pk_fma_f16 v157, v157, s41, s41\n"
    "v_pk_fma_f16 v159, v159, s41, s41\n"
    "v_pk_fma_f16 v161, v161, s41, s41\n"
    "v_rcp_f16_sdwa v157, v157 dst_sel:WORD_0 dst_unused:UNUSED_PRESERVE src0_sel:WORD_0\n"
    "ds_read_u16_d16_hi v169, v169 offset:0\n"
    "s_waitcnt lgkmcnt(8)\n"
    "v_mfma_f32_32x32x16_bf16 v[16:31], v[144:147], v[132:135], v[16:31]\n"
    "ds_read_b64_tr_b16 v[136:137], v1 offset:41984\n"
    "ds_read_b64_tr_b16 v[138:139], v1 offset:42240\n"
    "v_rcp_f16_sdwa v159, v159 dst_sel:WORD_0 dst_unused:UNUSED_PRESERVE src0_sel:WORD_0\n"
    "v_rcp_f16_sdwa v161, v161 dst_sel:WORD_0 dst_unused:UNUSED_PRESERVE src0_sel:WORD_0\n"
    "ds_read_u16 v170, v170 offset:0\n"
    "s_waitcnt lgkmcnt(8)\n"
    "v_mfma_f32_32x32x16_bf16 v[104:119], v[148:151], v[80:83], v[104:119]\n"
    "ds_read_b64_tr_b16 v[140:141], v2 offset:40064\n"
    "ds_read_b64_tr_b16 v[142:143], v2 offset:41600\n"
    "v_rcp_f16_sdwa v157, v157 dst_sel:WORD_1 dst_unused:UNUSED_PRESERVE src0_sel:WORD_1\n"
    "v_rcp_f16_sdwa v159, v159 dst_sel:WORD_1 dst_unused:UNUSED_PRESERVE src0_sel:WORD_1\n"
    "ds_read_u16_d16_hi v171, v171 offset:0\n"
    "s_setprio 2\n"
    "v_rcp_f16_sdwa v161, v161 dst_sel:WORD_1 dst_unused:UNUSED_PRESERVE src0_sel:WORD_1\n"
    "s_waitcnt lgkmcnt(8)\n"
    "v_mfma_f32_32x32x16_bf16 v[32:47], v[152:155], v[132:135], v[32:47]\n"
    "v_fma_mix_f32 v88, v88, v157, 0 op_sel:[0,0,0] op_sel_hi:[0,1,0]\n"
    "ds_read_u16 v172, v172 offset:0\n"
    "v_fma_mix_f32 v90, v90, v159, 0 op_sel:[0,0,0] op_sel_hi:[0,1,0]\n"
    "v_fma_mix_f32 v92, v92, v161, 0 op_sel:[0,0,0] op_sel_hi:[0,1,0]\n"
    "v_fma_mix_f32 v89, v89, v157, 0 op_sel:[0,1,0] op_sel_hi:[0,1,0]\n"
    "s_waitcnt lgkmcnt(5)\n"
    "v_mfma_f32_32x32x16_bf16 v[104:119], v[136:139], v[84:87], v[104:119]\n"
    "v_fma_mix_f32 v91, v91, v159, 0 op_sel:[0,1,0] op_sel_hi:[0,1,0]\n"
    "ds_read_u16_d16_hi v173, v173 offset:0\n"
    "v_fma_mix_f32 v93, v93, v161, 0 op_sel:[0,1,0] op_sel_hi:[0,1,0]\n"
    "v_cvt_pk_bf16_f32 v120, v88, v89\n"
    "v_cvt_pk_bf16_f32 v121, v90, v91\n"
    "v_cvt_pk_bf16_f32 v122, v92, v93\n"
    "s_waitcnt lgkmcnt(3)\n"
    "v_mfma_f32_32x32x16_bf16 v[48:63], v[140:143], v[132:135], v[48:63]\n"
    "v_or_b32_e32 v123, v164, v165\n"
    "v_or_b32_e32 v124, v166, v167\n"
    "v_or_b32_e32 v125, v168, v169\n"
    "s_waitcnt lgkmcnt(2)\n"
    "v_or_b32_e32 v126, v170, v171\n"
    "s_waitcnt lgkmcnt(0)\n"
    "v_or_b32_e32 v127, v172, v173\n"
    "ds_read_b64_tr_b16 v[136:137], v1 offset:0\n"
    "ds_read_b64_tr_b16 v[138:139], v1 offset:256\n"
    "ds_read_b64_tr_b16 v[140:141], v2 offset:0\n"
    "ds_read_b64_tr_b16 v[142:143], v2 offset:1536\n"
    "ds_read_b64_tr_b16 v[144:145], v1 offset:1024\n"
    "ds_read_b64_tr_b16 v[146:147], v1 offset:1280\n"
    "s_setprio 1\n"
    "v_cvt_pk_f16_f32 v165, v110, v111\n"
    "v_cvt_pk_f16_f32 v167, v112, v113\n"
    "v_cvt_pk_f16_f32 v169, v114, v115\n"
    "v_cvt_pk_f16_f32 v171, v116, v117\n"
    "v_cvt_pk_f16_f32 v173, v118, v119\n"
    "s_mov_b32 exec_hi, 0xffff\n"
    "s_add_u32 m0, s59, 6272\n"
    "s_add_u32 s44, s44, 768\n"
    "buffer_load_dwordx4 v5, s[28:31], s44 offen nt lds\n"
    "s_add_u32 s45, s58, 1\n"
    "s_cmp_lt_u32 s45, 512\n"
    "s_cselect_b32 s30, s61, 0\n"
    "s_cselect_b32 s45, s45, 0\n"
    "s_mul_i32 s46, s45, 98304\n"
    "s_add_u32 m0, s59, 7056\n"
    "s_add_u32 s44, s46, s64\n"
    "buffer_load_dwordx4 v4, s[28:31], s44 offen nt lds\n"
    "s_add_u32 m0, s59, 7840\n"
    "s_add_u32 s44, s46, s63\n"
    "buffer_load_dwordx4 v3, s[28:31], s44 offen nt lds\n"
    "s_add_u32 m0, s59, 8624\n"
    "s_add_u32 s44, s44, 768\n"
    "buffer_load_dwordx4 v3, s[28:31], s44 offen nt lds\n"
    "s_mov_b32 exec_hi, -1\n"
    "v_and_b32_e32 v165, s42, v165\n"
    "v_and_b32_e32 v167, s42, v167\n"
    "v_and_b32_e32 v169, s42, v169\n"
    "v_and_b32_e32 v171, s42, v171\n"
    "v_and_b32_e32 v173, s42, v173\n"
    "v_lshrrev_b32_sdwa v164, 2, v165 dst_sel:DWORD dst_unused:UNUSED_PAD src0_sel:DWORD src1_sel:WORD_0\n"
    "v_lshrrev_b32_sdwa v166, 2, v167 dst_sel:DWORD dst_unused:UNUSED_PAD src0_sel:DWORD src1_sel:WORD_0\n"
    "s_waitcnt lgkmcnt(4)\n"
    "v_mfma_f32_32x32x16_bf16 v[88:103], v[136:139], v[64:67], 0\n"
    "ds_read_b64_tr_b16 v[148:149], v2 offset:64\n"
    "ds_read_b64_tr_b16 v[150:151], v2 offset:1600\n"
    "v_lshrrev_b32_sdwa v168, 2, v169 dst_sel:DWORD dst_unused:UNUSED_PAD src0_sel:DWORD src1_sel:WORD_0\n"
    "v_lshrrev_b32_sdwa v170, 2, v171 dst_sel:DWORD dst_unused:UNUSED_PAD src0_sel:DWORD src1_sel:WORD_0\n"
    "v_lshrrev_b32_sdwa v172, 2, v173 dst_sel:DWORD dst_unused:UNUSED_PAD src0_sel:DWORD src1_sel:WORD_0\n"
    "v_lshrrev_b32_e32 v165, 18, v165\n"
    "s_waitcnt lgkmcnt(4)\n"
    "v_mfma_f32_32x32x16_bf16 v[16:31], v[140:143], v[120:123], v[16:31]\n"
    "ds_read_b64_tr_b16 v[152:153], v1 offset:2048\n"
    "ds_read_b64_tr_b16 v[154:155], v1 offset:2304\n"
    "v_lshrrev_b32_e32 v167, 18, v167\n"
    "v_lshrrev_b32_e32 v169, 18, v169\n"
    "s_setprio 0\n"
    "v_lshrrev_b32_e32 v171, 18, v171\n"
    "v_lshrrev_b32_e32 v173, 18, v173\n"
    "v_cvt_pk_f16_f32 v156, v104, v105\n"
    "s_waitcnt lgkmcnt(4)\n"
    "v_mfma_f32_32x32x16_bf16 v[88:103], v[144:147], v[68:71], v[88:103]\n"
    "ds_read_b64_tr_b16 v[136:137], v2 offset:128\n"
    "ds_read_b64_tr_b16 v[138:139], v2 offset:1664\n"
    "v_cvt_pk_f16_f32 v158, v106, v107\n"
    "v_cvt_pk_f16_f32 v160, v108, v109\n"
    "v_pk_fma_f16 v157, v156, v156, s40\n"
    "v_pk_fma_f16 v159, v158, v158, s40\n"
    "ds_read_u16 v164, v164 offset:0\n"
    "v_pk_fma_f16 v161, v160, v160, s40\n"
    "s_waitcnt lgkmcnt(5)\n"
    "v_mfma_f32_32x32x16_bf16 v[32:47], v[148:151], v[120:123], v[32:47]\n"
    "ds_read_b64_tr_b16 v[140:141], v1 offset:3072\n"
    "ds_read_b64_tr_b16 v[142:143], v1 offset:3328\n"
    "v_pk_mul_f16 v157, v157, v156 neg_lo:[1,0] neg_hi:[1,0]\n"
    "v_pk_mul_f16 v159, v159, v158 neg_lo:[1,0] neg_hi:[1,0]\n"
    "v_pk_mul_f16 v161, v161, v160 neg_lo:[1,0] neg_hi:[1,0]\n"
    "ds_read_u16_d16_hi v165, v165 offset:0\n"
    "v_exp_f16_sdwa v157, v157 dst_sel:WORD_0 dst_unused:UNUSED_PRESERVE src0_sel:WORD_0\n"
    "s_waitcnt lgkmcnt(6)\n"
    "v_mfma_f32_32x32x16_bf16 v[88:103], v[152:155], v[72:75], v[88:103]\n"
    "ds_read_b64_tr_b16 v[144:145], v2 offset:3072\n"
    "ds_read_b64_tr_b16 v[146:147], v2 offset:4608\n"
    "v_exp_f16_sdwa v159, v159 dst_sel:WORD_0 dst_unused:UNUSED_PRESERVE src0_sel:WORD_0\n"
    "ds_read_u16 v166, v166 offset:0\n"
    "v_exp_f16_sdwa v161, v161 dst_sel:WORD_0 dst_unused:UNUSED_PRESERVE src0_sel:WORD_0\n"
    "v_exp_f16_sdwa v157, v157 dst_sel:WORD_1 dst_unused:UNUSED_PRESERVE src0_sel:WORD_1\n"
    "s_waitcnt lgkmcnt(7)\n"
    "v_mfma_f32_32x32x16_bf16 v[48:63], v[136:139], v[120:123], v[48:63]\n"
    "s_barrier\n"
    "s_setprio 3\n"
    "ds_read_b64_tr_b16 v[148:149], v1 offset:4096\n"
    "ds_read_b64_tr_b16 v[150:151], v1 offset:4352\n"
    "ds_read_u16_d16_hi v167, v167 offset:0\n"
    "v_exp_f16_sdwa v159, v159 dst_sel:WORD_1 dst_unused:UNUSED_PRESERVE src0_sel:WORD_1\n"
    "v_exp_f16_sdwa v161, v161 dst_sel:WORD_1 dst_unused:UNUSED_PRESERVE src0_sel:WORD_1\n"
    "s_waitcnt lgkmcnt(7)\n"
    "v_mfma_f32_32x32x16_bf16 v[88:103], v[140:143], v[76:79], v[88:103]\n"
    "ds_read_b64_tr_b16 v[152:153], v2 offset:3136\n"
    "ds_read_b64_tr_b16 v[154:155], v2 offset:4672\n"
    "ds_read_u16 v168, v168 offset:0\n"
    "v_pk_fma_f16 v157, v157, s41, s41\n"
    "v_pk_fma_f16 v159, v159, s41, s41\n"
    "v_pk_fma_f16 v161, v161, s41, s41\n"
    "v_rcp_f16_sdwa v157, v157 dst_sel:WORD_0 dst_unused:UNUSED_PRESERVE src0_sel:WORD_0\n"
    "ds_read_u16_d16_hi v169, v169 offset:0\n"
    "s_waitcnt lgkmcnt(8)\n"
    "v_mfma_f32_32x32x16_bf16 v[16:31], v[144:147], v[124:127], v[16:31]\n"
    "ds_read_b64_tr_b16 v[136:137], v1 offset:5120\n"
    "ds_read_b64_tr_b16 v[138:139], v1 offset:5376\n"
    "v_rcp_f16_sdwa v159, v159 dst_sel:WORD_0 dst_unused:UNUSED_PRESERVE src0_sel:WORD_0\n"
    "v_rcp_f16_sdwa v161, v161 dst_sel:WORD_0 dst_unused:UNUSED_PRESERVE src0_sel:WORD_0\n"
    "ds_read_u16 v170, v170 offset:0\n"
    "s_waitcnt lgkmcnt(8)\n"
    "v_mfma_f32_32x32x16_bf16 v[88:103], v[148:151], v[80:83], v[88:103]\n"
    "ds_read_b64_tr_b16 v[140:141], v2 offset:3200\n"
    "ds_read_b64_tr_b16 v[142:143], v2 offset:4736\n"
    "v_rcp_f16_sdwa v157, v157 dst_sel:WORD_1 dst_unused:UNUSED_PRESERVE src0_sel:WORD_1\n"
    "v_rcp_f16_sdwa v159, v159 dst_sel:WORD_1 dst_unused:UNUSED_PRESERVE src0_sel:WORD_1\n"
    "ds_read_u16_d16_hi v171, v171 offset:0\n"
    "s_setprio 2\n"
    "v_rcp_f16_sdwa v161, v161 dst_sel:WORD_1 dst_unused:UNUSED_PRESERVE src0_sel:WORD_1\n"
    "s_waitcnt lgkmcnt(8)\n"
    "v_mfma_f32_32x32x16_bf16 v[32:47], v[152:155], v[124:127], v[32:47]\n"
    "v_fma_mix_f32 v104, v104, v157, 0 op_sel:[0,0,0] op_sel_hi:[0,1,0]\n"
    "ds_read_u16 v172, v172 offset:0\n"
    "v_fma_mix_f32 v106, v106, v159, 0 op_sel:[0,0,0] op_sel_hi:[0,1,0]\n"
    "v_fma_mix_f32 v108, v108, v161, 0 op_sel:[0,0,0] op_sel_hi:[0,1,0]\n"
    "v_fma_mix_f32 v105, v105, v157, 0 op_sel:[0,1,0] op_sel_hi:[0,1,0]\n"
    "s_waitcnt lgkmcnt(5)\n"
    "v_mfma_f32_32x32x16_bf16 v[88:103], v[136:139], v[84:87], v[88:103]\n"
    "v_fma_mix_f32 v107, v107, v159, 0 op_sel:[0,1,0] op_sel_hi:[0,1,0]\n"
    "ds_read_u16_d16_hi v173, v173 offset:0\n"
    "v_fma_mix_f32 v109, v109, v161, 0 op_sel:[0,1,0] op_sel_hi:[0,1,0]\n"
    "v_cvt_pk_bf16_f32 v128, v104, v105\n"
    "v_cvt_pk_bf16_f32 v129, v106, v107\n"
    "v_cvt_pk_bf16_f32 v130, v108, v109\n"
    "s_waitcnt lgkmcnt(3)\n"
    "v_mfma_f32_32x32x16_bf16 v[48:63], v[140:143], v[124:127], v[48:63]\n"
    "v_or_b32_e32 v131, v164, v165\n"
    "v_or_b32_e32 v132, v166, v167\n"
    "v_or_b32_e32 v133, v168, v169\n"
    "s_waitcnt lgkmcnt(2)\n"
    "v_or_b32_e32 v134, v170, v171\n"
    "s_waitcnt lgkmcnt(0)\n"
    "v_or_b32_e32 v135, v172, v173\n"
    "ds_read_b64_tr_b16 v[136:137], v1 offset:12288\n"
    "ds_read_b64_tr_b16 v[138:139], v1 offset:12544\n"
    "ds_read_b64_tr_b16 v[140:141], v2 offset:12288\n"
    "ds_read_b64_tr_b16 v[142:143], v2 offset:13824\n"
    "ds_read_b64_tr_b16 v[144:145], v1 offset:13312\n"
    "ds_read_b64_tr_b16 v[146:147], v1 offset:13568\n"
    "s_setprio 1\n"
    "v_cvt_pk_f16_f32 v165, v94, v95\n"
    "v_cvt_pk_f16_f32 v167, v96, v97\n"
    "v_cvt_pk_f16_f32 v169, v98, v99\n"
    "v_cvt_pk_f16_f32 v171, v100, v101\n"
    "v_cvt_pk_f16_f32 v173, v102, v103\n"
    "s_mov_b32 exec_hi, 0xffff\n"
    "s_add_u32 m0, s59, 9408\n"
    "s_add_u32 s44, s44, 768\n"
    "buffer_load_dwordx4 v3, s[28:31], s44 offen nt lds\n"
    "s_add_u32 m0, s59, 10192\n"
    "s_add_u32 s44, s44, 768\n"
    "buffer_load_dwordx4 v3, s[28:31], s44 offen nt lds\n"
    "s_add_u32 m0, s59, 10976\n"
    "s_add_u32 s44, s44, 768\n"
    "buffer_load_dwordx4 v3, s[28:31], s44 offen nt lds\n"
    "s_add_u32 m0, s59, 11760\n"
    "s_add_u32 s44, s44, 768\n"
    "buffer_load_dwordx4 v3, s[28:31], s44 offen nt lds\n"
    "s_mov_b32 exec_hi, -1\n"
    "v_and_b32_e32 v165, s42, v165\n"
    "v_and_b32_e32 v167, s42, v167\n"
    "v_and_b32_e32 v169, s42, v169\n"
    "v_and_b32_e32 v171, s42, v171\n"
    "v_and_b32_e32 v173, s42, v173\n"
    "v_lshrrev_b32_sdwa v164, 2, v165 dst_sel:DWORD dst_unused:UNUSED_PAD src0_sel:DWORD src1_sel:WORD_0\n"
    "v_lshrrev_b32_sdwa v166, 2, v167 dst_sel:DWORD dst_unused:UNUSED_PAD src0_sel:DWORD src1_sel:WORD_0\n"
    "s_waitcnt lgkmcnt(4)\n"
    "v_mfma_f32_32x32x16_bf16 v[104:119], v[136:139], v[64:67], 0\n"
    "ds_read_b64_tr_b16 v[148:149], v2 offset:12352\n"
    "ds_read_b64_tr_b16 v[150:151], v2 offset:13888\n"
    "v_lshrrev_b32_sdwa v168, 2, v169 dst_sel:DWORD dst_unused:UNUSED_PAD src0_sel:DWORD src1_sel:WORD_0\n"
    "v_lshrrev_b32_sdwa v170, 2, v171 dst_sel:DWORD dst_unused:UNUSED_PAD src0_sel:DWORD src1_sel:WORD_0\n"
    "v_lshrrev_b32_sdwa v172, 2, v173 dst_sel:DWORD dst_unused:UNUSED_PAD src0_sel:DWORD src1_sel:WORD_0\n"
    "v_lshrrev_b32_e32 v165, 18, v165\n"
    "s_waitcnt lgkmcnt(4)\n"
    "v_mfma_f32_32x32x16_bf16 v[16:31], v[140:143], v[128:131], v[16:31]\n"
    "ds_read_b64_tr_b16 v[152:153], v1 offset:14336\n"
    "ds_read_b64_tr_b16 v[154:155], v1 offset:14592\n"
    "v_lshrrev_b32_e32 v167, 18, v167\n"
    "v_lshrrev_b32_e32 v169, 18, v169\n"
    "s_setprio 0\n"
    "v_lshrrev_b32_e32 v171, 18, v171\n"
    "v_lshrrev_b32_e32 v173, 18, v173\n"
    "v_cvt_pk_f16_f32 v156, v88, v89\n"
    "s_waitcnt lgkmcnt(4)\n"
    "v_mfma_f32_32x32x16_bf16 v[104:119], v[144:147], v[68:71], v[104:119]\n"
    "ds_read_b64_tr_b16 v[136:137], v2 offset:12416\n"
    "ds_read_b64_tr_b16 v[138:139], v2 offset:13952\n"
    "v_cvt_pk_f16_f32 v158, v90, v91\n"
    "v_cvt_pk_f16_f32 v160, v92, v93\n"
    "v_pk_fma_f16 v157, v156, v156, s40\n"
    "v_pk_fma_f16 v159, v158, v158, s40\n"
    "ds_read_u16 v164, v164 offset:0\n"
    "v_pk_fma_f16 v161, v160, v160, s40\n"
    "s_waitcnt lgkmcnt(5)\n"
    "v_mfma_f32_32x32x16_bf16 v[32:47], v[148:151], v[128:131], v[32:47]\n"
    "ds_read_b64_tr_b16 v[140:141], v1 offset:15360\n"
    "ds_read_b64_tr_b16 v[142:143], v1 offset:15616\n"
    "v_pk_mul_f16 v157, v157, v156 neg_lo:[1,0] neg_hi:[1,0]\n"
    "v_pk_mul_f16 v159, v159, v158 neg_lo:[1,0] neg_hi:[1,0]\n"
    "v_pk_mul_f16 v161, v161, v160 neg_lo:[1,0] neg_hi:[1,0]\n"
    "ds_read_u16_d16_hi v165, v165 offset:0\n"
    "v_exp_f16_sdwa v157, v157 dst_sel:WORD_0 dst_unused:UNUSED_PRESERVE src0_sel:WORD_0\n"
    "s_waitcnt lgkmcnt(6)\n"
    "v_mfma_f32_32x32x16_bf16 v[104:119], v[152:155], v[72:75], v[104:119]\n"
    "ds_read_b64_tr_b16 v[144:145], v2 offset:15360\n"
    "ds_read_b64_tr_b16 v[146:147], v2 offset:16896\n"
    "v_exp_f16_sdwa v159, v159 dst_sel:WORD_0 dst_unused:UNUSED_PRESERVE src0_sel:WORD_0\n"
    "ds_read_u16 v166, v166 offset:0\n"
    "v_exp_f16_sdwa v161, v161 dst_sel:WORD_0 dst_unused:UNUSED_PRESERVE src0_sel:WORD_0\n"
    "v_exp_f16_sdwa v157, v157 dst_sel:WORD_1 dst_unused:UNUSED_PRESERVE src0_sel:WORD_1\n"
    "s_waitcnt lgkmcnt(7)\n"
    "v_mfma_f32_32x32x16_bf16 v[48:63], v[136:139], v[128:131], v[48:63]\n"
    "s_barrier\n"
    "s_setprio 3\n"
    "ds_read_b64_tr_b16 v[148:149], v1 offset:16384\n"
    "ds_read_b64_tr_b16 v[150:151], v1 offset:16640\n"
    "ds_read_u16_d16_hi v167, v167 offset:0\n"
    "v_exp_f16_sdwa v159, v159 dst_sel:WORD_1 dst_unused:UNUSED_PRESERVE src0_sel:WORD_1\n"
    "v_exp_f16_sdwa v161, v161 dst_sel:WORD_1 dst_unused:UNUSED_PRESERVE src0_sel:WORD_1\n"
    "s_waitcnt lgkmcnt(7)\n"
    "v_mfma_f32_32x32x16_bf16 v[104:119], v[140:143], v[76:79], v[104:119]\n"
    "ds_read_b64_tr_b16 v[152:153], v2 offset:15424\n"
    "ds_read_b64_tr_b16 v[154:155], v2 offset:16960\n"
    "ds_read_u16 v168, v168 offset:0\n"
    "v_pk_fma_f16 v157, v157, s41, s41\n"
    "v_pk_fma_f16 v159, v159, s41, s41\n"
    "v_pk_fma_f16 v161, v161, s41, s41\n"
    "v_rcp_f16_sdwa v157, v157 dst_sel:WORD_0 dst_unused:UNUSED_PRESERVE src0_sel:WORD_0\n"
    "ds_read_u16_d16_hi v169, v169 offset:0\n"
    "s_waitcnt lgkmcnt(8)\n"
    "v_mfma_f32_32x32x16_bf16 v[16:31], v[144:147], v[132:135], v[16:31]\n"
    "ds_read_b64_tr_b16 v[136:137], v1 offset:17408\n"
    "ds_read_b64_tr_b16 v[138:139], v1 offset:17664\n"
    "v_rcp_f16_sdwa v159, v159 dst_sel:WORD_0 dst_unused:UNUSED_PRESERVE src0_sel:WORD_0\n"
    "v_rcp_f16_sdwa v161, v161 dst_sel:WORD_0 dst_unused:UNUSED_PRESERVE src0_sel:WORD_0\n"
    "ds_read_u16 v170, v170 offset:0\n"
    "s_waitcnt lgkmcnt(8)\n"
    "v_mfma_f32_32x32x16_bf16 v[104:119], v[148:151], v[80:83], v[104:119]\n"
    "ds_read_b64_tr_b16 v[140:141], v2 offset:15488\n"
    "ds_read_b64_tr_b16 v[142:143], v2 offset:17024\n"
    "v_rcp_f16_sdwa v157, v157 dst_sel:WORD_1 dst_unused:UNUSED_PRESERVE src0_sel:WORD_1\n"
    "v_rcp_f16_sdwa v159, v159 dst_sel:WORD_1 dst_unused:UNUSED_PRESERVE src0_sel:WORD_1\n"
    "ds_read_u16_d16_hi v171, v171 offset:0\n"
    "s_setprio 2\n"
    "v_rcp_f16_sdwa v161, v161 dst_sel:WORD_1 dst_unused:UNUSED_PRESERVE src0_sel:WORD_1\n"
    "s_waitcnt lgkmcnt(8)\n"
    "v_mfma_f32_32x32x16_bf16 v[32:47], v[152:155], v[132:135], v[32:47]\n"
    "v_fma_mix_f32 v88, v88, v157, 0 op_sel:[0,0,0] op_sel_hi:[0,1,0]\n"
    "ds_read_u16 v172, v172 offset:0\n"
    "v_fma_mix_f32 v90, v90, v159, 0 op_sel:[0,0,0] op_sel_hi:[0,1,0]\n"
    "v_fma_mix_f32 v92, v92, v161, 0 op_sel:[0,0,0] op_sel_hi:[0,1,0]\n"
    "v_fma_mix_f32 v89, v89, v157, 0 op_sel:[0,1,0] op_sel_hi:[0,1,0]\n"
    "s_waitcnt lgkmcnt(5)\n"
    "v_mfma_f32_32x32x16_bf16 v[104:119], v[136:139], v[84:87], v[104:119]\n"
    "v_fma_mix_f32 v91, v91, v159, 0 op_sel:[0,1,0] op_sel_hi:[0,1,0]\n"
    "ds_read_u16_d16_hi v173, v173 offset:0\n"
    "v_fma_mix_f32 v93, v93, v161, 0 op_sel:[0,1,0] op_sel_hi:[0,1,0]\n"
    "v_cvt_pk_bf16_f32 v120, v88, v89\n"
    "v_cvt_pk_bf16_f32 v121, v90, v91\n"
    "v_cvt_pk_bf16_f32 v122, v92, v93\n"
    "s_waitcnt lgkmcnt(3)\n"
    "v_mfma_f32_32x32x16_bf16 v[48:63], v[140:143], v[132:135], v[48:63]\n"
    "v_or_b32_e32 v123, v164, v165\n"
    "v_or_b32_e32 v124, v166, v167\n"
    "v_or_b32_e32 v125, v168, v169\n"
    "s_waitcnt lgkmcnt(2)\n"
    "v_or_b32_e32 v126, v170, v171\n"
    "s_waitcnt lgkmcnt(0)\n"
    "v_or_b32_e32 v127, v172, v173\n"
    "ds_read_b64_tr_b16 v[136:137], v1 offset:24576\n"
    "ds_read_b64_tr_b16 v[138:139], v1 offset:24832\n"
    "ds_read_b64_tr_b16 v[140:141], v2 offset:24576\n"
    "ds_read_b64_tr_b16 v[142:143], v2 offset:26112\n"
    "ds_read_b64_tr_b16 v[144:145], v1 offset:25600\n"
    "ds_read_b64_tr_b16 v[146:147], v1 offset:25856\n"
    "s_setprio 1\n"
    "v_cvt_pk_f16_f32 v165, v110, v111\n"
    "v_cvt_pk_f16_f32 v167, v112, v113\n"
    "v_cvt_pk_f16_f32 v169, v114, v115\n"
    "v_cvt_pk_f16_f32 v171, v116, v117\n"
    "v_cvt_pk_f16_f32 v173, v118, v119\n"
    "s_mov_b32 exec_hi, 0xffff\n"
    "s_add_u32 m0, s59, 12544\n"
    "s_add_u32 s44, s44, 768\n"
    "buffer_load_dwordx4 v3, s[28:31], s44 offen nt lds\n"
    "s_add_u32 m0, s59, 13328\n"
    "s_add_u32 s44, s44, 768\n"
    "buffer_load_dwordx4 v5, s[28:31], s44 offen nt lds\n"
    "s_add_u32 s45, s58, 2\n"
    "s_cmp_lt_u32 s45, 512\n"
    "s_cselect_b32 s30, s61, 0\n"
    "s_cselect_b32 s45, s45, 0\n"
    "s_mul_i32 s46, s45, 98304\n"
    "s_add_u32 m0, s59, 14112\n"
    "s_add_u32 s44, s46, s64\n"
    "buffer_load_dwordx4 v4, s[28:31], s44 offen nt lds\n"
    "s_add_u32 m0, s59, 14896\n"
    "s_add_u32 s44, s46, s63\n"
    "buffer_load_dwordx4 v3, s[28:31], s44 offen nt lds\n"
    "s_mov_b32 exec_hi, -1\n"
    "v_and_b32_e32 v165, s42, v165\n"
    "v_and_b32_e32 v167, s42, v167\n"
    "v_and_b32_e32 v169, s42, v169\n"
    "v_and_b32_e32 v171, s42, v171\n"
    "v_and_b32_e32 v173, s42, v173\n"
    "v_lshrrev_b32_sdwa v164, 2, v165 dst_sel:DWORD dst_unused:UNUSED_PAD src0_sel:DWORD src1_sel:WORD_0\n"
    "v_lshrrev_b32_sdwa v166, 2, v167 dst_sel:DWORD dst_unused:UNUSED_PAD src0_sel:DWORD src1_sel:WORD_0\n"
    "s_waitcnt lgkmcnt(4)\n"
    "v_mfma_f32_32x32x16_bf16 v[88:103], v[136:139], v[64:67], 0\n"
    "ds_read_b64_tr_b16 v[148:149], v2 offset:24640\n"
    "ds_read_b64_tr_b16 v[150:151], v2 offset:26176\n"
    "v_lshrrev_b32_sdwa v168, 2, v169 dst_sel:DWORD dst_unused:UNUSED_PAD src0_sel:DWORD src1_sel:WORD_0\n"
    "v_lshrrev_b32_sdwa v170, 2, v171 dst_sel:DWORD dst_unused:UNUSED_PAD src0_sel:DWORD src1_sel:WORD_0\n"
    "v_lshrrev_b32_sdwa v172, 2, v173 dst_sel:DWORD dst_unused:UNUSED_PAD src0_sel:DWORD src1_sel:WORD_0\n"
    "v_lshrrev_b32_e32 v165, 18, v165\n"
    "s_waitcnt lgkmcnt(4)\n"
    "v_mfma_f32_32x32x16_bf16 v[16:31], v[140:143], v[120:123], v[16:31]\n"
    "ds_read_b64_tr_b16 v[152:153], v1 offset:26624\n"
    "ds_read_b64_tr_b16 v[154:155], v1 offset:26880\n"
    "v_lshrrev_b32_e32 v167, 18, v167\n"
    "v_lshrrev_b32_e32 v169, 18, v169\n"
    "s_setprio 0\n"
    "v_lshrrev_b32_e32 v171, 18, v171\n"
    "v_lshrrev_b32_e32 v173, 18, v173\n"
    "v_cvt_pk_f16_f32 v156, v104, v105\n"
    "s_waitcnt lgkmcnt(4)\n"
    "v_mfma_f32_32x32x16_bf16 v[88:103], v[144:147], v[68:71], v[88:103]\n"
    "ds_read_b64_tr_b16 v[136:137], v2 offset:24704\n"
    "ds_read_b64_tr_b16 v[138:139], v2 offset:26240\n"
    "v_cvt_pk_f16_f32 v158, v106, v107\n"
    "v_cvt_pk_f16_f32 v160, v108, v109\n"
    "v_pk_fma_f16 v157, v156, v156, s40\n"
    "v_pk_fma_f16 v159, v158, v158, s40\n"
    "ds_read_u16 v164, v164 offset:0\n"
    "v_pk_fma_f16 v161, v160, v160, s40\n"
    "s_waitcnt lgkmcnt(5)\n"
    "v_mfma_f32_32x32x16_bf16 v[32:47], v[148:151], v[120:123], v[32:47]\n"
    "ds_read_b64_tr_b16 v[140:141], v1 offset:27648\n"
    "ds_read_b64_tr_b16 v[142:143], v1 offset:27904\n"
    "v_pk_mul_f16 v157, v157, v156 neg_lo:[1,0] neg_hi:[1,0]\n"
    "v_pk_mul_f16 v159, v159, v158 neg_lo:[1,0] neg_hi:[1,0]\n"
    "v_pk_mul_f16 v161, v161, v160 neg_lo:[1,0] neg_hi:[1,0]\n"
    "ds_read_u16_d16_hi v165, v165 offset:0\n"
    "v_exp_f16_sdwa v157, v157 dst_sel:WORD_0 dst_unused:UNUSED_PRESERVE src0_sel:WORD_0\n"
    "s_waitcnt lgkmcnt(6)\n"
    "v_mfma_f32_32x32x16_bf16 v[88:103], v[152:155], v[72:75], v[88:103]\n"
    "ds_read_b64_tr_b16 v[144:145], v2 offset:27648\n"
    "ds_read_b64_tr_b16 v[146:147], v2 offset:29184\n"
    "v_exp_f16_sdwa v159, v159 dst_sel:WORD_0 dst_unused:UNUSED_PRESERVE src0_sel:WORD_0\n"
    "ds_read_u16 v166, v166 offset:0\n"
    "v_exp_f16_sdwa v161, v161 dst_sel:WORD_0 dst_unused:UNUSED_PRESERVE src0_sel:WORD_0\n"
    "v_exp_f16_sdwa v157, v157 dst_sel:WORD_1 dst_unused:UNUSED_PRESERVE src0_sel:WORD_1\n"
    "s_waitcnt lgkmcnt(7)\n"
    "v_mfma_f32_32x32x16_bf16 v[48:63], v[136:139], v[120:123], v[48:63]\n"
    "s_barrier\n"
    "s_setprio 3\n"
    "ds_read_b64_tr_b16 v[148:149], v1 offset:28672\n"
    "ds_read_b64_tr_b16 v[150:151], v1 offset:28928\n"
    "ds_read_u16_d16_hi v167, v167 offset:0\n"
    "v_exp_f16_sdwa v159, v159 dst_sel:WORD_1 dst_unused:UNUSED_PRESERVE src0_sel:WORD_1\n"
    "v_exp_f16_sdwa v161, v161 dst_sel:WORD_1 dst_unused:UNUSED_PRESERVE src0_sel:WORD_1\n"
    "s_waitcnt lgkmcnt(7)\n"
    "v_mfma_f32_32x32x16_bf16 v[88:103], v[140:143], v[76:79], v[88:103]\n"
    "ds_read_b64_tr_b16 v[152:153], v2 offset:27712\n"
    "ds_read_b64_tr_b16 v[154:155], v2 offset:29248\n"
    "ds_read_u16 v168, v168 offset:0\n"
    "v_pk_fma_f16 v157, v157, s41, s41\n"
    "v_pk_fma_f16 v159, v159, s41, s41\n"
    "v_pk_fma_f16 v161, v161, s41, s41\n"
    "v_rcp_f16_sdwa v157, v157 dst_sel:WORD_0 dst_unused:UNUSED_PRESERVE src0_sel:WORD_0\n"
    "ds_read_u16_d16_hi v169, v169 offset:0\n"
    "s_waitcnt lgkmcnt(8)\n"
    "v_mfma_f32_32x32x16_bf16 v[16:31], v[144:147], v[124:127], v[16:31]\n"
    "ds_read_b64_tr_b16 v[136:137], v1 offset:29696\n"
    "ds_read_b64_tr_b16 v[138:139], v1 offset:29952\n"
    "v_rcp_f16_sdwa v159, v159 dst_sel:WORD_0 dst_unused:UNUSED_PRESERVE src0_sel:WORD_0\n"
    "v_rcp_f16_sdwa v161, v161 dst_sel:WORD_0 dst_unused:UNUSED_PRESERVE src0_sel:WORD_0\n"
    "ds_read_u16 v170, v170 offset:0\n"
    "s_waitcnt lgkmcnt(8)\n"
    "v_mfma_f32_32x32x16_bf16 v[88:103], v[148:151], v[80:83], v[88:103]\n"
    "ds_read_b64_tr_b16 v[140:141], v2 offset:27776\n"
    "ds_read_b64_tr_b16 v[142:143], v2 offset:29312\n"
    "v_rcp_f16_sdwa v157, v157 dst_sel:WORD_1 dst_unused:UNUSED_PRESERVE src0_sel:WORD_1\n"
    "v_rcp_f16_sdwa v159, v159 dst_sel:WORD_1 dst_unused:UNUSED_PRESERVE src0_sel:WORD_1\n"
    "ds_read_u16_d16_hi v171, v171 offset:0\n"
    "s_setprio 2\n"
    "v_rcp_f16_sdwa v161, v161 dst_sel:WORD_1 dst_unused:UNUSED_PRESERVE src0_sel:WORD_1\n"
    "s_waitcnt lgkmcnt(8)\n"
    "v_mfma_f32_32x32x16_bf16 v[32:47], v[152:155], v[124:127], v[32:47]\n"
    "v_fma_mix_f32 v104, v104, v157, 0 op_sel:[0,0,0] op_sel_hi:[0,1,0]\n"
    "ds_read_u16 v172, v172 offset:0\n"
    "v_fma_mix_f32 v106, v106, v159, 0 op_sel:[0,0,0] op_sel_hi:[0,1,0]\n"
    "v_fma_mix_f32 v108, v108, v161, 0 op_sel:[0,0,0] op_sel_hi:[0,1,0]\n"
    "v_fma_mix_f32 v105, v105, v157, 0 op_sel:[0,1,0] op_sel_hi:[0,1,0]\n"
    "s_waitcnt lgkmcnt(5)\n"
    "v_mfma_f32_32x32x16_bf16 v[88:103], v[136:139], v[84:87], v[88:103]\n"
    "v_fma_mix_f32 v107, v107, v159, 0 op_sel:[0,1,0] op_sel_hi:[0,1,0]\n"
    "ds_read_u16_d16_hi v173, v173 offset:0\n"
    "v_fma_mix_f32 v109, v109, v161, 0 op_sel:[0,1,0] op_sel_hi:[0,1,0]\n"
    "v_cvt_pk_bf16_f32 v128, v104, v105\n"
    "v_cvt_pk_bf16_f32 v129, v106, v107\n"
    "v_cvt_pk_bf16_f32 v130, v108, v109\n"
    "s_waitcnt lgkmcnt(3)\n"
    "v_mfma_f32_32x32x16_bf16 v[48:63], v[140:143], v[124:127], v[48:63]\n"
    "v_or_b32_e32 v131, v164, v165\n"
    "v_or_b32_e32 v132, v166, v167\n"
    "v_or_b32_e32 v133, v168, v169\n"
    "s_waitcnt lgkmcnt(2)\n"
    "v_or_b32_e32 v134, v170, v171\n"
    "s_waitcnt lgkmcnt(0)\n"
    "v_or_b32_e32 v135, v172, v173\n"
    "ds_read_b64_tr_b16 v[136:137], v1 offset:36864\n"
    "ds_read_b64_tr_b16 v[138:139], v1 offset:37120\n"
    "ds_read_b64_tr_b16 v[140:141], v2 offset:36864\n"
    "ds_read_b64_tr_b16 v[142:143], v2 offset:38400\n"
    "ds_read_b64_tr_b16 v[144:145], v1 offset:37888\n"
    "ds_read_b64_tr_b16 v[146:147], v1 offset:38144\n"
    "s_setprio 1\n"
    "v_cvt_pk_f16_f32 v165, v94, v95\n"
    "v_cvt_pk_f16_f32 v167, v96, v97\n"
    "v_cvt_pk_f16_f32 v169, v98, v99\n"
    "v_cvt_pk_f16_f32 v171, v100, v101\n"
    "v_cvt_pk_f16_f32 v173, v102, v103\n"
    "s_mov_b32 exec_hi, 0xffff\n"
    "s_add_u32 m0, s59, 15680\n"
    "s_add_u32 s44, s44, 768\n"
    "buffer_load_dwordx4 v3, s[28:31], s44 offen nt lds\n"
    "s_add_u32 m0, s59, 16464\n"
    "s_add_u32 s44, s44, 768\n"
    "buffer_load_dwordx4 v3, s[28:31], s44 offen nt lds\n"
    "s_add_u32 m0, s59, 17248\n"
    "s_add_u32 s44, s44, 768\n"
    "buffer_load_dwordx4 v3, s[28:31], s44 offen nt lds\n"
    "s_add_u32 m0, s59, 18032\n"
    "s_add_u32 s44, s44, 768\n"
    "buffer_load_dwordx4 v3, s[28:31], s44 offen nt lds\n"
    "s_mov_b32 exec_hi, -1\n"
    "v_and_b32_e32 v165, s42, v165\n"
    "v_and_b32_e32 v167, s42, v167\n"
    "v_and_b32_e32 v169, s42, v169\n"
    "v_and_b32_e32 v171, s42, v171\n"
    "v_and_b32_e32 v173, s42, v173\n"
    "v_lshrrev_b32_sdwa v164, 2, v165 dst_sel:DWORD dst_unused:UNUSED_PAD src0_sel:DWORD src1_sel:WORD_0\n"
    "v_lshrrev_b32_sdwa v166, 2, v167 dst_sel:DWORD dst_unused:UNUSED_PAD src0_sel:DWORD src1_sel:WORD_0\n"
    "s_waitcnt lgkmcnt(4)\n"
    "v_mfma_f32_32x32x16_bf16 v[104:119], v[136:139], v[64:67], 0\n"
    "ds_read_b64_tr_b16 v[148:149], v2 offset:36928\n"
    "ds_read_b64_tr_b16 v[150:151], v2 offset:38464\n"
    "v_lshrrev_b32_sdwa v168, 2, v169 dst_sel:DWORD dst_unused:UNUSED_PAD src0_sel:DWORD src1_sel:WORD_0\n"
    "v_lshrrev_b32_sdwa v170, 2, v171 dst_sel:DWORD dst_unused:UNUSED_PAD src0_sel:DWORD src1_sel:WORD_0\n"
    "v_lshrrev_b32_sdwa v172, 2, v173 dst_sel:DWORD dst_unused:UNUSED_PAD src0_sel:DWORD src1_sel:WORD_0\n"
    "v_lshrrev_b32_e32 v165, 18, v165\n"
    "s_waitcnt lgkmcnt(4)\n"
    "v_mfma_f32_32x32x16_bf16 v[16:31], v[140:143], v[128:131], v[16:31]\n"
    "ds_read_b64_tr_b16 v[152:153], v1 offset:38912\n"
    "ds_read_b64_tr_b16 v[154:155], v1 offset:39168\n"
    "v_lshrrev_b32_e32 v167, 18, v167\n"
    "v_lshrrev_b32_e32 v169, 18, v169\n"
    "s_setprio 0\n"
    "v_lshrrev_b32_e32 v171, 18, v171\n"
    "v_lshrrev_b32_e32 v173, 18, v173\n"
    "v_cvt_pk_f16_f32 v156, v88, v89\n"
    "s_waitcnt lgkmcnt(4)\n"
    "v_mfma_f32_32x32x16_bf16 v[104:119], v[144:147], v[68:71], v[104:119]\n"
    "ds_read_b64_tr_b16 v[136:137], v2 offset:36992\n"
    "ds_read_b64_tr_b16 v[138:139], v2 offset:38528\n"
    "v_cvt_pk_f16_f32 v158, v90, v91\n"
    "v_cvt_pk_f16_f32 v160, v92, v93\n"
    "v_pk_fma_f16 v157, v156, v156, s40\n"
    "v_pk_fma_f16 v159, v158, v158, s40\n"
    "ds_read_u16 v164, v164 offset:0\n"
    "v_pk_fma_f16 v161, v160, v160, s40\n"
    "s_waitcnt lgkmcnt(5)\n"
    "v_mfma_f32_32x32x16_bf16 v[32:47], v[148:151], v[128:131], v[32:47]\n"
    "ds_read_b64_tr_b16 v[140:141], v1 offset:39936\n"
    "ds_read_b64_tr_b16 v[142:143], v1 offset:40192\n"
    "v_pk_mul_f16 v157, v157, v156 neg_lo:[1,0] neg_hi:[1,0]\n"
    "v_pk_mul_f16 v159, v159, v158 neg_lo:[1,0] neg_hi:[1,0]\n"
    "v_pk_mul_f16 v161, v161, v160 neg_lo:[1,0] neg_hi:[1,0]\n"
    "ds_read_u16_d16_hi v165, v165 offset:0\n"
    "v_exp_f16_sdwa v157, v157 dst_sel:WORD_0 dst_unused:UNUSED_PRESERVE src0_sel:WORD_0\n"
    "s_waitcnt lgkmcnt(6)\n"
    "v_mfma_f32_32x32x16_bf16 v[104:119], v[152:155], v[72:75], v[104:119]\n"
    "ds_read_b64_tr_b16 v[144:145], v2 offset:39936\n"
    "ds_read_b64_tr_b16 v[146:147], v2 offset:41472\n"
    "v_exp_f16_sdwa v159, v159 dst_sel:WORD_0 dst_unused:UNUSED_PRESERVE src0_sel:WORD_0\n"
    "ds_read_u16 v166, v166 offset:0\n"
    "v_exp_f16_sdwa v161, v161 dst_sel:WORD_0 dst_unused:UNUSED_PRESERVE src0_sel:WORD_0\n"
    "v_exp_f16_sdwa v157, v157 dst_sel:WORD_1 dst_unused:UNUSED_PRESERVE src0_sel:WORD_1\n"
    "s_waitcnt lgkmcnt(7)\n"
    "v_mfma_f32_32x32x16_bf16 v[48:63], v[136:139], v[128:131], v[48:63]\n"
    "s_barrier\n"
    "s_setprio 3\n"
    "ds_read_b64_tr_b16 v[148:149], v1 offset:40960\n"
    "ds_read_b64_tr_b16 v[150:151], v1 offset:41216\n"
    "ds_read_u16_d16_hi v167, v167 offset:0\n"
    "v_exp_f16_sdwa v159, v159 dst_sel:WORD_1 dst_unused:UNUSED_PRESERVE src0_sel:WORD_1\n"
    "v_exp_f16_sdwa v161, v161 dst_sel:WORD_1 dst_unused:UNUSED_PRESERVE src0_sel:WORD_1\n"
    "s_waitcnt lgkmcnt(7)\n"
    "v_mfma_f32_32x32x16_bf16 v[104:119], v[140:143], v[76:79], v[104:119]\n"
    "ds_read_b64_tr_b16 v[152:153], v2 offset:40000\n"
    "ds_read_b64_tr_b16 v[154:155], v2 offset:41536\n"
    "ds_read_u16 v168, v168 offset:0\n"
    "v_pk_fma_f16 v157, v157, s41, s41\n"
    "v_pk_fma_f16 v159, v159, s41, s41\n"
    "v_pk_fma_f16 v161, v161, s41, s41\n"
    "v_rcp_f16_sdwa v157, v157 dst_sel:WORD_0 dst_unused:UNUSED_PRESERVE src0_sel:WORD_0\n"
    "ds_read_u16_d16_hi v169, v169 offset:0\n"
    "s_waitcnt lgkmcnt(8)\n"
    "v_mfma_f32_32x32x16_bf16 v[16:31], v[144:147], v[132:135], v[16:31]\n"
    "ds_read_b64_tr_b16 v[136:137], v1 offset:41984\n"
    "ds_read_b64_tr_b16 v[138:139], v1 offset:42240\n"
    "v_rcp_f16_sdwa v159, v159 dst_sel:WORD_0 dst_unused:UNUSED_PRESERVE src0_sel:WORD_0\n"
    "v_rcp_f16_sdwa v161, v161 dst_sel:WORD_0 dst_unused:UNUSED_PRESERVE src0_sel:WORD_0\n"
    "ds_read_u16 v170, v170 offset:0\n"
    "s_waitcnt lgkmcnt(8)\n"
    "v_mfma_f32_32x32x16_bf16 v[104:119], v[148:151], v[80:83], v[104:119]\n"
    "ds_read_b64_tr_b16 v[140:141], v2 offset:40064\n"
    "ds_read_b64_tr_b16 v[142:143], v2 offset:41600\n"
    "v_rcp_f16_sdwa v157, v157 dst_sel:WORD_1 dst_unused:UNUSED_PRESERVE src0_sel:WORD_1\n"
    "v_rcp_f16_sdwa v159, v159 dst_sel:WORD_1 dst_unused:UNUSED_PRESERVE src0_sel:WORD_1\n"
    "ds_read_u16_d16_hi v171, v171 offset:0\n"
    "s_setprio 2\n"
    "v_rcp_f16_sdwa v161, v161 dst_sel:WORD_1 dst_unused:UNUSED_PRESERVE src0_sel:WORD_1\n"
    "s_waitcnt lgkmcnt(8)\n"
    "v_mfma_f32_32x32x16_bf16 v[32:47], v[152:155], v[132:135], v[32:47]\n"
    "v_fma_mix_f32 v88, v88, v157, 0 op_sel:[0,0,0] op_sel_hi:[0,1,0]\n"
    "ds_read_u16 v172, v172 offset:0\n"
    "v_fma_mix_f32 v90, v90, v159, 0 op_sel:[0,0,0] op_sel_hi:[0,1,0]\n"
    "v_fma_mix_f32 v92, v92, v161, 0 op_sel:[0,0,0] op_sel_hi:[0,1,0]\n"
    "v_fma_mix_f32 v89, v89, v157, 0 op_sel:[0,1,0] op_sel_hi:[0,1,0]\n"
    "s_waitcnt lgkmcnt(5)\n"
    "v_mfma_f32_32x32x16_bf16 v[104:119], v[136:139], v[84:87], v[104:119]\n"
    "v_fma_mix_f32 v91, v91, v159, 0 op_sel:[0,1,0] op_sel_hi:[0,1,0]\n"
    "ds_read_u16_d16_hi v173, v173 offset:0\n"
    "v_fma_mix_f32 v93, v93, v161, 0 op_sel:[0,1,0] op_sel_hi:[0,1,0]\n"
    "v_cvt_pk_bf16_f32 v120, v88, v89\n"
    "v_cvt_pk_bf16_f32 v121, v90, v91\n"
    "v_cvt_pk_bf16_f32 v122, v92, v93\n"
    "s_waitcnt lgkmcnt(3)\n"
    "v_mfma_f32_32x32x16_bf16 v[48:63], v[140:143], v[132:135], v[48:63]\n"
    "v_or_b32_e32 v123, v164, v165\n"
    "v_or_b32_e32 v124, v166, v167\n"
    "v_or_b32_e32 v125, v168, v169\n"
    "s_waitcnt lgkmcnt(2)\n"
    "v_or_b32_e32 v126, v170, v171\n"
    "s_waitcnt lgkmcnt(0)\n"
    "v_or_b32_e32 v127, v172, v173\n"
    "ds_read_b64_tr_b16 v[136:137], v1 offset:0\n"
    "ds_read_b64_tr_b16 v[138:139], v1 offset:256\n"
    "ds_read_b64_tr_b16 v[140:141], v2 offset:0\n"
    "ds_read_b64_tr_b16 v[142:143], v2 offset:1536\n"
    "ds_read_b64_tr_b16 v[144:145], v1 offset:1024\n"
    "ds_read_b64_tr_b16 v[146:147], v1 offset:1280\n"
    "s_setprio 1\n"
    "v_cvt_pk_f16_f32 v165, v110, v111\n"
    "v_cvt_pk_f16_f32 v167, v112, v113\n"
    "v_cvt_pk_f16_f32 v169, v114, v115\n"
    "v_cvt_pk_f16_f32 v171, v116, v117\n"
    "v_cvt_pk_f16_f32 v173, v118, v119\n"
    "s_mov_b32 exec_hi, 0xffff\n"
    "s_add_u32 m0, s59, 18816\n"
    "s_add_u32 s44, s44, 768\n"
    "buffer_load_dwordx4 v3, s[28:31], s44 offen nt lds\n"
    "s_add_u32 m0, s59, 19600\n"
    "s_add_u32 s44, s44, 768\n"
    "buffer_load_dwordx4 v3, s[28:31], s44 offen nt lds\n"
    "s_add_u32 m0, s59, 20384\n"
    "s_add_u32 s44, s44, 768\n"
    "buffer_load_dwordx4 v5, s[28:31], s44 offen nt lds\n"
    "s_mov_b32 exec_hi, -1\n"
    "v_and_b32_e32 v165, s42, v165\n"
    "v_and_b32_e32 v167, s42, v167\n"
    "v_and_b32_e32 v169, s42, v169\n"
    "v_and_b32_e32 v171, s42, v171\n"
    "v_and_b32_e32 v173, s42, v173\n"
    "v_lshrrev_b32_sdwa v164, 2, v165 dst_sel:DWORD dst_unused:UNUSED_PAD src0_sel:DWORD src1_sel:WORD_0\n"
    "v_lshrrev_b32_sdwa v166, 2, v167 dst_sel:DWORD dst_unused:UNUSED_PAD src0_sel:DWORD src1_sel:WORD_0\n"
    "s_waitcnt lgkmcnt(4)\n"
    "v_mfma_f32_32x32x16_bf16 v[88:103], v[136:139], v[64:67], 0\n"
    "ds_read_b64_tr_b16 v[148:149], v2 offset:64\n"
    "ds_read_b64_tr_b16 v[150:151], v2 offset:1600\n"
    "v_lshrrev_b32_sdwa v168, 2, v169 dst_sel:DWORD dst_unused:UNUSED_PAD src0_sel:DWORD src1_sel:WORD_0\n"
    "v_lshrrev_b32_sdwa v170, 2, v171 dst_sel:DWORD dst_unused:UNUSED_PAD src0_sel:DWORD src1_sel:WORD_0\n"
    "v_lshrrev_b32_sdwa v172, 2, v173 dst_sel:DWORD dst_unused:UNUSED_PAD src0_sel:DWORD src1_sel:WORD_0\n"
    "v_lshrrev_b32_e32 v165, 18, v165\n"
    "s_waitcnt lgkmcnt(4)\n"
    "v_mfma_f32_32x32x16_bf16 v[16:31], v[140:143], v[120:123], v[16:31]\n"
    "ds_read_b64_tr_b16 v[152:153], v1 offset:2048\n"
    "ds_read_b64_tr_b16 v[154:155], v1 offset:2304\n"
    "v_lshrrev_b32_e32 v167, 18, v167\n"
    "v_lshrrev_b32_e32 v169, 18, v169\n"
    "s_setprio 0\n"
    "v_lshrrev_b32_e32 v171, 18, v171\n"
    "v_lshrrev_b32_e32 v173, 18, v173\n"
    "v_cvt_pk_f16_f32 v156, v104, v105\n"
    "s_waitcnt lgkmcnt(4)\n"
    "v_mfma_f32_32x32x16_bf16 v[88:103], v[144:147], v[68:71], v[88:103]\n"
    "ds_read_b64_tr_b16 v[136:137], v2 offset:128\n"
    "ds_read_b64_tr_b16 v[138:139], v2 offset:1664\n"
    "v_cvt_pk_f16_f32 v158, v106, v107\n"
    "v_cvt_pk_f16_f32 v160, v108, v109\n"
    "v_pk_fma_f16 v157, v156, v156, s40\n"
    "v_pk_fma_f16 v159, v158, v158, s40\n"
    "ds_read_u16 v164, v164 offset:0\n"
    "v_pk_fma_f16 v161, v160, v160, s40\n"
    "s_waitcnt lgkmcnt(5)\n"
    "v_mfma_f32_32x32x16_bf16 v[32:47], v[148:151], v[120:123], v[32:47]\n"
    "ds_read_b64_tr_b16 v[140:141], v1 offset:3072\n"
    "ds_read_b64_tr_b16 v[142:143], v1 offset:3328\n"
    "v_pk_mul_f16 v157, v157, v156 neg_lo:[1,0] neg_hi:[1,0]\n"
    "v_pk_mul_f16 v159, v159, v158 neg_lo:[1,0] neg_hi:[1,0]\n"
    "v_pk_mul_f16 v161, v161, v160 neg_lo:[1,0] neg_hi:[1,0]\n"
    "ds_read_u16_d16_hi v165, v165 offset:0\n"
    "v_exp_f16_sdwa v157, v157 dst_sel:WORD_0 dst_unused:UNUSED_PRESERVE src0_sel:WORD_0\n"
    "s_waitcnt lgkmcnt(6)\n"
    "v_mfma_f32_32x32x16_bf16 v[88:103], v[152:155], v[72:75], v[88:103]\n"
    "ds_read_b64_tr_b16 v[144:145], v2 offset:3072\n"
    "ds_read_b64_tr_b16 v[146:147], v2 offset:4608\n"
    "v_exp_f16_sdwa v159, v159 dst_sel:WORD_0 dst_unused:UNUSED_PRESERVE src0_sel:WORD_0\n"
    "ds_read_u16 v166, v166 offset:0\n"
    "v_exp_f16_sdwa v161, v161 dst_sel:WORD_0 dst_unused:UNUSED_PRESERVE src0_sel:WORD_0\n"
    "v_exp_f16_sdwa v157, v157 dst_sel:WORD_1 dst_unused:UNUSED_PRESERVE src0_sel:WORD_1\n"
    "s_waitcnt lgkmcnt(7)\n"
    "v_mfma_f32_32x32x16_bf16 v[48:63], v[136:139], v[120:123], v[48:63]\n"
    "s_barrier\n"
    "s_setprio 3\n"
    "ds_read_b64_tr_b16 v[148:149], v1 offset:4096\n"
    "ds_read_b64_tr_b16 v[150:151], v1 offset:4352\n"
    "ds_read_u16_d16_hi v167, v167 offset:0\n"
    "v_exp_f16_sdwa v159, v159 dst_sel:WORD_1 dst_unused:UNUSED_PRESERVE src0_sel:WORD_1\n"
    "v_exp_f16_sdwa v161, v161 dst_sel:WORD_1 dst_unused:UNUSED_PRESERVE src0_sel:WORD_1\n"
    "s_waitcnt lgkmcnt(7)\n"
    "v_mfma_f32_32x32x16_bf16 v[88:103], v[140:143], v[76:79], v[88:103]\n"
    "ds_read_b64_tr_b16 v[152:153], v2 offset:3136\n"
    "ds_read_b64_tr_b16 v[154:155], v2 offset:4672\n"
    "ds_read_u16 v168, v168 offset:0\n"
    "v_pk_fma_f16 v157, v157, s41, s41\n"
    "v_pk_fma_f16 v159, v159, s41, s41\n"
    "v_pk_fma_f16 v161, v161, s41, s41\n"
    "v_rcp_f16_sdwa v157, v157 dst_sel:WORD_0 dst_unused:UNUSED_PRESERVE src0_sel:WORD_0\n"
    "ds_read_u16_d16_hi v169, v169 offset:0\n"
    "s_waitcnt lgkmcnt(8)\n"
    "v_mfma_f32_32x32x16_bf16 v[16:31], v[144:147], v[124:127], v[16:31]\n"
    "ds_read_b64_tr_b16 v[136:137], v1 offset:5120\n"
    "ds_read_b64_tr_b16 v[138:139], v1 offset:5376\n"
    "v_rcp_f16_sdwa v159, v159 dst_sel:WORD_0 dst_unused:UNUSED_PRESERVE src0_sel:WORD_0\n"
    "v_rcp_f16_sdwa v161, v161 dst_sel:WORD_0 dst_unused:UNUSED_PRESERVE src0_sel:WORD_0\n"
    "ds_read_u16 v170, v170 offset:0\n"
    "s_waitcnt lgkmcnt(8)\n"
    "v_mfma_f32_32x32x16_bf16 v[88:103], v[148:151], v[80:83], v[88:103]\n"
    "ds_read_b64_tr_b16 v[140:141], v2 offset:3200\n"
    "ds_read_b64_tr_b16 v[142:143], v2 offset:4736\n"
    "v_rcp_f16_sdwa v157, v157 dst_sel:WORD_1 dst_unused:UNUSED_PRESERVE src0_sel:WORD_1\n"
    "v_rcp_f16_sdwa v159, v159 dst_sel:WORD_1 dst_unused:UNUSED_PRESERVE src0_sel:WORD_1\n"
    "ds_read_u16_d16_hi v171, v171 offset:0\n"
    "s_setprio 2\n"
    "v_rcp_f16_sdwa v161, v161 dst_sel:WORD_1 dst_unused:UNUSED_PRESERVE src0_sel:WORD_1\n"
    "s_waitcnt lgkmcnt(8)\n"
    "v_mfma_f32_32x32x16_bf16 v[32:47], v[152:155], v[124:127], v[32:47]\n"
    "v_fma_mix_f32 v104, v104, v157, 0 op_sel:[0,0,0] op_sel_hi:[0,1,0]\n"
    "ds_read_u16 v172, v172 offset:0\n"
    "v_fma_mix_f32 v106, v106, v159, 0 op_sel:[0,0,0] op_sel_hi:[0,1,0]\n"
    "v_fma_mix_f32 v108, v108, v161, 0 op_sel:[0,0,0] op_sel_hi:[0,1,0]\n"
    "v_fma_mix_f32 v105, v105, v157, 0 op_sel:[0,1,0] op_sel_hi:[0,1,0]\n"
    "s_waitcnt lgkmcnt(5)\n"
    "v_mfma_f32_32x32x16_bf16 v[88:103], v[136:139], v[84:87], v[88:103]\n"
    "v_fma_mix_f32 v107, v107, v159, 0 op_sel:[0,1,0] op_sel_hi:[0,1,0]\n"
    "ds_read_u16_d16_hi v173, v173 offset:0\n"
    "v_fma_mix_f32 v109, v109, v161, 0 op_sel:[0,1,0] op_sel_hi:[0,1,0]\n"
    "v_cvt_pk_bf16_f32 v128, v104, v105\n"
    "v_cvt_pk_bf16_f32 v129, v106, v107\n"
    "v_cvt_pk_bf16_f32 v130, v108, v109\n"
    "s_waitcnt lgkmcnt(3)\n"
    "v_mfma_f32_32x32x16_bf16 v[48:63], v[140:143], v[124:127], v[48:63]\n"
    "v_or_b32_e32 v131, v164, v165\n"
    "v_or_b32_e32 v132, v166, v167\n"
    "v_or_b32_e32 v133, v168, v169\n"
    "s_waitcnt lgkmcnt(2)\n"
    "v_or_b32_e32 v134, v170, v171\n"
    "s_waitcnt lgkmcnt(0)\n"
    "v_or_b32_e32 v135, v172, v173\n"
    "ds_read_b64_tr_b16 v[136:137], v1 offset:12288\n"
    "ds_read_b64_tr_b16 v[138:139], v1 offset:12544\n"
    "ds_read_b64_tr_b16 v[140:141], v2 offset:12288\n"
    "ds_read_b64_tr_b16 v[142:143], v2 offset:13824\n"
    "ds_read_b64_tr_b16 v[144:145], v1 offset:13312\n"
    "ds_read_b64_tr_b16 v[146:147], v1 offset:13568\n"
    "s_setprio 1\n"
    "v_cvt_pk_f16_f32 v165, v94, v95\n"
    "v_cvt_pk_f16_f32 v167, v96, v97\n"
    "v_cvt_pk_f16_f32 v169, v98, v99\n"
    "v_cvt_pk_f16_f32 v171, v100, v101\n"
    "v_cvt_pk_f16_f32 v173, v102, v103\n"
    "v_and_b32_e32 v165, s42, v165\n"
    "v_and_b32_e32 v167, s42, v167\n"
    "v_and_b32_e32 v169, s42, v169\n"
    "v_and_b32_e32 v171, s42, v171\n"
    "v_and_b32_e32 v173, s42, v173\n"
    "v_lshrrev_b32_sdwa v164, 2, v165 dst_sel:DWORD dst_unused:UNUSED_PAD src0_sel:DWORD src1_sel:WORD_0\n"
    "v_lshrrev_b32_sdwa v166, 2, v167 dst_sel:DWORD dst_unused:UNUSED_PAD src0_sel:DWORD src1_sel:WORD_0\n"
    "s_waitcnt lgkmcnt(4)\n"
    "v_mfma_f32_32x32x16_bf16 v[104:119], v[136:139], v[64:67], 0\n"
    "ds_read_b64_tr_b16 v[148:149], v2 offset:12352\n"
    "ds_read_b64_tr_b16 v[150:151], v2 offset:13888\n"
    "v_lshrrev_b32_sdwa v168, 2, v169 dst_sel:DWORD dst_unused:UNUSED_PAD src0_sel:DWORD src1_sel:WORD_0\n"
    "v_lshrrev_b32_sdwa v170, 2, v171 dst_sel:DWORD dst_unused:UNUSED_PAD src0_sel:DWORD src1_sel:WORD_0\n"
    "v_lshrrev_b32_sdwa v172, 2, v173 dst_sel:DWORD dst_unused:UNUSED_PAD src0_sel:DWORD src1_sel:WORD_0\n"
    "v_lshrrev_b32_e32 v165, 18, v165\n"
    "s_waitcnt lgkmcnt(4)\n"
    "v_mfma_f32_32x32x16_bf16 v[16:31], v[140:143], v[128:131], v[16:31]\n"
    "ds_read_b64_tr_b16 v[152:153], v1 offset:14336\n"
    "ds_read_b64_tr_b16 v[154:155], v1 offset:14592\n"
    "v_lshrrev_b32_e32 v167, 18, v167\n"
    "v_lshrrev_b32_e32 v169, 18, v169\n"
    "s_setprio 0\n"
    "v_lshrrev_b32_e32 v171, 18, v171\n"
    "v_lshrrev_b32_e32 v173, 18, v173\n"
    "v_cvt_pk_f16_f32 v156, v88, v89\n"
    "s_waitcnt lgkmcnt(4)\n"
    "v_mfma_f32_32x32x16_bf16 v[104:119], v[144:147], v[68:71], v[104:119]\n"
    "ds_read_b64_tr_b16 v[136:137], v2 offset:12416\n"
    "ds_read_b64_tr_b16 v[138:139], v2 offset:13952\n"
    "v_cvt_pk_f16_f32 v158, v90, v91\n"
    "v_cvt_pk_f16_f32 v160, v92, v93\n"
    "v_pk_fma_f16 v157, v156, v156, s40\n"
    "v_pk_fma_f16 v159, v158, v158, s40\n"
    "ds_read_u16 v164, v164 offset:0\n"
    "v_pk_fma_f16 v161, v160, v160, s40\n"
    "s_waitcnt lgkmcnt(5)\n"
    "v_mfma_f32_32x32x16_bf16 v[32:47], v[148:151], v[128:131], v[32:47]\n"
    "ds_read_b64_tr_b16 v[140:141], v1 offset:15360\n"
    "ds_read_b64_tr_b16 v[142:143], v1 offset:15616\n"
    "v_pk_mul_f16 v157, v157, v156 neg_lo:[1,0] neg_hi:[1,0]\n"
    "v_pk_mul_f16 v159, v159, v158 neg_lo:[1,0] neg_hi:[1,0]\n"
    "v_pk_mul_f16 v161, v161, v160 neg_lo:[1,0] neg_hi:[1,0]\n"
    "ds_read_u16_d16_hi v165, v165 offset:0\n"
    "v_exp_f16_sdwa v157, v157 dst_sel:WORD_0 dst_unused:UNUSED_PRESERVE src0_sel:WORD_0\n"
    "s_waitcnt lgkmcnt(6)\n"
    "v_mfma_f32_32x32x16_bf16 v[104:119], v[152:155], v[72:75], v[104:119]\n"
    "ds_read_b64_tr_b16 v[144:145], v2 offset:15360\n"
    "ds_read_b64_tr_b16 v[146:147], v2 offset:16896\n"
    "v_exp_f16_sdwa v159, v159 dst_sel:WORD_0 dst_unused:UNUSED_PRESERVE src0_sel:WORD_0\n"
    "ds_read_u16 v166, v166 offset:0\n"
    "v_exp_f16_sdwa v161, v161 dst_sel:WORD_0 dst_unused:UNUSED_PRESERVE src0_sel:WORD_0\n"
    "v_exp_f16_sdwa v157, v157 dst_sel:WORD_1 dst_unused:UNUSED_PRESERVE src0_sel:WORD_1\n"
    "s_waitcnt lgkmcnt(7)\n"
    "v_mfma_f32_32x32x16_bf16 v[48:63], v[136:139], v[128:131], v[48:63]\n"
    "s_barrier\n"
    "s_setprio 3\n"
    "ds_read_b64_tr_b16 v[148:149], v1 offset:16384\n"
    "ds_read_b64_tr_b16 v[150:151], v1 offset:16640\n"
    "ds_read_u16_d16_hi v167, v167 offset:0\n"
    "v_exp_f16_sdwa v159, v159 dst_sel:WORD_1 dst_unused:UNUSED_PRESERVE src0_sel:WORD_1\n"
    "v_exp_f16_sdwa v161, v161 dst_sel:WORD_1 dst_unused:UNUSED_PRESERVE src0_sel:WORD_1\n"
    "s_waitcnt lgkmcnt(7)\n"
    "v_mfma_f32_32x32x16_bf16 v[104:119], v[140:143], v[76:79], v[104:119]\n"
    "ds_read_b64_tr_b16 v[152:153], v2 offset:15424\n"
    "ds_read_b64_tr_b16 v[154:155], v2 offset:16960\n"
    "ds_read_u16 v168, v168 offset:0\n"
    "v_pk_fma_f16 v157, v157, s41, s41\n"
    "v_pk_fma_f16 v159, v159, s41, s41\n"
    "v_pk_fma_f16 v161, v161, s41, s41\n"
    "v_rcp_f16_sdwa v157, v157 dst_sel:WORD_0 dst_unused:UNUSED_PRESERVE src0_sel:WORD_0\n"
    "ds_read_u16_d16_hi v169, v169 offset:0\n"
    "s_waitcnt lgkmcnt(8)\n"
    "v_mfma_f32_32x32x16_bf16 v[16:31], v[144:147], v[132:135], v[16:31]\n"
    "ds_read_b64_tr_b16 v[136:137], v1 offset:17408\n"
    "ds_read_b64_tr_b16 v[138:139], v1 offset:17664\n"
    "v_rcp_f16_sdwa v159, v159 dst_sel:WORD_0 dst_unused:UNUSED_PRESERVE src0_sel:WORD_0\n"
    "v_rcp_f16_sdwa v161, v161 dst_sel:WORD_0 dst_unused:UNUSED_PRESERVE src0_sel:WORD_0\n"
    "ds_read_u16 v170, v170 offset:0\n"
    "s_waitcnt lgkmcnt(8)\n"
    "v_mfma_f32_32x32x16_bf16 v[104:119], v[148:151], v[80:83], v[104:119]\n"
    "ds_read_b64_tr_b16 v[140:141], v2 offset:15488\n"
    "ds_read_b64_tr_b16 v[142:143], v2 offset:17024\n"
    "v_rcp_f16_sdwa v157, v157 dst_sel:WORD_1 dst_unused:UNUSED_PRESERVE src0_sel:WORD_1\n"
    "v_rcp_f16_sdwa v159, v159 dst_sel:WORD_1 dst_unused:UNUSED_PRESERVE src0_sel:WORD_1\n"
    "ds_read_u16_d16_hi v171, v171 offset:0\n"
    "s_setprio 2\n"
    "v_rcp_f16_sdwa v161, v161 dst_sel:WORD_1 dst_unused:UNUSED_PRESERVE src0_sel:WORD_1\n"
    "s_waitcnt lgkmcnt(8)\n"
    "v_mfma_f32_32x32x16_bf16 v[32:47], v[152:155], v[132:135], v[32:47]\n"
    "v_fma_mix_f32 v88, v88, v157, 0 op_sel:[0,0,0] op_sel_hi:[0,1,0]\n"
    "ds_read_u16 v172, v172 offset:0\n"
    "v_fma_mix_f32 v90, v90, v159, 0 op_sel:[0,0,0] op_sel_hi:[0,1,0]\n"
    "v_fma_mix_f32 v92, v92, v161, 0 op_sel:[0,0,0] op_sel_hi:[0,1,0]\n"
    "v_fma_mix_f32 v89, v89, v157, 0 op_sel:[0,1,0] op_sel_hi:[0,1,0]\n"
    "s_waitcnt lgkmcnt(5)\n"
    "v_mfma_f32_32x32x16_bf16 v[104:119], v[136:139], v[84:87], v[104:119]\n"
    "v_fma_mix_f32 v91, v91, v159, 0 op_sel:[0,1,0] op_sel_hi:[0,1,0]\n"
    "ds_read_u16_d16_hi v173, v173 offset:0\n"
    "v_fma_mix_f32 v93, v93, v161, 0 op_sel:[0,1,0] op_sel_hi:[0,1,0]\n"
    "v_cvt_pk_bf16_f32 v120, v88, v89\n"
    "v_cvt_pk_bf16_f32 v121, v90, v91\n"
    "v_cvt_pk_bf16_f32 v122, v92, v93\n"
    "s_waitcnt lgkmcnt(3)\n"
    "v_mfma_f32_32x32x16_bf16 v[48:63], v[140:143], v[132:135], v[48:63]\n"
    "v_or_b32_e32 v123, v164, v165\n"
    "v_or_b32_e32 v124, v166, v167\n"
    "v_or_b32_e32 v125, v168, v169\n"
    "s_waitcnt lgkmcnt(2)\n"
    "v_or_b32_e32 v126, v170, v171\n"
    "s_waitcnt lgkmcnt(0)\n"
    "v_or_b32_e32 v127, v172, v173\n"
    "ds_read_b64_tr_b16 v[136:137], v1 offset:24576\n"
    "ds_read_b64_tr_b16 v[138:139], v1 offset:24832\n"
    "ds_read_b64_tr_b16 v[140:141], v2 offset:24576\n"
    "ds_read_b64_tr_b16 v[142:143], v2 offset:26112\n"
    "ds_read_b64_tr_b16 v[144:145], v1 offset:25600\n"
    "ds_read_b64_tr_b16 v[146:147], v1 offset:25856\n"
    "s_setprio 1\n"
    "v_cvt_pk_f16_f32 v165, v110, v111\n"
    "v_cvt_pk_f16_f32 v167, v112, v113\n"
    "v_cvt_pk_f16_f32 v169, v114, v115\n"
    "v_cvt_pk_f16_f32 v171, v116, v117\n"
    "v_cvt_pk_f16_f32 v173, v118, v119\n"
    "v_and_b32_e32 v165, s42, v165\n"
    "v_and_b32_e32 v167, s42, v167\n"
    "v_and_b32_e32 v169, s42, v169\n"
    "v_and_b32_e32 v171, s42, v171\n"
    "v_and_b32_e32 v173, s42, v173\n"
    "v_lshrrev_b32_sdwa v164, 2, v165 dst_sel:DWORD dst_unused:UNUSED_PAD src0_sel:DWORD src1_sel:WORD_0\n"
    "v_lshrrev_b32_sdwa v166, 2, v167 dst_sel:DWORD dst_unused:UNUSED_PAD src0_sel:DWORD src1_sel:WORD_0\n"
    "s_waitcnt lgkmcnt(4)\n"
    "v_mfma_f32_32x32x16_bf16 v[88:103], v[136:139], v[64:67], 0\n"
    "ds_read_b64_tr_b16 v[148:149], v2 offset:24640\n"
    "ds_read_b64_tr_b16 v[150:151], v2 offset:26176\n"
    "v_lshrrev_b32_sdwa v168, 2, v169 dst_sel:DWORD dst_unused:UNUSED_PAD src0_sel:DWORD src1_sel:WORD_0\n"
    "v_lshrrev_b32_sdwa v170, 2, v171 dst_sel:DWORD dst_unused:UNUSED_PAD src0_sel:DWORD src1_sel:WORD_0\n"
    "v_lshrrev_b32_sdwa v172, 2, v173 dst_sel:DWORD dst_unused:UNUSED_PAD src0_sel:DWORD src1_sel:WORD_0\n"
    "v_lshrrev_b32_e32 v165, 18, v165\n"
    "s_waitcnt lgkmcnt(4)\n"
    "v_mfma_f32_32x32x16_bf16 v[16:31], v[140:143], v[120:123], v[16:31]\n"
    "ds_read_b64_tr_b16 v[152:153], v1 offset:26624\n"
    "ds_read_b64_tr_b16 v[154:155], v1 offset:26880\n"
    "v_lshrrev_b32_e32 v167, 18, v167\n"
    "v_lshrrev_b32_e32 v169, 18, v169\n"
    "s_setprio 0\n"
    "v_lshrrev_b32_e32 v171, 18, v171\n"
    "v_lshrrev_b32_e32 v173, 18, v173\n"
    "v_cvt_pk_f16_f32 v156, v104, v105\n"
    "s_waitcnt lgkmcnt(4)\n"
    "v_mfma_f32_32x32x16_bf16 v[88:103], v[144:147], v[68:71], v[88:103]\n"
    "ds_read_b64_tr_b16 v[136:137], v2 offset:24704\n"
    "ds_read_b64_tr_b16 v[138:139], v2 offset:26240\n"
    "v_cvt_pk_f16_f32 v158, v106, v107\n"
    "v_cvt_pk_f16_f32 v160, v108, v109\n"
    "v_pk_fma_f16 v157, v156, v156, s40\n"
    "v_pk_fma_f16 v159, v158, v158, s40\n"
    "ds_read_u16 v164, v164 offset:0\n"
    "v_pk_fma_f16 v161, v160, v160, s40\n"
    "s_waitcnt lgkmcnt(5)\n"
    "v_mfma_f32_32x32x16_bf16 v[32:47], v[148:151], v[120:123], v[32:47]\n"
    "ds_read_b64_tr_b16 v[140:141], v1 offset:27648\n"
    "ds_read_b64_tr_b16 v[142:143], v1 offset:27904\n"
    "v_pk_mul_f16 v157, v157, v156 neg_lo:[1,0] neg_hi:[1,0]\n"
    "v_pk_mul_f16 v159, v159, v158 neg_lo:[1,0] neg_hi:[1,0]\n"
    "v_pk_mul_f16 v161, v161, v160 neg_lo:[1,0] neg_hi:[1,0]\n"
    "ds_read_u16_d16_hi v165, v165 offset:0\n"
    "v_exp_f16_sdwa v157, v157 dst_sel:WORD_0 dst_unused:UNUSED_PRESERVE src0_sel:WORD_0\n"
    "s_waitcnt lgkmcnt(6)\n"
    "v_mfma_f32_32x32x16_bf16 v[88:103], v[152:155], v[72:75], v[88:103]\n"
    "ds_read_b64_tr_b16 v[144:145], v2 offset:27648\n"
    "ds_read_b64_tr_b16 v[146:147], v2 offset:29184\n"
    "v_exp_f16_sdwa v159, v159 dst_sel:WORD_0 dst_unused:UNUSED_PRESERVE src0_sel:WORD_0\n"
    "ds_read_u16 v166, v166 offset:0\n"
    "v_exp_f16_sdwa v161, v161 dst_sel:WORD_0 dst_unused:UNUSED_PRESERVE src0_sel:WORD_0\n"
    "v_exp_f16_sdwa v157, v157 dst_sel:WORD_1 dst_unused:UNUSED_PRESERVE src0_sel:WORD_1\n"
    "s_waitcnt lgkmcnt(7)\n"
    "v_mfma_f32_32x32x16_bf16 v[48:63], v[136:139], v[120:123], v[48:63]\n"
    "s_barrier\n"
    "s_setprio 3\n"
    "ds_read_b64_tr_b16 v[148:149], v1 offset:28672\n"
    "ds_read_b64_tr_b16 v[150:151], v1 offset:28928\n"
    "ds_read_u16_d16_hi v167, v167 offset:0\n"
    "v_exp_f16_sdwa v159, v159 dst_sel:WORD_1 dst_unused:UNUSED_PRESERVE src0_sel:WORD_1\n"
    "v_exp_f16_sdwa v161, v161 dst_sel:WORD_1 dst_unused:UNUSED_PRESERVE src0_sel:WORD_1\n"
    "s_waitcnt lgkmcnt(7)\n"
    "v_mfma_f32_32x32x16_bf16 v[88:103], v[140:143], v[76:79], v[88:103]\n"
    "ds_read_b64_tr_b16 v[152:153], v2 offset:27712\n"
    "ds_read_b64_tr_b16 v[154:155], v2 offset:29248\n"
    "ds_read_u16 v168, v168 offset:0\n"
    "v_pk_fma_f16 v157, v157, s41, s41\n"
    "v_pk_fma_f16 v159, v159, s41, s41\n"
    "v_pk_fma_f16 v161, v161, s41, s41\n"
    "v_rcp_f16_sdwa v157, v157 dst_sel:WORD_0 dst_unused:UNUSED_PRESERVE src0_sel:WORD_0\n"
    "ds_read_u16_d16_hi v169, v169 offset:0\n"
    "s_waitcnt lgkmcnt(8)\n"
    "v_mfma_f32_32x32x16_bf16 v[16:31], v[144:147], v[124:127], v[16:31]\n"
    "ds_read_b64_tr_b16 v[136:137], v1 offset:29696\n"
    "ds_read_b64_tr_b16 v[138:139], v1 offset:29952\n"
    "v_rcp_f16_sdwa v159, v159 dst_sel:WORD_0 dst_unused:UNUSED_PRESERVE src0_sel:WORD_0\n"
    "v_rcp_f16_sdwa v161, v161 dst_sel:WORD_0 dst_unused:UNUSED_PRESERVE src0_sel:WORD_0\n"
    "ds_read_u16 v170, v170 offset:0\n"
    "s_waitcnt lgkmcnt(8)\n"
    "v_mfma_f32_32x32x16_bf16 v[88:103], v[148:151], v[80:83], v[88:103]\n"
    "ds_read_b64_tr_b16 v[140:141], v2 offset:27776\n"
    "ds_read_b64_tr_b16 v[142:143], v2 offset:29312\n"
    "v_rcp_f16_sdwa v157, v157 dst_sel:WORD_1 dst_unused:UNUSED_PRESERVE src0_sel:WORD_1\n"
    "v_rcp_f16_sdwa v159, v159 dst_sel:WORD_1 dst_unused:UNUSED_PRESERVE src0_sel:WORD_1\n"
    "ds_read_u16_d16_hi v171, v171 offset:0\n"
    "s_setprio 2\n"
    "v_rcp_f16_sdwa v161, v161 dst_sel:WORD_1 dst_unused:UNUSED_PRESERVE src0_sel:WORD_1\n"
    "s_waitcnt lgkmcnt(8)\n"
    "v_mfma_f32_32x32x16_bf16 v[32:47], v[152:155], v[124:127], v[32:47]\n"
    "v_fma_mix_f32 v104, v104, v157, 0 op_sel:[0,0,0] op_sel_hi:[0,1,0]\n"
    "ds_read_u16 v172, v172 offset:0\n"
    "v_fma_mix_f32 v106, v106, v159, 0 op_sel:[0,0,0] op_sel_hi:[0,1,0]\n"
    "v_fma_mix_f32 v108, v108, v161, 0 op_sel:[0,0,0] op_sel_hi:[0,1,0]\n"
    "v_fma_mix_f32 v105, v105, v157, 0 op_sel:[0,1,0] op_sel_hi:[0,1,0]\n"
    "s_waitcnt lgkmcnt(5)\n"
    "v_mfma_f32_32x32x16_bf16 v[88:103], v[136:139], v[84:87], v[88:103]\n"
    "v_fma_mix_f32 v107, v107, v159, 0 op_sel:[0,1,0] op_sel_hi:[0,1,0]\n"
    "ds_read_u16_d16_hi v173, v173 offset:0\n"
    "v_fma_mix_f32 v109, v109, v161, 0 op_sel:[0,1,0] op_sel_hi:[0,1,0]\n"
    "v_cvt_pk_bf16_f32 v128, v104, v105\n"
    "v_cvt_pk_bf16_f32 v129, v106, v107\n"
    "v_cvt_pk_bf16_f32 v130, v108, v109\n"
    "s_waitcnt lgkmcnt(3)\n"
    "v_mfma_f32_32x32x16_bf16 v[48:63], v[140:143], v[124:127], v[48:63]\n"
    "v_or_b32_e32 v131, v164, v165\n"
    "v_or_b32_e32 v132, v166, v167\n"
    "v_or_b32_e32 v133, v168, v169\n"
    "s_waitcnt lgkmcnt(2)\n"
    "v_or_b32_e32 v134, v170, v171\n"
    "s_waitcnt lgkmcnt(0)\n"
    "v_or_b32_e32 v135, v172, v173\n"
    "ds_read_b64_tr_b16 v[136:137], v1 offset:36864\n"
    "ds_read_b64_tr_b16 v[138:139], v1 offset:37120\n"
    "ds_read_b64_tr_b16 v[140:141], v2 offset:36864\n"
    "ds_read_b64_tr_b16 v[142:143], v2 offset:38400\n"
    "ds_read_b64_tr_b16 v[144:145], v1 offset:37888\n"
    "ds_read_b64_tr_b16 v[146:147], v1 offset:38144\n"
    "s_setprio 1\n"
    "v_cvt_pk_f16_f32 v165, v94, v95\n"
    "v_cvt_pk_f16_f32 v167, v96, v97\n"
    "v_cvt_pk_f16_f32 v169, v98, v99\n"
    "v_cvt_pk_f16_f32 v171, v100, v101\n"
    "v_cvt_pk_f16_f32 v173, v102, v103\n"
    "v_and_b32_e32 v165, s42, v165\n"
    "v_and_b32_e32 v167, s42, v167\n"
    "v_and_b32_e32 v169, s42, v169\n"
    "v_and_b32_e32 v171, s42, v171\n"
    "v_and_b32_e32 v173, s42, v173\n"
    "v_lshrrev_b32_sdwa v164, 2, v165 dst_sel:DWORD dst_unused:UNUSED_PAD src0_sel:DWORD src1_sel:WORD_0\n"
    "v_lshrrev_b32_sdwa v166, 2, v167 dst_sel:DWORD dst_unused:UNUSED_PAD src0_sel:DWORD src1_sel:WORD_0\n"
    "s_waitcnt lgkmcnt(4)\n"
    "v_mfma_f32_32x32x16_bf16 v[104:119], v[136:139], v[64:67], 0\n"
    "ds_read_b64_tr_b16 v[148:149], v2 offset:36928\n"
    "ds_read_b64_tr_b16 v[150:151], v2 offset:38464\n"
    "v_lshrrev_b32_sdwa v168, 2, v169 dst_sel:DWORD dst_unused:UNUSED_PAD src0_sel:DWORD src1_sel:WORD_0\n"
    "v_lshrrev_b32_sdwa v170, 2, v171 dst_sel:DWORD dst_unused:UNUSED_PAD src0_sel:DWORD src1_sel:WORD_0\n"
    "v_lshrrev_b32_sdwa v172, 2, v173 dst_sel:DWORD dst_unused:UNUSED_PAD src0_sel:DWORD src1_sel:WORD_0\n"
    "v_lshrrev_b32_e32 v165, 18, v165\n"
    "s_waitcnt lgkmcnt(4)\n"
    "v_mfma_f32_32x32x16_bf16 v[16:31], v[140:143], v[128:131], v[16:31]\n"
    "ds_read_b64_tr_b16 v[152:153], v1 offset:38912\n"
    "ds_read_b64_tr_b16 v[154:155], v1 offset:39168\n"
    "v_lshrrev_b32_e32 v167, 18, v167\n"
    "v_lshrrev_b32_e32 v169, 18, v169\n"
    "s_setprio 0\n"
    "v_lshrrev_b32_e32 v171, 18, v171\n"
    "v_lshrrev_b32_e32 v173, 18, v173\n"
    "v_cvt_pk_f16_f32 v156, v88, v89\n"
    "s_waitcnt lgkmcnt(4)\n"
    "v_mfma_f32_32x32x16_bf16 v[104:119], v[144:147], v[68:71], v[104:119]\n"
    "ds_read_b64_tr_b16 v[136:137], v2 offset:36992\n"
    "ds_read_b64_tr_b16 v[138:139], v2 offset:38528\n"
    "v_cvt_pk_f16_f32 v158, v90, v91\n"
    "v_cvt_pk_f16_f32 v160, v92, v93\n"
    "v_pk_fma_f16 v157, v156, v156, s40\n"
    "v_pk_fma_f16 v159, v158, v158, s40\n"
    "ds_read_u16 v164, v164 offset:0\n"
    "v_pk_fma_f16 v161, v160, v160, s40\n"
    "s_waitcnt lgkmcnt(5)\n"
    "v_mfma_f32_32x32x16_bf16 v[32:47], v[148:151], v[128:131], v[32:47]\n"
    "ds_read_b64_tr_b16 v[140:141], v1 offset:39936\n"
    "ds_read_b64_tr_b16 v[142:143], v1 offset:40192\n"
    "v_pk_mul_f16 v157, v157, v156 neg_lo:[1,0] neg_hi:[1,0]\n"
    "v_pk_mul_f16 v159, v159, v158 neg_lo:[1,0] neg_hi:[1,0]\n"
    "v_pk_mul_f16 v161, v161, v160 neg_lo:[1,0] neg_hi:[1,0]\n"
    "ds_read_u16_d16_hi v165, v165 offset:0\n"
    "v_exp_f16_sdwa v157, v157 dst_sel:WORD_0 dst_unused:UNUSED_PRESERVE src0_sel:WORD_0\n"
    "s_waitcnt lgkmcnt(6)\n"
    "v_mfma_f32_32x32x16_bf16 v[104:119], v[152:155], v[72:75], v[104:119]\n"
    "ds_read_b64_tr_b16 v[144:145], v2 offset:39936\n"
    "ds_read_b64_tr_b16 v[146:147], v2 offset:41472\n"
    "v_exp_f16_sdwa v159, v159 dst_sel:WORD_0 dst_unused:UNUSED_PRESERVE src0_sel:WORD_0\n"
    "ds_read_u16 v166, v166 offset:0\n"
    "v_exp_f16_sdwa v161, v161 dst_sel:WORD_0 dst_unused:UNUSED_PRESERVE src0_sel:WORD_0\n"
    "v_exp_f16_sdwa v157, v157 dst_sel:WORD_1 dst_unused:UNUSED_PRESERVE src0_sel:WORD_1\n"
    "s_waitcnt lgkmcnt(7)\n"
    "v_mfma_f32_32x32x16_bf16 v[48:63], v[136:139], v[128:131], v[48:63]\n"
    "s_waitcnt vmcnt(0)\n"
    "s_barrier\n"
    "s_setprio 3\n"
    "ds_read_b64_tr_b16 v[148:149], v1 offset:40960\n"
    "ds_read_b64_tr_b16 v[150:151], v1 offset:41216\n"
    "ds_read_u16_d16_hi v167, v167 offset:0\n"
    "v_exp_f16_sdwa v159, v159 dst_sel:WORD_1 dst_unused:UNUSED_PRESERVE src0_sel:WORD_1\n"
    "v_exp_f16_sdwa v161, v161 dst_sel:WORD_1 dst_unused:UNUSED_PRESERVE src0_sel:WORD_1\n"
    "s_waitcnt lgkmcnt(7)\n"
    "v_mfma_f32_32x32x16_bf16 v[104:119], v[140:143], v[76:79], v[104:119]\n"
    "ds_read_b64_tr_b16 v[152:153], v2 offset:40000\n"
    "ds_read_b64_tr_b16 v[154:155], v2 offset:41536\n"
    "ds_read_u16 v168, v168 offset:0\n"
    "v_pk_fma_f16 v157, v157, s41, s41\n"
    "v_pk_fma_f16 v159, v159, s41, s41\n"
    "v_pk_fma_f16 v161, v161, s41, s41\n"
    "v_rcp_f16_sdwa v157, v157 dst_sel:WORD_0 dst_unused:UNUSED_PRESERVE src0_sel:WORD_0\n"
    "ds_read_u16_d16_hi v169, v169 offset:0\n"
    "s_waitcnt lgkmcnt(8)\n"
    "v_mfma_f32_32x32x16_bf16 v[16:31], v[144:147], v[132:135], v[16:31]\n"
    "ds_read_b64_tr_b16 v[136:137], v1 offset:41984\n"
    "ds_read_b64_tr_b16 v[138:139], v1 offset:42240\n"
    "v_rcp_f16_sdwa v159, v159 dst_sel:WORD_0 dst_unused:UNUSED_PRESERVE src0_sel:WORD_0\n"
    "v_rcp_f16_sdwa v161, v161 dst_sel:WORD_0 dst_unused:UNUSED_PRESERVE src0_sel:WORD_0\n"
    "ds_read_u16 v170, v170 offset:0\n"
    "s_waitcnt lgkmcnt(8)\n"
    "v_mfma_f32_32x32x16_bf16 v[104:119], v[148:151], v[80:83], v[104:119]\n"
    "ds_read_b64_tr_b16 v[140:141], v2 offset:40064\n"
    "ds_read_b64_tr_b16 v[142:143], v2 offset:41600\n"
    "v_rcp_f16_sdwa v157, v157 dst_sel:WORD_1 dst_unused:UNUSED_PRESERVE src0_sel:WORD_1\n"
    "v_rcp_f16_sdwa v159, v159 dst_sel:WORD_1 dst_unused:UNUSED_PRESERVE src0_sel:WORD_1\n"
    "ds_read_u16_d16_hi v171, v171 offset:0\n"
    "s_setprio 2\n"
    "v_rcp_f16_sdwa v161, v161 dst_sel:WORD_1 dst_unused:UNUSED_PRESERVE src0_sel:WORD_1\n"
    "s_waitcnt lgkmcnt(8)\n"
    "v_mfma_f32_32x32x16_bf16 v[32:47], v[152:155], v[132:135], v[32:47]\n"
    "v_fma_mix_f32 v88, v88, v157, 0 op_sel:[0,0,0] op_sel_hi:[0,1,0]\n"
    "ds_read_u16 v172, v172 offset:0\n"
    "v_fma_mix_f32 v90, v90, v159, 0 op_sel:[0,0,0] op_sel_hi:[0,1,0]\n"
    "v_fma_mix_f32 v92, v92, v161, 0 op_sel:[0,0,0] op_sel_hi:[0,1,0]\n"
    "v_fma_mix_f32 v89, v89, v157, 0 op_sel:[0,1,0] op_sel_hi:[0,1,0]\n"
    "s_waitcnt lgkmcnt(5)\n"
    "v_mfma_f32_32x32x16_bf16 v[104:119], v[136:139], v[84:87], v[104:119]\n"
    "v_fma_mix_f32 v91, v91, v159, 0 op_sel:[0,1,0] op_sel_hi:[0,1,0]\n"
    "ds_read_u16_d16_hi v173, v173 offset:0\n"
    "v_fma_mix_f32 v93, v93, v161, 0 op_sel:[0,1,0] op_sel_hi:[0,1,0]\n"
    "v_cvt_pk_bf16_f32 v120, v88, v89\n"
    "v_cvt_pk_bf16_f32 v121, v90, v91\n"
    "v_cvt_pk_bf16_f32 v122, v92, v93\n"
    "s_waitcnt lgkmcnt(3)\n"
    "v_mfma_f32_32x32x16_bf16 v[48:63], v[140:143], v[132:135], v[48:63]\n"
    "v_or_b32_e32 v123, v164, v165\n"
    "v_or_b32_e32 v124, v166, v167\n"
    "v_or_b32_e32 v125, v168, v169\n"
    "s_waitcnt lgkmcnt(2)\n"
    "v_or_b32_e32 v126, v170, v171\n"
    "s_waitcnt lgkmcnt(0)\n"
    "v_or_b32_e32 v127, v172, v173\n"
    "ds_read_b128 v[64:67], v11 offset:0\n"
    "ds_read_b128 v[68:71], v11 offset:32\n"
    "ds_read_b128 v[72:75], v11 offset:64\n"
    "ds_read_b128 v[76:79], v11 offset:96\n"
    "ds_read_b128 v[80:83], v11 offset:128\n"
    "ds_read_b128 v[84:87], v11 offset:160\n"
    "ds_read_b64_tr_b16 v[136:137], v2 offset:0\n"
    "ds_read_b64_tr_b16 v[138:139], v2 offset:1536\n"
    "ds_read_b64_tr_b16 v[140:141], v2 offset:64\n"
    "ds_read_b64_tr_b16 v[142:143], v2 offset:1600\n"
    "ds_read_b64_tr_b16 v[144:145], v2 offset:128\n"
    "ds_read_b64_tr_b16 v[146:147], v2 offset:1664\n"
    "s_setprio 1\n"
    "v_cvt_pk_f16_f32 v165, v110, v111\n"
    "v_cvt_pk_f16_f32 v167, v112, v113\n"
    "v_cvt_pk_f16_f32 v169, v114, v115\n"
    "v_cvt_pk_f16_f32 v171, v116, v117\n"
    "v_cvt_pk_f16_f32 v173, v118, v119\n"
    "v_and_b32_e32 v165, s42, v165\n"
    "v_and_b32_e32 v167, s42, v167\n"
    "v_and_b32_e32 v169, s42, v169\n"
    "v_and_b32_e32 v171, s42, v171\n"
    "v_and_b32_e32 v173, s42, v173\n"
    "v_lshrrev_b32_sdwa v164, 2, v165 dst_sel:DWORD dst_unused:UNUSED_PAD src0_sel:DWORD src1_sel:WORD_0\n"
    "v_lshrrev_b32_sdwa v166, 2, v167 dst_sel:DWORD dst_unused:UNUSED_PAD src0_sel:DWORD src1_sel:WORD_0\n"
    "s_waitcnt lgkmcnt(4)\n"
    "v_mfma_f32_32x32x16_bf16 v[16:31], v[136:139], v[120:123], v[16:31]\n"
    "ds_read_b64_tr_b16 v[148:149], v2 offset:3072\n"
    "ds_read_b64_tr_b16 v[150:151], v2 offset:4608\n"
    "v_lshrrev_b32_sdwa v168, 2, v169 dst_sel:DWORD dst_unused:UNUSED_PAD src0_sel:DWORD src1_sel:WORD_0\n"
    "v_lshrrev_b32_sdwa v170, 2, v171 dst_sel:DWORD dst_unused:UNUSED_PAD src0_sel:DWORD src1_sel:WORD_0\n"
    "v_lshrrev_b32_sdwa v172, 2, v173 dst_sel:DWORD dst_unused:UNUSED_PAD src0_sel:DWORD src1_sel:WORD_0\n"
    "v_lshrrev_b32_e32 v165, 18, v165\n"
    "v_lshrrev_b32_e32 v167, 18, v167\n"
    "v_lshrrev_b32_e32 v169, 18, v169\n"
    "s_setprio 0\n"
    "v_lshrrev_b32_e32 v171, 18, v171\n"
    "v_lshrrev_b32_e32 v173, 18, v173\n"
    "v_cvt_pk_f16_f32 v156, v104, v105\n"
    "v_cvt_pk_f16_f32 v158, v106, v107\n"
    "s_waitcnt lgkmcnt(4)\n"
    "v_mfma_f32_32x32x16_bf16 v[32:47], v[140:143], v[120:123], v[32:47]\n"
    "ds_read_b64_tr_b16 v[152:153], v2 offset:3136\n"
    "ds_read_b64_tr_b16 v[154:155], v2 offset:4672\n"
    "v_cvt_pk_f16_f32 v160, v108, v109\n"
    "v_pk_fma_f16 v157, v156, v156, s40\n"
    "v_pk_fma_f16 v159, v158, v158, s40\n"
    "ds_read_u16 v164, v164 offset:0\n"
    "v_pk_fma_f16 v161, v160, v160, s40\n"
    "v_pk_mul_f16 v157, v157, v156 neg_lo:[1,0] neg_hi:[1,0]\n"
    "v_pk_mul_f16 v159, v159, v158 neg_lo:[1,0] neg_hi:[1,0]\n"
    "v_pk_mul_f16 v161, v161, v160 neg_lo:[1,0] neg_hi:[1,0]\n"
    "ds_read_u16_d16_hi v165, v165 offset:0\n"
    "v_exp_f16_sdwa v157, v157 dst_sel:WORD_0 dst_unused:UNUSED_PRESERVE src0_sel:WORD_0\n"
    "v_exp_f16_sdwa v159, v159 dst_sel:WORD_0 dst_unused:UNUSED_PRESERVE src0_sel:WORD_0\n"
    "s_waitcnt lgkmcnt(6)\n"
    "v_mfma_f32_32x32x16_bf16 v[48:63], v[144:147], v[120:123], v[48:63]\n"
    "ds_read_u16 v166, v166 offset:0\n"
    "ds_read_b64_tr_b16 v[136:137], v2 offset:3200\n"
    "ds_read_b64_tr_b16 v[138:139], v2 offset:4736\n"
    "v_exp_f16_sdwa v161, v161 dst_sel:WORD_0 dst_unused:UNUSED_PRESERVE src0_sel:WORD_0\n"
    "v_exp_f16_sdwa v157, v157 dst_sel:WORD_1 dst_unused:UNUSED_PRESERVE src0_sel:WORD_1\n"
    "s_nop 0\n"
    "s_barrier\n"
    "s_setprio 3\n"
    "ds_read_u16_d16_hi v167, v167 offset:0\n"
    "v_exp_f16_sdwa v159, v159 dst_sel:WORD_1 dst_unused:UNUSED_PRESERVE src0_sel:WORD_1\n"
    "v_exp_f16_sdwa v161, v161 dst_sel:WORD_1 dst_unused:UNUSED_PRESERVE src0_sel:WORD_1\n"
    "ds_read_u16 v168, v168 offset:0\n"
    "v_pk_fma_f16 v157, v157, s41, s41\n"
    "v_pk_fma_f16 v159, v159, s41, s41\n"
    "s_waitcnt lgkmcnt(9)\n"
    "v_mfma_f32_32x32x16_bf16 v[16:31], v[148:151], v[124:127], v[16:31]\n"
    "v_pk_fma_f16 v161, v161, s41, s41\n"
    "v_rcp_f16_sdwa v157, v157 dst_sel:WORD_0 dst_unused:UNUSED_PRESERVE src0_sel:WORD_0\n"
    "ds_read_u16_d16_hi v169, v169 offset:0\n"
    "v_rcp_f16_sdwa v159, v159 dst_sel:WORD_0 dst_unused:UNUSED_PRESERVE src0_sel:WORD_0\n"
    "v_rcp_f16_sdwa v161, v161 dst_sel:WORD_0 dst_unused:UNUSED_PRESERVE src0_sel:WORD_0\n"
    "ds_read_u16 v170, v170 offset:0\n"
    "v_rcp_f16_sdwa v157, v157 dst_sel:WORD_1 dst_unused:UNUSED_PRESERVE src0_sel:WORD_1\n"
    "v_rcp_f16_sdwa v159, v159 dst_sel:WORD_1 dst_unused:UNUSED_PRESERVE src0_sel:WORD_1\n"
    "ds_read_u16_d16_hi v171, v171 offset:0\n"
    "s_setprio 2\n"
    "s_waitcnt lgkmcnt(10)\n"
    "v_mfma_f32_32x32x16_bf16 v[32:47], v[152:155], v[124:127], v[32:47]\n"
    "v_rcp_f16_sdwa v161, v161 dst_sel:WORD_1 dst_unused:UNUSED_PRESERVE src0_sel:WORD_1\n"
    "v_fma_mix_f32 v104, v104, v157, 0 op_sel:[0,0,0] op_sel_hi:[0,1,0]\n"
    "ds_read_u16 v172, v172 offset:0\n"
    "v_fma_mix_f32 v106, v106, v159, 0 op_sel:[0,0,0] op_sel_hi:[0,1,0]\n"
    "v_fma_mix_f32 v108, v108, v161, 0 op_sel:[0,0,0] op_sel_hi:[0,1,0]\n"
    "v_fma_mix_f32 v105, v105, v157, 0 op_sel:[0,1,0] op_sel_hi:[0,1,0]\n"
    "v_fma_mix_f32 v107, v107, v159, 0 op_sel:[0,1,0] op_sel_hi:[0,1,0]\n"
    "ds_read_u16_d16_hi v173, v173 offset:0\n"
    "v_fma_mix_f32 v109, v109, v161, 0 op_sel:[0,1,0] op_sel_hi:[0,1,0]\n"
    "v_cvt_pk_bf16_f32 v128, v104, v105\n"
    "v_cvt_pk_bf16_f32 v129, v106, v107\n"
    "v_cvt_pk_bf16_f32 v130, v108, v109\n"
    "s_waitcnt lgkmcnt(7)\n"
    "v_mfma_f32_32x32x16_bf16 v[48:63], v[136:139], v[124:127], v[48:63]\n"
    "v_or_b32_e32 v131, v164, v165\n"
    "s_waitcnt lgkmcnt(6)\n"
    "v_or_b32_e32 v132, v166, v167\n"
    "s_waitcnt lgkmcnt(4)\n"
    "v_or_b32_e32 v133, v168, v169\n"
    "s_waitcnt lgkmcnt(2)\n"
    "v_or_b32_e32 v134, v170, v171\n"
    "s_waitcnt lgkmcnt(0)\n"
    "v_or_b32_e32 v135, v172, v173\n"
    "s_add_u32 s57, s57, 1\n"
    "s_cmp_ge_u32 s57, 4\n"
    "s_cbranch_scc1 L_tailX_%=\n"
    "s_setprio 3\n"
    "ds_read_b64_tr_b16 v[136:137], v2 offset:12288\n"
    "ds_read_b64_tr_b16 v[138:139], v2 offset:13824\n"
    "ds_read_b64_tr_b16 v[140:141], v2 offset:12352\n"
    "ds_read_b64_tr_b16 v[142:143], v2 offset:13888\n"
    "ds_read_b64_tr_b16 v[144:145], v2 offset:12416\n"
    "ds_read_b64_tr_b16 v[146:147], v2 offset:13952\n"
    "ds_read_b64_tr_b16 v[148:149], v2 offset:15360\n"
    "ds_read_b64_tr_b16 v[150:151], v2 offset:16896\n"
    "ds_read_b64_tr_b16 v[152:153], v2 offset:15424\n"
    "ds_read_b64_tr_b16 v[154:155], v2 offset:16960\n"
    "ds_read_b64 v[104:105], v7 offset:0\n"
    "ds_read_b64 v[106:107], v7 offset:192\n"
    "ds_read_b64 v[108:109], v7 offset:7056\n"
    "ds_read_b64 v[110:111], v7 offset:7248\n"
    "s_waitcnt lgkmcnt(12)\n"
    "v_mfma_f32_32x32x16_bf16 v[16:31], v[136:139], v[128:131], v[16:31]\n"
    "ds_read_b64_tr_b16 v[136:137], v2 offset:15488\n"
    "ds_read_b64_tr_b16 v[138:139], v2 offset:17024\n"
    "ds_read_b64 v[112:113], v7 offset:14112\n"
    "s_waitcnt lgkmcnt(14)\n"
    "ds_read_b64 v[114:115], v7 offset:14304\n"
    "s_waitcnt lgkmcnt(14)\n"
    "ds_read_b64 v[116:117], v7 offset:21168\n"
    "s_waitcnt lgkmcnt(14)\n"
    "ds_read_b64 v[118:119], v7 offset:21360\n"
    "s_waitcnt lgkmcnt(14)\n"
    "ds_read_b64 v[120:121], v7 offset:28224\n"
    "s_waitcnt lgkmcnt(14)\n"
    "ds_read_b64 v[122:123], v7 offset:28416\n"
    "s_waitcnt lgkmcnt(14)\n"
    "ds_read_b64 v[124:125], v7 offset:35280\n"
    "s_waitcnt lgkmcnt(14)\n"
    "ds_read_b64 v[126:127], v7 offset:35472\n"
    "v_mfma_f32_32x32x16_bf16 v[32:47], v[140:143], v[128:131], v[32:47]\n"
    "s_waitcnt lgkmcnt(12)\n"
    "v_mfma_f32_16x16x32_bf16 v[88:91], v[180:183], v[104:107], 0\n"
    "ds_read_b64 v[248:249], v7 offset:42336\n"
    "ds_read_b64 v[250:251], v7 offset:42528\n"
    "v_mfma_f32_32x32x16_bf16 v[48:63], v[144:147], v[128:131], v[48:63]\n"
    "s_waitcnt lgkmcnt(12)\n"
    "v_mfma_f32_16x16x32_bf16 v[88:91], v[184:187], v[108:111], v[88:91]\n"
    "v_mfma_f32_32x32x16_bf16 v[16:31], v[148:151], v[132:135], v[16:31]\n"
    "v_mfma_f32_16x16x32_bf16 v[92:95], v[180:183], v[108:111], 0\n"
    "ds_read_b64 v[252:253], v7 offset:49392\n"
    "ds_read_b64 v[254:255], v7 offset:49584\n"
    "v_mfma_f32_32x32x16_bf16 v[32:47], v[152:155], v[132:135], v[32:47]\n"
    "s_waitcnt lgkmcnt(10)\n"
    "v_mfma_f32_16x16x32_bf16 v[88:91], v[188:191], v[112:115], v[88:91]\n"
    "v_mfma_f32_32x32x16_bf16 v[48:63], v[136:139], v[132:135], v[48:63]\n"
    "v_mfma_f32_16x16x32_bf16 v[92:95], v[184:187], v[112:115], v[92:95]\n"
    "v_mfma_f32_16x16x32_bf16 v[96:99], v[180:183], v[112:115], 0\n"
    "ds_read_b64 v[136:137], v7 offset:8\n"
    "ds_read_b64 v[138:139], v7 offset:200\n"
    "s_waitcnt lgkmcnt(10)\n"
    "v_mfma_f32_16x16x32_bf16 v[88:91], v[192:195], v[116:119], v[88:91]\n"
    "v_mfma_f32_16x16x32_bf16 v[92:95], v[188:191], v[116:119], v[92:95]\n"
    "v_cmp_u_f32 vcc, v16, v16\n"
    "v_cvt_pk_bf16_f32 v156, v16, v17\n"
    "v_cvt_pk_bf16_f32 v157, v18, v19\n"
    "s_nop 1\n"
    "s_or_b64 s[70:71], s[70:71], vcc\n"
    "v_cvt_pk_bf16_f32 v158, v20, v21\n"
    "v_cvt_pk_bf16_f32 v159, v22, v23\n"
    "v_cvt_pk_bf16_f32 v160, v24, v25\n"
    "v_cvt_pk_bf16_f32 v161, v26, v27\n"
    "v_cvt_pk_bf16_f32 v162, v28, v29\n"
    "v_cvt_pk_bf16_f32 v163, v30, v31\n"
    "v_permlane16_swap_b32 v156, v160\n"
    "v_permlane16_swap_b32 v157, v161\n"
    "v_permlane16_swap_b32 v158, v162\n"
    "v_permlane16_swap_b32 v159, v163\n"
    "v_mfma_f32_16x16x32_bf16 v[96:99], v[184:187], v[116:119], v[96:99]\n"
    "v_cvt_pk_bf16_f32 v164, v32, v33\n"
    "v_cvt_pk_bf16_f32 v165, v34, v35\n"
    "v_cvt_pk_bf16_f32 v166, v36, v37\n"
    "v_cvt_pk_bf16_f32 v167, v38, v39\n"
    "v_cvt_pk_bf16_f32 v168, v40, v41\n"
    "v_cvt_pk_bf16_f32 v169, v42, v43\n"
    "v_cvt_pk_bf16_f32 v170, v44, v45\n"
    "v_cvt_pk_bf16_f32 v171, v46, v47\n"
    "v_permlane16_swap_b32 v164, v168\n"
    "v_permlane16_swap_b32 v165, v169\n"
    "v_permlane16_swap_b32 v166, v170\n"
    "v_permlane16_swap_b32 v167, v171\n"
    "v_lshlrev_b32 v16, 16, v64\n"
    "v_and_b32 v17, 0xffff0000, v64\n"
    "v_lshlrev_b32 v18, 16, v65\n"
    "v_and_b32 v19, 0xffff0000, v65\n"
    "v_lshlrev_b32 v20, 16, v66\n"
    "v_and_b32 v21, 0xffff0000, v66\n"
    "v_lshlrev_b32 v22, 16, v67\n"
    "v_and_b32 v23, 0xffff0000, v67\n"
    "v_lshlrev_b32 v24, 16, v68\n"
    "v_and_b32 v25, 0xffff0000, v68\n"
    "v_lshlrev_b32 v26, 16, v69\n"
    "v_and_b32 v27, 0xffff0000, v69\n"
    "v_lshlrev_b32 v28, 16, v70\n"
    "v_and_b32 v29, 0xffff0000, v70\n"
    "v_lshlrev_b32 v30, 16, v71\n"
    "v_and_b32 v31, 0xffff0000, v71\n"
    "v_mfma_f32_16x16x32_bf16 v[100:103], v[180:183], v[116:119], 0\n"
    "ds_read_b64 v[140:141], v7 offset:7064\n"
    "ds_read_b64 v[142:143], v7 offset:7256\n"
    "v_cvt_pk_bf16_f32 v172, v48, v49\n"
    "v_cvt_pk_bf16_f32 v173, v50, v51\n"
    "v_cvt_pk_bf16_f32 v174, v52, v53\n"
    "v_cvt_pk_bf16_f32 v175, v54, v55\n"
    "v_cvt_pk_bf16_f32 v176, v56, v57\n"
    "v_cvt_pk_bf16_f32 v177, v58, v59\n"
    "v_cvt_pk_bf16_f32 v178, v60, v61\n"
    "v_cvt_pk_bf16_f32 v179, v62, v63\n"
    "v_permlane16_swap_b32 v172, v176\n"
    "v_permlane16_swap_b32 v173, v177\n"
    "v_permlane16_swap_b32 v174, v178\n"
    "v_permlane16_swap_b32 v175, v179\n"
    "v_lshlrev_b32 v32, 16, v72\n"
    "v_and_b32 v33, 0xffff0000, v72\n"
    "v_lshlrev_b32 v34, 16, v73\n"
    "v_and_b32 v35, 0xffff0000, v73\n"
    "v_lshlrev_b32 v36, 16, v74\n"
    "v_and_b32 v37, 0xffff0000, v74\n"
    "v_lshlrev_b32 v38, 16, v75\n"
    "v_and_b32 v39, 0xffff0000, v75\n"
    "v_lshlrev_b32 v40, 16, v76\n"
    "v_and_b32 v41, 0xffff0000, v76\n"
    "v_lshlrev_b32 v42, 16, v77\n"
    "v_and_b32 v43, 0xffff0000, v77\n"
    "v_lshlrev_b32 v44, 16, v78\n"
    "v_and_b32 v45, 0xffff0000, v78\n"
    "v_lshlrev_b32 v46, 16, v79\n"
    "v_and_b32 v47, 0xffff0000, v79\n"
    "s_waitcnt lgkmcnt(10)\n"
    "v_mfma_f32_16x16x32_bf16 v[88:91], v[196:199], v[120:123], v[88:91]\n"
    "v_lshlrev_b32 v48, 16, v80\n"
    "v_and_b32 v49, 0xffff0000, v80\n"
    "v_lshlrev_b32 v50, 16, v81\n"
    "v_and_b32 v51, 0xffff0000, v81\n"
    "v_lshlrev_b32 v52, 16, v82\n"
    "v_and_b32 v53, 0xffff0000, v82\n"
    "v_lshlrev_b32 v54, 16, v83\n"
    "v_and_b32 v55, 0xffff0000, v83\n"
    "v_lshlrev_b32 v56, 16, v84\n"
    "v_and_b32 v57, 0xffff0000, v84\n"
    "v_lshlrev_b32 v58, 16, v85\n"
    "v_and_b32 v59, 0xffff0000, v85\n"
    "v_lshlrev_b32 v60, 16, v86\n"
    "v_and_b32 v61, 0xffff0000, v86\n"
    "v_lshlrev_b32 v62, 16, v87\n"
    "v_and_b32 v63, 0xffff0000, v87\n"
    "v_mfma_f32_16x16x32_bf16 v[92:95], v[192:195], v[120:123], v[92:95]\n"
    "v_mfma_f32_16x16x32_bf16 v[96:99], v[188:191], v[120:123], v[96:99]\n"
    "buffer_store_dwordx4 v[156:159], v12, s[32:35], s60 offen offset:0 nt\n"
    "v_mfma_f32_16x16x32_bf16 v[100:103], v[184:187], v[120:123], v[100:103]\n"
    "ds_read_b64 v[144:145], v7 offset:14120\n"
    "ds_read_b64 v[146:147], v7 offset:14312\n"
    "s_waitcnt lgkmcnt(10)\n"
    "v_mfma_f32_16x16x32_bf16 v[92:95], v[196:199], v[124:127], v[92:95]\n"
    "s_setprio 2\n"
    "v_mfma_f32_16x16x32_bf16 v[96:99], v[192:195], v[124:127], v[96:99]\n"
    "v_mfma_f32_16x16x32_bf16 v[100:103], v[188:191], v[124:127], v[100:103]\n"
    "v_cvt_pk_bf16_f32 v64, v88, v89\n"
    "v_cvt_pk_bf16_f32 v65, v90, v91\n"
    "ds_read_b64 v[148:149], v7 offset:21176\n"
    "ds_read_b64 v[150:151], v7 offset:21368\n"
    "s_waitcnt lgkmcnt(10)\n"
    "v_mfma_f32_16x16x32_bf16 v[96:99], v[196:199], v[248:251], v[96:99]\n"
    "v_mfma_f32_16x16x32_bf16 v[100:103], v[192:195], v[248:251], v[100:103]\n"
    "ds_read_b64 v[152:153], v7 offset:28232\n"
    "ds_read_b64 v[154:155], v7 offset:28424\n"
    "buffer_store_dwordx4 v[160:163], v10, s[32:35], s60 offen offset:0 nt\n"
    "s_waitcnt lgkmcnt(10)\n"
    "v_mfma_f32_16x16x32_bf16 v[100:103], v[196:199], v[252:255], v[100:103]\n"
    "v_cvt_pk_bf16_f32 v66, v92, v93\n"
    "v_cvt_pk_bf16_f32 v67, v94, v95\n"
    "ds_read_b64 v[128:129], v7 offset:35288\n"
    "ds_read_b64 v[130:131], v7 offset:35480\n"
    "s_waitcnt lgkmcnt(10)\n"
    "v_mfma_f32_16x16x32_bf16 v[104:107], v[200:203], v[136:139], 0\n"
    "ds_read_b64 v[132:133], v7 offset:42344\n"
    "ds_read_b64 v[134:135], v7 offset:42536\n"
    "s_waitcnt lgkmcnt(10)\n"
    "v_mfma_f32_16x16x32_bf16 v[104:107], v[204:207], v[140:143], v[104:107]\n"
    "v_cvt_pk_bf16_f32 v68, v96, v97\n"
    "v_cvt_pk_bf16_f32 v69, v98, v99\n"
    "v_mfma_f32_16x16x32_bf16 v[108:111], v[200:203], v[140:143], 0\n"
    "v_cvt_pk_bf16_f32 v70, v100, v101\n"
    "v_cvt_pk_bf16_f32 v71, v102, v103\n"
    "ds_read_b64 v[136:137], v7 offset:49400\n"
    "ds_read_b64 v[138:139], v7 offset:49592\n"
    "s_waitcnt lgkmcnt(10)\n"
    "v_mfma_f32_16x16x32_bf16 v[104:107], v[208:211], v[144:147], v[104:107]\n"
    "v_mfma_f32_16x16x32_bf16 v[108:111], v[204:207], v[144:147], v[108:111]\n"
    "buffer_store_dwordx4 v[164:167], v12, s[32:35], s60 offen offset:64 nt\n"
    "ds_write_b64 v9, v[64:65] offset:0\n"
    "v_mfma_f32_16x16x32_bf16 v[112:115], v[200:203], v[144:147], 0\n"
    "ds_read_b64 v[140:141], v7 offset:16\n"
    "ds_read_b64 v[142:143], v7 offset:208\n"
    "s_waitcnt lgkmcnt(11)\n"
    "v_mfma_f32_16x16x32_bf16 v[104:107], v[212:215], v[148:151], v[104:107]\n"
    "ds_write_b64 v9, v[66:67] offset:7056\n"
    "v_mfma_f32_16x16x32_bf16 v[108:111], v[208:211], v[148:151], v[108:111]\n"
    "v_mfma_f32_16x16x32_bf16 v[112:115], v[204:207], v[148:151], v[112:115]\n"
    "ds_write_b64 v9, v[68:69] offset:14112\n"
    "v_mfma_f32_16x16x32_bf16 v[116:119], v[200:203], v[148:151], 0\n"
    "ds_read_b64 v[144:145], v7 offset:7072\n"
    "ds_read_b64 v[146:147], v7 offset:7264\n"
    "s_setprio 1\n"
    "s_waitcnt lgkmcnt(13)\n"
    "v_mfma_f32_16x16x32_bf16 v[104:107], v[216:219], v[152:155], v[104:107]\n"
    "buffer_store_dwordx4 v[168:171], v10, s[32:35], s60 offen offset:64 nt\n"
    "ds_write_b64 v9, v[70:71] offset:21168\n"
    "v_mfma_f32_16x16x32_bf16 v[108:111], v[212:215], v[152:155], v[108:111]\n"
    "v_mfma_f32_16x16x32_bf16 v[112:115], v[208:211], v[152:155], v[112:115]\n"
    "v_mfma_f32_16x16x32_bf16 v[116:119], v[204:207], v[152:155], v[116:119]\n"
    "ds_read_b64 v[148:149], v7 offset:14128\n"
    "s_waitcnt lgkmcnt(14)\n"
    "ds_read_b64 v[150:151], v7 offset:14320\n"
    "s_waitcnt lgkmcnt(14)\n"
    "v_mfma_f32_16x16x32_bf16 v[108:111], v[216:219], v[128:131], v[108:111]\n"
    "v_mfma_f32_16x16x32_bf16 v[112:115], v[212:215], v[128:131], v[112:115]\n"
    "v_mfma_f32_16x16x32_bf16 v[116:119], v[208:211], v[128:131], v[116:119]\n"
    "v_cvt_pk_bf16_f32 v72, v104, v105\n"
    "v_cvt_pk_bf16_f32 v73, v106, v107\n"
    "ds_read_b64 v[152:153], v7 offset:21184\n"
    "s_waitcnt lgkmcnt(14)\n"
    "ds_read_b64 v[154:155], v7 offset:21376\n"
    "buffer_store_dwordx4 v[172:175], v12, s[32:35], s60 offen offset:128 nt\n"
    "s_waitcnt lgkmcnt(14)\n"
    "v_mfma_f32_16x16x32_bf16 v[112:115], v[216:219], v[132:135], v[112:115]\n"
    "v_mfma_f32_16x16x32_bf16 v[116:119], v[212:215], v[132:135], v[116:119]\n"
    "ds_read_b64 v[128:129], v7 offset:28240\n"
    "s_waitcnt lgkmcnt(14)\n"
    "ds_read_b64 v[130:131], v7 offset:28432\n"
    "s_waitcnt lgkmcnt(14)\n"
    "v_mfma_f32_16x16x32_bf16 v[116:119], v[216:219], v[136:139], v[116:119]\n"
    "v_cvt_pk_bf16_f32 v74, v108, v109\n"
    "v_cvt_pk_bf16_f32 v75, v110, v111\n"
    "ds_read_b64 v[132:133], v7 offset:35296\n"
    "s_waitcnt lgkmcnt(14)\n"
    "ds_read_b64 v[134:135], v7 offset:35488\n"
    "s_waitcnt lgkmcnt(13)\n"
    "v_mfma_f32_16x16x32_bf16 v[88:91], v[220:223], v[140:143], 0\n"
    "ds_read_b64 v[136:137], v7 offset:42352\n"
    "ds_read_b64 v[138:139], v7 offset:42544\n"
    "s_waitcnt lgkmcnt(11)\n"
    "v_mfma_f32_16x16x32_bf16 v[88:91], v[224:227], v[144:147], v[88:91]\n"
    "v_cvt_pk_bf16_f32 v76, v112, v113\n"
    "v_cvt_pk_bf16_f32 v77, v114, v115\n"
    "v_mfma_f32_16x16x32_bf16 v[92:95], v[220:223], v[144:147], 0\n"
    "v_cvt_pk_bf16_f32 v78, v116, v117\n"
    "v_cvt_pk_bf16_f32 v79, v118, v119\n"
    "ds_read_b64 v[140:141], v7 offset:49408\n"
    "ds_read_b64 v[142:143], v7 offset:49600\n"
    "buffer_store_dwordx4 v[176:179], v10, s[32:35], s60 offen offset:128 nt\n"
    "s_waitcnt lgkmcnt(10)\n"
    "v_mfma_f32_16x16x32_bf16 v[88:91], v[228:231], v[148:151], v[88:91]\n"
    "v_mfma_f32_16x16x32_bf16 v[92:95], v[224:227], v[148:151], v[92:95]\n"
    "ds_write_b64 v9, v[72:73] offset:8\n"
    "s_setprio 0\n"
    "v_mfma_f32_16x16x32_bf16 v[96:99], v[220:223], v[148:151], 0\n"
    "s_waitcnt lgkmcnt(9)\n"
    "v_mfma_f32_16x16x32_bf16 v[88:91], v[232:235], v[152:155], v[88:91]\n"
    "ds_write_b64 v9, v[74:75] offset:7064\n"
    "v_mfma_f32_16x16x32_bf16 v[92:95], v[228:231], v[152:155], v[92:95]\n"
    "v_mfma_f32_16x16x32_bf16 v[96:99], v[224:227], v[152:155], v[96:99]\n"
    "ds_write_b64 v9, v[76:77] offset:14120\n"
    "v_mfma_f32_16x16x32_bf16 v[100:103], v[220:223], v[152:155], 0\n"
    "s_waitcnt lgkmcnt(9)\n"
    "v_mfma_f32_16x16x32_bf16 v[88:91], v[236:239], v[128:131], v[88:91]\n"
    "ds_write_b64 v9, v[78:79] offset:21176\n"
    "v_mfma_f32_16x16x32_bf16 v[92:95], v[232:235], v[128:131], v[92:95]\n"
    "v_mfma_f32_16x16x32_bf16 v[96:99], v[228:231], v[128:131], v[96:99]\n"
    "v_mfma_f32_16x16x32_bf16 v[100:103], v[224:227], v[128:131], v[100:103]\n"
    "s_waitcnt lgkmcnt(8)\n"
    "v_mfma_f32_16x16x32_bf16 v[92:95], v[236:239], v[132:135], v[92:95]\n"
    "v_mfma_f32_16x16x32_bf16 v[96:99], v[232:235], v[132:135], v[96:99]\n"
    "v_mfma_f32_16x16x32_bf16 v[100:103], v[228:231], v[132:135], v[100:103]\n"
    "v_cvt_pk_bf16_f32 v80, v88, v89\n"
    "v_cvt_pk_bf16_f32 v81, v90, v91\n"
    "s_waitcnt lgkmcnt(6)\n"
    "v_mfma_f32_16x16x32_bf16 v[96:99], v[236:239], v[136:139], v[96:99]\n"
    "v_mfma_f32_16x16x32_bf16 v[100:103], v[232:235], v[136:139], v[100:103]\n"
    "s_waitcnt lgkmcnt(4)\n"
    "v_mfma_f32_16x16x32_bf16 v[100:103], v[236:239], v[140:143], v[100:103]\n"
    "v_cvt_pk_bf16_f32 v82, v92, v93\n"
    "v_cvt_pk_bf16_f32 v83, v94, v95\n"
    "s_nop 2\n"
    "v_cvt_pk_bf16_f32 v84, v96, v97\n"
    "v_cvt_pk_bf16_f32 v85, v98, v99\n"
    "s_nop 0\n"
    "v_cvt_pk_bf16_f32 v86, v100, v101\n"
    "v_cvt_pk_bf16_f32 v87, v102, v103\n"
    "ds_write_b64 v9, v[80:81] offset:16\n"
    "ds_write_b64 v9, v[82:83] offset:7072\n"
    "ds_write_b64 v9, v[84:85] offset:14128\n"
    "ds_write_b64 v9, v[86:87] offset:21184\n"
    "s_branch L_loopX_%=\n"
    "s_waitcnt lgkmcnt(0)\n"
    "L_tailX_%=:\n"
    "ds_read_b64_tr_b16 v[136:137], v2 offset:12288\n"
    "ds_read_b64_tr_b16 v[138:139], v2 offset:13824\n"
    "ds_read_b64_tr_b16 v[140:141], v2 offset:12352\n"
    "ds_read_b64_tr_b16 v[142:143], v2 offset:13888\n"
    "ds_read_b64_tr_b16 v[144:145], v2 offset:12416\n"
    "ds_read_b64_tr_b16 v[146:147], v2 offset:13952\n"
    "ds_read_b64_tr_b16 v[148:149], v2 offset:15360\n"
    "ds_read_b64_tr_b16 v[150:151], v2 offset:16896\n"
    "ds_read_b64_tr_b16 v[152:153], v2 offset:15424\n"
    "ds_read_b64_tr_b16 v[154:155], v2 offset:16960\n"
    "s_waitcnt lgkmcnt(8)\n"
    "v_mfma_f32_32x32x16_bf16 v[16:31], v[136:139], v[128:131], v[16:31]\n"
    "ds_read_b64_tr_b16 v[136:137], v2 offset:15488\n"
    "ds_read_b64_tr_b16 v[138:139], v2 offset:17024\n"
    "s_waitcnt lgkmcnt(8)\n"
    "v_mfma_f32_32x32x16_bf16 v[32:47], v[140:143], v[128:131], v[32:47]\n"
    "s_waitcnt lgkmcnt(6)\n"
    "v_mfma_f32_32x32x16_bf16 v[48:63], v[144:147], v[128:131], v[48:63]\n"
    "s_waitcnt lgkmcnt(4)\n"
    "v_mfma_f32_32x32x16_bf16 v[16:31], v[148:151], v[132:135], v[16:31]\n"
    "s_waitcnt lgkmcnt(2)\n"
    "v_mfma_f32_32x32x16_bf16 v[32:47], v[152:155], v[132:135], v[32:47]\n"
    "s_waitcnt lgkmcnt(0)\n"
    "v_mfma_f32_32x32x16_bf16 v[48:63], v[136:139], v[132:135], v[48:63]\n"
    "s_nop 7\n"
    "v_cmp_u_f32 vcc, v16, v16\n"
    "v_cvt_pk_bf16_f32 v156, v16, v17\n"
    "v_cvt_pk_bf16_f32 v157, v18, v19\n"
    "v_cvt_pk_bf16_f32 v158, v20, v21\n"
    "v_cvt_pk_bf16_f32 v159, v22, v23\n"
    "v_cvt_pk_bf16_f32 v160, v24, v25\n"
    "v_cvt_pk_bf16_f32 v161, v26, v27\n"
    "v_cvt_pk_bf16_f32 v162, v28, v29\n"
    "v_cvt_pk_bf16_f32 v163, v30, v31\n"
    "v_permlane16_swap_b32 v156, v160\n"
    "v_permlane16_swap_b32 v157, v161\n"
    "v_permlane16_swap_b32 v158, v162\n"
    "v_permlane16_swap_b32 v159, v163\n"
    "s_or_b64 s[70:71], s[70:71], vcc\n"
    "buffer_store_dwordx4 v[156:159], v12, s[32:35], s60 offen offset:0 nt\n"
    "buffer_store_dwordx4 v[160:163], v10, s[32:35], s60 offen offset:0 nt\n"
    "v_cvt_pk_bf16_f32 v164, v32, v33\n"
    "v_cvt_pk_bf16_f32 v165, v34, v35\n"
    "v_cvt_pk_bf16_f32 v166, v36, v37\n"
    "v_cvt_pk_bf16_f32 v167, v38, v39\n"
    "v_cvt_pk_bf16_f32 v168, v40, v41\n"
    "v_cvt_pk_bf16_f32 v169, v42, v43\n"
    "v_cvt_pk_bf16_f32 v170, v44, v45\n"
    "v_cvt_pk_bf16_f32 v171, v46, v47\n"
    "v_permlane16_swap_b32 v164, v168\n"
    "v_permlane16_swap_b32 v165, v169\n"
    "v_permlane16_swap_b32 v166, v170\n"
    "v_permlane16_swap_b32 v167, v171\n"
    "buffer_store_dwordx4 v[164:167], v12, s[32:35], s60 offen offset:64 nt\n"
    "buffer_store_dwordx4 v[168:171], v10, s[32:35], s60 offen offset:64 nt\n"
    "v_cvt_pk_bf16_f32 v172, v48, v49\n"
    "v_cvt_pk_bf16_f32 v173, v50, v51\n"
    "v_cvt_pk_bf16_f32 v174, v52, v53\n"
    "v_cvt_pk_bf16_f32 v175, v54, v55\n"
    "v_cvt_pk_bf16_f32 v176, v56, v57\n"
    "v_cvt_pk_bf16_f32 v177, v58, v59\n"
    "v_cvt_pk_bf16_f32 v178, v60, v61\n"
    "v_cvt_pk_bf16_f32 v179, v62, v63\n"
    "v_permlane16_swap_b32 v172, v176\n"
    "v_permlane16_swap_b32 v173, v177\n"
    "v_permlane16_swap_b32 v174, v178\n"
    "v_permlane16_swap_b32 v175, v179\n"
    "buffer_store_dwordx4 v[172:175], v12, s[32:35], s60 offen offset:128 nt\n"
    "buffer_store_dwordx4 v[176:179], v10, s[32:35], s60 offen offset:128 nt\n"
    "L_done_%=:\n"
    "s_cmp_lg_u64 s[70:71], 0\n"
    "s_cselect_b32 s62, 1, 0\n"
    "v_mov_b32 v14, s62\n"
    "s_lshl_b32 s45, s43, 2\n"
    "s_add_u32 s45, s45, 156352\n"
    "v_mov_b32 v15, s45\n"
    "ds_write_b32 v15, v14\n"
    "s_waitcnt lgkmcnt(0)\n"
    "s_barrier\n"
    "v_mov_b32 v15, 156352\n"
    "ds_read_b128 v[88:91], v15\n"
    "ds_read_b128 v[92:95], v15 offset:16\n"
    "s_waitcnt lgkmcnt(0)\n"
    "v_or_b32 v88, v88, v89\n"
    "v_or_b32 v88, v88, v90\n"
    "v_or_b32 v88, v88, v91\n"
    "v_or_b32 v88, v88, v92\n"
    "v_or_b32 v88, v88, v93\n"
    "v_or_b32 v88, v88, v94\n"
    "v_or_b32 v88, v88, v95\n"
    "s_nop 0\n"
    "v_readfirstlane_b32 s62, v88\n"
    "s_waitcnt vmcnt(0) lgkmcnt(0)\n"
    "s_nop 3\n"
    "s_mov_b32 %0, s62\n"
    : "=s"(flag) : "s"(x), "s"(dwl), "s"(dws), "s"(w1), "s"(w2), "s"(out), "v"(threadIdx.x), "s"(blockIdx.x), "s"(gelu_tab)
    : "v1","v2","v3","v4","v5","v6","v7","v8","v9","v10","v11","v12","v13","v14","v15","v16","v17","v18","v19","v20","v21","v22","v23","v24","v25","v26","v27","v28","v29","v30","v31","v32","v33","v34","v35","v36","v37","v38","v39","v40","v41","v42","v43","v44","v45","v46","v47","v48","v49","v50","v51","v52","v53","v54","v55","v56","v57","v58","v59","v60","v61","v62","v63","v64","v65","v66","v67","v68","v69","v70","v71","v72","v73","v74","v75","v76","v77","v78","v79","v80","v81","v82","v83","v84","v85","v86","v87","v88","v89","v90","v91","v92","v93","v94","v95","v96","v97","v98","v99","v100","v101","v102","v103","v104","v105","v106","v107","v108","v109","v110","v111","v112","v113","v114","v115","v116","v117","v118","v119","v120","v121","v122","v123","v124","v125","v126","v127","v128","v129","v130","v131","v132","v133","v134","v135","v136","v137","v138","v139","v140","v141","v142","v143","v144","v145","v146","v147","v148","v149","v150","v151","v152","v153","v154","v155","v156","v157","v158","v159","v160","v161","v162","v163","v164","v165","v166","v167","v168","v169","v170","v171","v172","v173","v174","v175","v176","v177","v178","v179","v180","v181","v182","v183","v184","v185","v186","v187","v188","v189","v190","v191","v192","v193","v194","v195","v196","v197","v198","v199","v200","v201","v202","v203","v204","v205","v206","v207","v208","v209","v210","v211","v212","v213","v214","v215","v216","v217","v218","v219","v220","v221","v222","v223","v224","v225","v226","v227","v228","v229","v230","v231","v232","v233","v234","v235","v236","v237","v238","v239","v240","v241","v242","v243","v244","v245","v246","v247","v248","v249","v250","v251","v252","v253","v254","v255","s20","s21","s22","s23","s24","s25","s26","s27","s28","s29","s30","s31","s32","s33","s34","s35","s36","s37","s38","s39","s40","s41","s42","s43","s44","s45","s46","s47","s48","s49","s50","s51","s52","s53","s54","s55","s56","s57","s58","s59","s60","s61","s62","s63","s64","s65","s66","s67","s68","s69","s70","s71","s72","s73","s74","s75","s76","s77","s78","s79","vcc","scc","memory");
  if (flag) slow_path(smem, x, dwl, dws, w1, w2, out);
}

extern "C" void kernel_launch(void* const* d_in, const int* in_sizes, int n_in, void* d_out, int out_size, void* d_ws, size_t ws_size, hipStream_t stream) {
  replk_block_kernel<<<256, 512, 0, stream>>>((const unsigned short*)d_in[0], (const unsigned short*)d_in[1], (const unsigned short*)d_in[2],
                                              (const unsigned short*)d_in[3], (const unsigned short*)d_in[4], (unsigned short*)d_out);
}
```
